```python
import torch

def _softmax(x, axis=-1):
    return torch.softmax(x, dim=axis)
def _gelu(x):
    return torch.nn.functional.gelu(x, approximate="tanh")
def _silu(x):
    return torch.nn.functional.silu(x)
def _sigmoid(x):
    return torch.sigmoid(x)
def _layernorm(x, axis=-1, eps=1e-5):
    m = x.mean(dim=axis, keepdim=True)
    v = x.var(dim=axis, keepdim=True, unbiased=False)
    return (x - m) / torch.sqrt(v + eps)
def _scatter_add(tgt, idx, src, axis=0):
    return tgt.clone().index_add_(axis, idx, src)
def _segment_sum(src, seg, n):
    out = torch.zeros((n,) + src.shape[1:], dtype=src.dtype)
    return out.index_add_(0, seg, src)
def _pad(x, pw):
    flat = tuple(v for pair in reversed(pw) for v in pair)
    return torch.nn.functional.pad(x, flat)

kernel_name = 'standalone__glu_gated_reglu__prompt'

def setup_inputs():

    _w = (lambda a: (lambda t: t.to(torch.bfloat16) if t.is_floating_point() else t)(torch.as_tensor(a)))
    B, S, D, F = 1, 2048, 512, 1792
    return {
        "x": _w((lambda *s: torch.randn(*s, dtype=torch.bfloat16) * 0.05)(B, S, D)),
        "W_gate": _w((lambda *s: torch.randn(*s, dtype=torch.bfloat16) * 0.05)(D, F) * (2.0 / D) ** 0.5),
        "W_up": _w((lambda *s: torch.randn(*s, dtype=torch.bfloat16) * 0.05)(D, F) * (2.0 / D) ** 0.5),
        "W_down": _w((lambda *s: torch.randn(*s, dtype=torch.bfloat16) * 0.05)(F, D) * (1.0 / F) ** 0.5),
    }

def reference(x, W_gate, W_up, W_down):
    gate = torch.einsum("bsd,df->bsf", x, W_gate)
    up = torch.einsum("bsd,df->bsf", x, W_up)
    gate = gate * (lambda x, dt: x.to(dt))(gate > 0, gate.dtype)
    h = gate * up
    return torch.einsum("bsf,fd->bsd", h, W_down)
```

```cpp
#include <hip/hip_runtime.h>
#include <hip/hip_bf16.h>
#include <stdint.h>

typedef __attribute__((ext_vector_type(8))) __bf16 bf16x8;
typedef __attribute__((ext_vector_type(4))) short s16x4;
typedef __attribute__((ext_vector_type(8))) short s16x8;
typedef __attribute__((ext_vector_type(2))) __bf16 bf16x2;
typedef __attribute__((ext_vector_type(2))) float f32x2;
typedef __attribute__((ext_vector_type(4))) float f32x4;
typedef __attribute__((ext_vector_type(16))) float f32x16;
typedef __attribute__((ext_vector_type(4))) unsigned u32x4;
typedef __attribute__((ext_vector_type(2))) unsigned u32x2;
#define LDS_AS __attribute__((address_space(3)))

constexpr int M_ = 2048, D_ = 512, F_ = 1792;

__device__ __forceinline__ unsigned rfl(unsigned v) { return __builtin_amdgcn_readfirstlane(v); }

__device__ __forceinline__ u32x4 make_srd(const void* p, unsigned bytes) {
  uint64_t a = (uint64_t)p;
  u32x4 r;
  r[0] = rfl((unsigned)a);
  r[1] = rfl((unsigned)(a >> 32)) & 0xffffu;
  r[2] = bytes;
  r[3] = 0x00020000u;
  return r;
}

template <int IMM>
__device__ __forceinline__ void dma16(unsigned m0v, unsigned voff, u32x4 srd, unsigned soff) {
  asm volatile("s_mov_b32 m0, %0\n\ts_nop 0\n\tbuffer_load_dwordx4 %1, %2, %3 offen offset:%4 lds"
               ::"s"(m0v), "v"(voff), "s"(srd), "s"(soff), "i"(IMM) : "memory");
}
template <int IMM>
__device__ __forceinline__ void gload16(u32x4& dst, unsigned voff, u32x4 srd, unsigned soff) {
  asm volatile("buffer_load_dwordx4 %0, %1, %2, %3 offen offset:%4"
               : "=v"(dst) : "v"(voff), "s"(srd), "s"(soff), "i"(IMM) : "memory");
}

__device__ __forceinline__ s16x4 tr_read(unsigned addr) {
  return __builtin_amdgcn_ds_read_tr16_b64_v4i16((LDS_AS s16x4*)(uintptr_t)addr);
}
__device__ __forceinline__ bf16x8 tr_frag(unsigned a0, unsigned a1) {
  s16x4 lo = tr_read(a0), hi = tr_read(a1);
  s16x8 v = __builtin_shufflevector(lo, hi, 0, 1, 2, 3, 4, 5, 6, 7);
  return __builtin_bit_cast(bf16x8, v);
}
__device__ __forceinline__ bf16x8 lds_read16(unsigned addr) {
  return *(LDS_AS bf16x8*)(uintptr_t)addr;
}
__device__ __forceinline__ unsigned pack_bf16(float a, float b) {
  f32x2 v = {a, b};
  bf16x2 r = __builtin_convertvector(v, bf16x2);
  return __builtin_bit_cast(unsigned, r);
}
__device__ __forceinline__ float bf_lo(unsigned u) { return __builtin_bit_cast(float, u << 16); }
__device__ __forceinline__ float bf_hi(unsigned u) { return __builtin_bit_cast(float, u & 0xffff0000u); }

#define WAIT_VM(n) asm volatile("s_waitcnt vmcnt(" #n ")" ::: "memory")
template <int N> __device__ __forceinline__ void wait_vm() { asm volatile("s_waitcnt vmcnt(%0)" ::"i"(N) : "memory"); }
template <int N> __device__ __forceinline__ void wait_vm_lgkm0() { asm volatile("s_waitcnt vmcnt(%0) lgkmcnt(0)" ::"i"(N) : "memory"); }

constexpr int P1_XOFF = 0, P1_GOFF = 16384, P1_UOFF = 16384 + 14336 + 128, P1_SLOT = P1_UOFF + 14336;
constexpr int P1_NSLOT = 3;
constexpr int P1_LDS = P1_SLOT * P1_NSLOT;

__global__ __launch_bounds__(256, 1) void k_gateup(const void* __restrict__ x, const void* __restrict__ Wg,
                                                   const void* __restrict__ Wu, void* __restrict__ h) {
  extern __shared__ __attribute__((aligned(16))) char smem[];
  const unsigned lds0 = (unsigned)(uintptr_t)(LDS_AS char*)smem;
  const unsigned bid = blockIdx.x, xcd = bid & 7, r = bid >> 3;
  const unsigned mi = (xcd >> 2) * 8 + (r & 7), fj = (xcd & 3) * 4 + (r >> 3);
  const unsigned tid = threadIdx.x, lane = tid & 63;
  const unsigned w = rfl(tid >> 6);

  const u32x4 srdX = make_srd(x, M_ * D_ * 2);
  const u32x4 srdW = make_srd((w >> 1) ? Wu : Wg, D_ * F_ * 2);

  unsigned vx[2];
#pragma unroll
  for (int i = 0; i < 2; ++i) {
    unsigned row = 8 * i + (lane >> 3);
    unsigned ch = (lane & 7) ^ ((row >> 1) & 7);
    vx[i] = row * 1024 + ch * 16 - 1024 * i;
  }
  const unsigned sx0 = (mi * 128 + 32 * w) * 1024;
  unsigned vw[7];
#pragma unroll
  for (int i = 0; i < 7; ++i) {
    unsigned q = ((w & 1) * 7 + i) * 64 + lane;
    unsigned row = q / 14, c = q % 14;
    vw[i] = row * 3584 + c * 16 - 1024 * (i & 3);
  }
  const unsigned sw0 = fj * 224;
  const unsigned mx0 = lds0 + P1_XOFF + w * 4096;
  const unsigned mw0 = lds0 + ((w >> 1) ? P1_UOFF : P1_GOFF) + (w & 1) * 7168;

  auto issue = [&](int s) {
    const unsigned sl = (s % P1_NSLOT) * P1_SLOT;
    dma16<0>(mx0 + sl, vx[0], srdX, sx0 + s * 128);
    dma16<1024>(mx0 + sl, vx[1], srdX, sx0 + s * 128);
    dma16<2048>(mx0 + sl, vx[0], srdX, sx0 + s * 128 + 16384 - 2048);
    dma16<3072>(mx0 + sl, vx[1], srdX, sx0 + s * 128 + 16384 - 2048);
    const unsigned so = sw0 + s * (64 * 3584);
    dma16<0>(mw0 + sl, vw[0], srdW, so);
    dma16<1024>(mw0 + sl, vw[1], srdW, so);
    dma16<2048>(mw0 + sl, vw[2], srdW, so);
    dma16<3072>(mw0 + sl, vw[3], srdW, so);
    dma16<0>(mw0 + sl + 4096, vw[4], srdW, so);
    dma16<1024>(mw0 + sl + 4096, vw[5], srdW, so);
    dma16<2048>(mw0 + sl + 4096, vw[6], srdW, so);
  };
  issue(0); issue(1); issue(2);

  const unsigned m = lane & 31, hh = lane >> 5;
  const unsigned xrow = 32 * w + m;
  const unsigned xa0 = lds0 + P1_XOFF + xrow * 128 + ((hh ^ ((xrow >> 1) & 7)) << 4);
  const unsigned wa0 = lds0 + (((lane >> 4) & 1) ? P1_UOFF : P1_GOFF) + (8 * hh + ((lane & 15) >> 2)) * 224 + (lane & 3) * 8;

  f32x16 acc[7];
#pragma unroll
  for (int t = 0; t < 7; ++t)
#pragma unroll
    for (int i = 0; i < 16; ++i) acc[t][i] = 0.f;

  bf16x8 xf[2], wf[2][7];
  auto ldfrag = [&](int s, int j, int b) {
    const unsigned sl = (s % P1_NSLOT) * P1_SLOT;
    xf[b] = lds_read16((xa0 ^ (j << 5)) + sl);
#pragma unroll
    for (int t = 0; t < 7; ++t) {
      const unsigned a = wa0 + sl + (16 * j) * 224 + 32 * t;
      wf[b][t] = tr_frag(a, a + 4 * 224);
    }
  };
  auto mma = [&](int b) {
#pragma unroll
    for (int t = 0; t < 7; ++t) acc[t] = __builtin_amdgcn_mfma_f32_32x32x16_bf16(wf[b][t], xf[b], acc[t], 0, 0, 0);
  };
  auto sched = [&]() {
#pragma unroll
    for (int t = 0; t < 7; ++t) {
      __builtin_amdgcn_sched_group_barrier(0x008, 1, 0);
      __builtin_amdgcn_sched_group_barrier(0x100, 2, 0);
    }
    __builtin_amdgcn_sched_group_barrier(0x100, 1, 0);
  };

  wait_vm<22>();
  __builtin_amdgcn_s_barrier();
  ldfrag(0, 0, 0);
#pragma unroll
  for (int s = 0; s < 8; ++s) {
#pragma unroll
    for (int j = 0; j < 3; ++j) {
      ldfrag(s, j + 1, (j + 1) & 1);
      mma(j & 1);
      sched();
      __builtin_amdgcn_sched_barrier(0);
    }
    if (s < 7) {
      if (s < 6) wait_vm_lgkm0<11>(); else wait_vm_lgkm0<0>();
      __builtin_amdgcn_s_barrier();
      if (s + 3 < 8) issue(s + 3);
      ldfrag(s + 1, 0, 0);
    }
    mma(1);
    if (s < 7) sched();
    __builtin_amdgcn_sched_barrier(0);
  }

  u32x4* hp = (u32x4*)h + ((size_t)(mi * 4 + w) * 112 + fj * 7) * 64 + lane;
#pragma unroll
  for (int t = 0; t < 7; ++t) {
    u32x4 o;
#pragma unroll
    for (int i = 0; i < 4; ++i) {
      unsigned g = pack_bf16(acc[t][2 * i], acc[t][2 * i + 1]);
      unsigned u = pack_bf16(acc[t][8 + 2 * i], acc[t][8 + 2 * i + 1]);
      float g0 = bf_lo(g), g1 = bf_hi(g);
      g0 = g0 * (g0 > 0.f ? 1.f : 0.f);
      g1 = g1 * (g1 > 0.f ? 1.f : 0.f);
      o[i] = pack_bf16(g0 * bf_lo(u), g1 * bf_hi(u));
    }
    hp[t * 64] = o;
  }
}

constexpr int P2_DEPTH = 14;
constexpr int P2_RING = 16;
constexpr int P2_LDS = 4 * P2_RING * 2048;

__global__ __launch_bounds__(256, 1) void k_down(const void* __restrict__ h, const void* __restrict__ Wd,
                                                 void* __restrict__ out) {
  extern __shared__ __attribute__((aligned(16))) char smem[];
  const unsigned lds0 = (unsigned)(uintptr_t)(LDS_AS char*)smem;
  const unsigned bid = blockIdx.x, xcd = bid & 7, r = bid >> 3;
  const unsigned m64 = xcd * 4 + (r & 3), n64 = r >> 2;
  const unsigned tid = threadIdx.x, lane = tid & 63;
  const unsigned w = rfl(tid >> 6);

  const u32x4 srdH = make_srd(h, M_ * F_ * 2);
  const u32x4 srdD = make_srd(Wd, F_ * D_ * 2);
  const unsigned vh = lane * 16;
  const unsigned sh0 = ((m64 * 2) * 112 + 28 * w) * 1024;
  unsigned vd;
  {
    unsigned row = lane >> 3, ch = (lane & 7) ^ (((row >> 1) & 1) << 2);
    vd = row * 1024 + ch * 16;
  }
  const unsigned sd0 = (28 * 16 * w) * 1024 + n64 * 128;
  const unsigned ring0 = lds0 + w * (P2_RING * 2048);

  u32x4 hf[28][2];
  auto issue = [&](int s) {
    const unsigned sl = ring0 + (s % P2_RING) * 2048;
    dma16<0>(sl, vd, srdD, sd0 + s * 16384);
    dma16<1024>(sl, vd, srdD, sd0 + s * 16384 + 8192 - 1024);
    gload16<0>(hf[s][0], vh, srdH, sh0 + s * 1024);
    gload16<0>(hf[s][1], vh, srdH, sh0 + s * 1024 + 112 * 1024);
  };
#pragma unroll
  for (int s = 0; s < P2_DEPTH; ++s) issue(s);

  const unsigned hh = lane >> 5, q = (lane & 15) >> 2, p = lane & 3;
  const unsigned krow = 4 * hh + q;
  const unsigned da0 = ring0 + krow * 128 + (((2 * ((lane >> 4) & 1) + (p >> 1)) ^ (((krow >> 1) & 1) << 2)) << 4) + (p & 1) * 8;

  f32x16 acc[2][2];
#pragma unroll
  for (int a = 0; a < 2; ++a)
#pragma unroll
    for (int b = 0; b < 2; ++b)
#pragma unroll
      for (int i = 0; i < 16; ++i) acc[a][b][i] = 0.f;

  bf16x8 df[2][2];
  auto ldfrag = [&](int s, int b) {
    const unsigned o = (s % P2_RING) * 2048;
    df[b][0] = tr_frag(da0 + o, da0 + o + 1024);
    df[b][1] = tr_frag((da0 ^ 64) + o, (da0 ^ 64) + o + 1024);
  };
#define P2_WAIT(s_, n_) asm volatile("s_waitcnt vmcnt(%2)" : "+v"(hf[s_][0]), "+v"(hf[s_][1]) : "i"(n_) : "memory")

  P2_WAIT(0, 4 * (P2_DEPTH - 1));
  ldfrag(0, 0);
#pragma unroll
  for (int s = 0; s < 28; ++s) {
    if (s + 1 < 28) {
      constexpr int dummy = 0; (void)dummy;
      const int last = (s + P2_DEPTH < 28) ? (s + P2_DEPTH) : 28;
      const int n = 4 * (last - (s + 2));
      switch (n) {
#define C(k) case k: P2_WAIT(s + 1, k); break;
        C(0) C(4) C(8) C(12) C(16) C(20) C(24) C(28) C(32) C(36) C(40) C(44) C(48) C(52) C(56)
#undef C
      }
      ldfrag(s + 1, (s + 1) & 1);
    }
#pragma unroll
    for (int a = 0; a < 2; ++a)
#pragma unroll
      for (int b = 0; b < 2; ++b)
        acc[a][b] = __builtin_amdgcn_mfma_f32_32x32x16_bf16(df[s & 1][a], __builtin_bit_cast(bf16x8, hf[s][b]), acc[a][b], 0, 0, 0);
    __builtin_amdgcn_sched_barrier(0);
    if (s + P2_DEPTH < 28) issue(s + P2_DEPTH);
  }

#pragma unroll
  for (int a = 0; a < 2; ++a)
#pragma unroll
    for (int b = 0; b < 2; ++b)
#pragma unroll
      for (int rq = 0; rq < 4; ++rq) {
        f32x4 v = {acc[a][b][4 * rq], acc[a][b][4 * rq + 1], acc[a][b][4 * rq + 2], acc[a][b][4 * rq + 3]};
        *(LDS_AS f32x4*)(uintptr_t)(ring0 + ((a + 2 * b) * 4 + rq) * 1024 + lane * 16) = v;
      }
  __syncthreads();
  f32x4 sum[4];
#pragma unroll
  for (int rq = 0; rq < 4; ++rq) {
    sum[rq] = *(LDS_AS f32x4*)(uintptr_t)(lds0 + (w * 4 + rq) * 1024 + lane * 16);
#pragma unroll
    for (int v = 1; v < 4; ++v)
      sum[rq] += *(LDS_AS f32x4*)(uintptr_t)(lds0 + v * (P2_RING * 2048) + (w * 4 + rq) * 1024 + lane * 16);
  }
  const unsigned a = w & 1, b = w >> 1;
  char* op = (char*)out + (size_t)(m64 * 64 + b * 32 + (lane & 31)) * 1024 + n64 * 128 + a * 64 + hh * 16;
#pragma unroll
  for (int k = 0; k < 2; ++k) {
    unsigned ax = pack_bf16(sum[2 * k][0], sum[2 * k][1]), ay = pack_bf16(sum[2 * k][2], sum[2 * k][3]);
    unsigned bx = pack_bf16(sum[2 * k + 1][0], sum[2 * k + 1][1]), by = pack_bf16(sum[2 * k + 1][2], sum[2 * k + 1][3]);
    auto r0 = __builtin_amdgcn_permlane32_swap(ax, bx, false, false);
    auto r1 = __builtin_amdgcn_permlane32_swap(ay, by, false, false);
    u32x4 o = {r0[0], r1[0], r0[1], r1[1]};
    *(u32x4*)(op + k * 32) = o;
  }
}

extern "C" void kernel_launch(void* const* d_in, const int* in_sizes, int n_in, void* d_out, int out_size,
                              void* d_ws, size_t ws_size, hipStream_t stream) {
  (void)in_sizes; (void)n_in; (void)out_size; (void)ws_size;
  (void)hipFuncSetAttribute((const void*)k_gateup, hipFuncAttributeMaxDynamicSharedMemorySize, P1_LDS);
  (void)hipFuncSetAttribute((const void*)k_down, hipFuncAttributeMaxDynamicSharedMemorySize, P2_LDS);
  k_gateup<<<256, 256, P1_LDS, stream>>>(d_in[0], d_in[1], d_in[2], d_ws);
  k_down<<<256, 256, P2_LDS, stream>>>(d_ws, d_in[3], d_out);
}
```

```cpp
#include <hip/hip_runtime.h>
#include <hip/hip_bf16.h>
#include <stdint.h>

typedef __attribute__((ext_vector_type(8))) __bf16 bf16x8;
typedef __attribute__((ext_vector_type(4))) short s16x4;
typedef __attribute__((ext_vector_type(8))) short s16x8;
typedef __attribute__((ext_vector_type(2))) __bf16 bf16x2;
typedef __attribute__((ext_vector_type(2))) float f32x2;
typedef __attribute__((ext_vector_type(4))) float f32x4;
typedef __attribute__((ext_vector_type(4))) unsigned u32x4;
typedef __attribute__((ext_vector_type(2))) unsigned u32x2;
#define LDS_AS __attribute__((address_space(3)))
#define ST_DECL
#define ST(i)
#define ST_FLUSH(a,b)

constexpr int M_ = 2048, D_ = 512, F_ = 1792;
constexpr unsigned SYNC_OFF = 8u << 20;

__device__ __forceinline__ unsigned rfl(unsigned v) { return __builtin_amdgcn_readfirstlane(v); }

__device__ __forceinline__ u32x4 make_srd(const void* p, unsigned bytes) {
  uint64_t a = (uint64_t)p;
  u32x4 r;
  r[0] = rfl((unsigned)a);
  r[1] = rfl((unsigned)(a >> 32)) & 0xffffu;
  r[2] = bytes;
  r[3] = 0x00020000u;
  return r;
}

template <int IMM>
__device__ __forceinline__ void dma16(unsigned m0v, unsigned voff, u32x4 srd, unsigned soff) {
#define NTS ""
  asm volatile("s_mov_b32 m0, %0\n\ts_nop 0\n\tbuffer_load_dwordx4 %1, %2, %3 offen offset:%4" NTS " lds"
               ::"s"(m0v), "v"(voff), "s"(srd), "s"(soff), "i"(IMM) : "memory");
}
__device__ __forceinline__ void gload16_sc1(u32x4& dst, unsigned voff, u32x4 srd, unsigned soff) {
  asm volatile("buffer_load_dwordx4 %0, %1, %2, %3 offen sc1" : "=v"(dst) : "v"(voff), "s"(srd), "s"(soff) : "memory");
}
__device__ __forceinline__ void gstore16_sc1(u32x4 v, unsigned voff, u32x4 srd, unsigned soff) {
  asm volatile("buffer_store_dwordx4 %0, %1, %2, %3 offen sc1\n\ts_nop 1" ::"v"(v), "v"(voff), "s"(srd), "s"(soff) : "memory");
}
__device__ __forceinline__ void gload8_sc1(u32x2& dst, unsigned voff, u32x4 srd, unsigned soff) {
  asm volatile("buffer_load_dwordx2 %0, %1, %2, %3 offen sc1" : "=v"(dst) : "v"(voff), "s"(srd), "s"(soff) : "memory");
}
__device__ __forceinline__ void gstore8_sc1(u32x2 v, unsigned voff, u32x4 srd, unsigned soff) {
  asm volatile("buffer_store_dwordx2 %0, %1, %2, %3 offen sc1" ::"v"(v), "v"(voff), "s"(srd), "s"(soff) : "memory");
}

__device__ __forceinline__ s16x4 tr_read(unsigned addr) {
  return __builtin_amdgcn_ds_read_tr16_b64_v4i16((LDS_AS s16x4*)(uintptr_t)addr);
}
__device__ __forceinline__ bf16x8 tr_frag(unsigned a0, unsigned a1) {
  s16x4 lo = tr_read(a0), hi = tr_read(a1);
  s16x8 v = __builtin_shufflevector(lo, hi, 0, 1, 2, 3, 4, 5, 6, 7);
  return __builtin_bit_cast(bf16x8, v);
}
__device__ __forceinline__ bf16x8 lds_read16(unsigned addr) { return *(LDS_AS bf16x8*)(uintptr_t)addr; }
__device__ __forceinline__ unsigned pack_bf16(float a, float b) {
  f32x2 v = {a, b};
  bf16x2 r = __builtin_convertvector(v, bf16x2);
  return __builtin_bit_cast(unsigned, r);
}
__device__ __forceinline__ float rnd_bf16(float a) {
  unsigned r;
  asm("v_cvt_pk_bf16_f32 %0, 0, %1" : "=v"(r) : "v"(a));
  return __builtin_bit_cast(float, r);
}

template <int N> __device__ __forceinline__ void wait_vm() { asm volatile("s_waitcnt vmcnt(%0)" ::"i"(N) : "memory"); }
template <int N> __device__ __forceinline__ void wait_vm_lgkm0() { asm volatile("s_waitcnt vmcnt(%0) lgkmcnt(0)" ::"i"(N) : "memory"); }
__device__ __forceinline__ void wait_lgkm0() { asm volatile("s_waitcnt lgkmcnt(0)" ::: "memory"); }
#define BAR() __builtin_amdgcn_s_barrier()
#define SB() __builtin_amdgcn_sched_barrier(0)
#define MFMA16(a, b, c) __builtin_amdgcn_mfma_f32_16x16x32_bf16(a, b, c, 0, 0, 0)

constexpr int SLOT = 49152, XOFF = 0, GOFF = 16384, LDS_TOTAL = 163840;
__host__ __device__ constexpr int wd_off(int s) {
  return s >= 10 ? wd_off(s - 10) : (s == 0 ? 3 * SLOT : (s < 4 ? 2 * SLOT + (s - 1) * 4096 : (s < 7 ? (s - 4) * 4096 : SLOT + (s - 7) * 4096)));
}
__host__ __device__ constexpr bool wd_spare(int s) { return (s % 10) == 0; }

#define P2_WIN 4

__global__ __launch_bounds__(512, 2) void k_fused(const void* __restrict__ x, const void* __restrict__ Wg,
                                                  const void* __restrict__ Wu, const void* __restrict__ Wd,
                                                  void* __restrict__ out, void* __restrict__ ws) {
  extern __shared__ __attribute__((aligned(16))) char smem[];
  const unsigned lds0 = (unsigned)(uintptr_t)(LDS_AS char*)smem;
  ST_DECL ST(0)
  const unsigned bid = blockIdx.x, xcd = bid & 7, r = bid >> 3;
  const unsigned half = xcd & 1, ml = r & 3, mi = (xcd >> 1) * 4 + ml, idx = r >> 2;
  const unsigned fj = half * 7 + idx, rank = half * 8 + idx;
  const unsigned m64 = 2 * mi + half, n64 = idx;
  const unsigned tid = threadIdx.x, lane = tid & 63;
  const unsigned w = rfl(tid >> 6);
  const unsigned g = lane >> 4, q = (lane & 15) >> 2, p = lane & 3;
#define ROTMODE 2
  const unsigned rotf = (2 * ml + idx) & 7;
  const unsigned rot = ROTMODE == 2 ? rotf : (ROTMODE == 1 ? ((ml + idx) & 1) * 4 : 0);

  const u32x4 srdH = make_srd(ws, SYNC_OFF + 65536);
  const u32x4 srdD = make_srd(Wd, F_ * D_ * 2);
  const u32x4 srdX = make_srd(x, M_ * D_ * 2);
  const u32x4 srdW = make_srd((w >> 2) ? Wu : Wg, D_ * F_ * 2);
  const unsigned syncb = SYNC_OFF + mi * 4096;

  const unsigned kq = w >> 1, mb = w & 1;
  const unsigned vd = (lane >> 3) * 1024 + (((lane & 7) ^ (g << 1)) << 4);
  const unsigned sd0 = (448 * kq + 16 * mb) * 1024 + n64 * 128;
  const unsigned md1 = lds0 + kq * 4096 + mb * 2048, md3 = lds0 + kq * 12288 + mb * 2048;
  const unsigned rot2 = (rotf * 7) >> 2;
  auto kap = [&](int s) { unsigned k = rot2 + s; return k >= 14 ? k - 14 : k; };
  auto issue_wd = [&](int s, int i) {
    const unsigned m0v = (wd_spare(s) ? md1 : md3) + wd_off(s);
    if (i == 0) dma16<0>(m0v, vd, srdD, sd0 + kap(s) * 32768);
    else dma16<1024>(m0v, vd, srdD, sd0 + kap(s) * 32768 + 8192 - 1024);
  };
  u32x2 ep = {0u, 0u};

  if (idx < 7) {
    const unsigned vx0 = (lane >> 3) * 1024 + (((lane & 7) ^ g) << 4);
    const unsigned vx1 = (vx0 ^ 64) + 8192 - 1024;
    const unsigned sx0 = (mi * 128 + 16 * w) * 1024;
    const unsigned vw0 = g * 3584 + (((lane & 15) ^ (g << 1)) << 4);
    const unsigned vw1 = g * 3584 + (((lane & 15) ^ (g << 1) ^ 8) << 4);
    const unsigned sw0 = (16 * (w & 3)) * 3584 + fj * 256;
    const unsigned mx0 = lds0 + XOFF + w * 2048;
    const unsigned mw0 = lds0 + GOFF + w * 4096;
    auto issue1 = [&](int s, int i) {
      const unsigned sl = (s % 3) * SLOT;
      if (s < 8) {
        const unsigned sg = (s + rot) & 7;
        if (i == 0) dma16<0>(mw0 + sl, vw0, srdW, sw0 + sg * (64 * 3584));
        if (i == 1) dma16<1024>(mw0 + sl, vw0, srdW, sw0 + sg * (64 * 3584) + 1 * (14336 - 1024));
        if (i == 2) dma16<2048>(mw0 + sl, vw1, srdW, sw0 + sg * (64 * 3584) + 2 * (14336 - 1024));
        if (i == 3) dma16<3072>(mw0 + sl, vw1, srdW, sw0 + sg * (64 * 3584) + 3 * (14336 - 1024));
        if (i == 4) dma16<0>(mx0 + sl, vx0, srdX, sx0 + sg * 128);
        if (i == 5) dma16<1024>(mx0 + sl, vx1, srdX, sx0 + sg * 128);
      } else {
        issue_wd(1 + (s - 8) * 3 + (i >> 1), i & 1);
      }
    };
#pragma unroll
    for (int i = 0; i < 6; ++i) issue1(0, i);
    if (w == 0) gload8_sc1(ep, 0u, srdH, syncb);
    else asm volatile("" : "+v"(ep));
#pragma unroll
    for (int i = 0; i < 6; ++i) issue1(1, i);
    ST(1)

    const unsigned wm = w & 1, wn = w >> 1;
    const unsigned xrow = 64 * wm + (lane & 15);
    const unsigned xa0 = lds0 + XOFF + xrow * 128 + ((g ^ ((xrow >> 1) & 7)) << 4);
    const unsigned wa0 = lds0 + GOFF + (8 * g + q) * 256 + (((2 * wn) ^ (q + 4 * (g & 1))) << 5) + p * 8;

    f32x4 acc[4][4];
#pragma unroll
    for (int b = 0; b < 4; ++b)
#pragma unroll
      for (int a = 0; a < 4; ++a) acc[b][a] = f32x4{0.f, 0.f, 0.f, 0.f};

    bf16x8 xf[4][2], wf[2][4][2];
    auto ldx = [&](int s, int b, int kk) { xf[b][kk] = lds_read16((xa0 ^ (kk << 6)) + (s % 3) * SLOT + b * 2048); };
    auto ldw = [&](int s, int a, int kk) {
      const unsigned ad = (wa0 ^ ((a & 1) << 5)) + (s % 3) * SLOT + (a >> 1) * 16384 + kk * 8192;
      wf[s & 1][a][kk] = tr_frag(ad, ad + 1024);
    };
    const unsigned sh = ((mi * 8 + wm * 4) * 56 + fj * 4 + wn) * 1024;
    auto epi = [&](int b) {
      u32x4 o;
#pragma unroll
      for (int t = 0; t < 2; ++t)
#pragma unroll
        for (int i = 0; i < 2; ++i) {
          float g0 = rnd_bf16(__builtin_elementwise_maximum(acc[b][t][2 * i], 0.f));
          float g1 = rnd_bf16(__builtin_elementwise_maximum(acc[b][t][2 * i + 1], 0.f));
          float u0 = rnd_bf16(acc[b][2 + t][2 * i]), u1 = rnd_bf16(acc[b][2 + t][2 * i + 1]);
          o[2 * t + i] = pack_bf16(g0 * u0, g1 * u1);
        }
      gstore16_sc1(o, lane * 16, srdH, sh + b * (56 * 1024));
    };

    wait_vm<6>();
    ST(2)
    BAR();
#pragma unroll
    for (int a = 0; a < 4; ++a) { ldw(0, a, 0); if (a == 0) { ldx(0, 0, 0); ldx(0, 0, 1); } ldw(0, a, 1); }
    ldx(0, 1, 0); ldx(0, 1, 1);
    SB();
#pragma unroll
    for (int s = 0; s < 8; ++s) {
#pragma unroll
      for (int b = 0; b < 4; ++b) {
        if (b == 2) {
          if (s == 0) wait_vm_lgkm0<4>(); else if (s == 1) wait_vm_lgkm0<8>(); else if (s < 7) wait_vm_lgkm0<6>(); else wait_lgkm0();
          BAR();
          if (s == 0 || s == 3 || s == 7) { ST(3 + s) }
        }
#pragma unroll
        for (int a = 0; a < 4; ++a) {
          acc[b][a] = MFMA16(wf[s & 1][a][0], xf[b][0], acc[b][a]);
          acc[b][a] = MFMA16(wf[s & 1][a][1], xf[b][1], acc[b][a]);
          SB();
          if (b < 2) { if (a < 2) ldx(s, b + 2, a); }
          else if (s < 7) {
            ldw(s + 1, 2 * (b - 2) + (a >> 1), a & 1);
            if (a >= 2) ldx(s + 1, b - 2, a - 2);
          }
          if (s == 0 && b < 2) { if (a & 1) issue1(2, 2 * b + (a >> 1)); }
          else if (s == 0 && b == 2) { if (a < 2) issue1(2, 4 + a); else issue_wd(0, a - 2); }
          else {
            const int ds = (b >= 2) ? s + 3 : s + 2;
            int di = -1;
            if (ds == 3) { if (b == 3 && (a & 1)) di = a >> 1; if (b < 2 && (a & 1)) di = 2 + 2 * b + (a >> 1); }
            else if (ds < 10) { if (b == 2 && (a & 1)) di = a >> 1; if (b == 3 && a == 1) di = 2; if (b == 0 && (a & 1)) di = 3 + (a >> 1); if (b == 1 && a == 1) di = 5; }
            else { if (b >= 2 && a < 3) di = 3 * (b - 2) + a; }
            if (di >= 0) issue1(ds, di);
          }
          if (s == 7 && b > 0 && a == 1) epi(b - 1);
          SB();
        }
      }
    }
    ST(11)
    epi(3);
    ST(12)
  } else {
    gload8_sc1(ep, 0u, srdH, syncb);
#pragma unroll
    for (int s = 0; s < 10; ++s) { issue_wd(s, 0); issue_wd(s, 1); }
  }
  asm volatile("s_waitcnt vmcnt(0)" : "+v"(ep)::"memory");
  ST(13)
  BAR();
  const uint64_t tag64 = (((uint64_t)ep[1] << 32) | ep[0]) * 0x9E3779B97F4A7C15ull + 1ull;
  const u32x2 tag = {(unsigned)tag64, (unsigned)(tag64 >> 32)};
  if (w == 0 && lane == 0) gstore8_sc1(tag, 0u, srdH, syncb + 128 * (1 + rank));

  const unsigned dl = (4 * g + q) * 128 + (((2 * g + (q >> 1)) & 3) << 5) + p * 8;
  const unsigned da1 = lds0 + kq * 4096 + dl, da3 = lds0 + kq * 12288 + dl;
  auto ldd = [&](int s, int a) {
    const unsigned ad = ((wd_spare(s) ? da1 : da3) ^ (a << 5)) + wd_off(s);
    return tr_frag(ad, ad + 2048);
  };
  bf16x8 dr[4][4];
#pragma unroll
  for (int s = 0; s < 4; ++s)
#pragma unroll
    for (int a = 0; a < 4; ++a) dr[s][a] = ldd(s, a);
  wait_lgkm0();
  SB();
  BAR();
#pragma unroll
  for (int s = 10; s < 14; ++s) { issue_wd(s, 0); issue_wd(s, 1); }
  ST(14)
  if (w == 0) {
    const unsigned fo = 128 * (1 + (lane & 15));
    for (unsigned spin = 0; spin < (1u << 20); ++spin) {
      u32x2 f;
      gload8_sc1(f, fo, srdH, syncb);
      asm volatile("s_waitcnt vmcnt(0)" : "+v"(f)::"memory");
      if (__all(f[0] == tag[0] && f[1] == tag[1])) break;
      __builtin_amdgcn_s_sleep(1);
    }
    if (rank == 0 && lane == 0) gstore8_sc1(tag, 0u, srdH, syncb);
  } else {
    wait_vm<0>();
  }
  ST(15)
  BAR();
  u32x4 hf[14][2];
  const unsigned sh2 = ((m64 * 4 + mb * 2) * 56 + 14 * kq) * 1024;
  auto issue_h = [&](int s) {
    gload16_sc1(hf[s][0], lane * 16, srdH, sh2 + kap(s) * 1024);
    gload16_sc1(hf[s][1], lane * 16, srdH, sh2 + kap(s) * 1024 + 56 * 1024);
  };
#pragma unroll
  for (int s = 0; s < P2_WIN; ++s) issue_h(s);
  ST(16)
  f32x4 acc2[2][4];
#pragma unroll
  for (int b = 0; b < 2; ++b)
#pragma unroll
    for (int a = 0; a < 4; ++a) acc2[b][a] = f32x4{0.f, 0.f, 0.f, 0.f};
  bf16x8 df[2][4];
#pragma unroll
  for (int s = 0; s < 14; ++s) {
    if (s + 1 >= 4 && s + 1 < 14) {
#pragma unroll
      for (int a = 0; a < 4; ++a) df[(s + 1) & 1][a] = ldd(s + 1, a);
    }
    const int inflight = ((s + P2_WIN < 14) ? P2_WIN : 14 - s) - 1;
    switch (inflight) {
#define C(k) case k: asm volatile("s_waitcnt vmcnt(%2)" : "+v"(hf[s][0]), "+v"(hf[s][1]) : "i"(2 * k) : "memory"); break;
      C(0) C(1) C(2) C(3) C(4) C(5) C(6) C(7) C(8) C(9) C(10) C(11) C(12) C(13)
#undef C
    }
    if (s == 0) { ST(17) }
    if (s + P2_WIN < 14) issue_h(s + P2_WIN);
#pragma unroll
    for (int a = 0; a < 4; ++a)
#pragma unroll
      for (int b = 0; b < 2; ++b)
        acc2[b][a] = MFMA16(s < 4 ? dr[s][a] : df[s & 1][a], __builtin_bit_cast(bf16x8, hf[s][b]), acc2[b][a]);
    SB();
  }
  ST(18)
  BAR();
  {
    const unsigned pa = lds0 + kq * (64 * 272) + (mb * 32 + (lane & 15)) * 272 + g * 16;
#pragma unroll
    for (int b = 0; b < 2; ++b)
#pragma unroll
      for (int a = 0; a < 4; ++a) *(LDS_AS f32x4*)(uintptr_t)(pa + b * (16 * 272) + a * 64) = acc2[b][a];
  }
  wait_lgkm0();
  BAR();
  ST(19)
  {
    const unsigned row = w * 8 + (lane >> 3);
    const unsigned ra = lds0 + row * 272 + (lane & 7) * 32;
    f32x4 s0 = *(LDS_AS f32x4*)(uintptr_t)(ra), s1 = *(LDS_AS f32x4*)(uintptr_t)(ra + 16);
#pragma unroll
    for (int v = 1; v < 4; ++v) {
      s0 += *(LDS_AS f32x4*)(uintptr_t)(ra + v * (64 * 272));
      s1 += *(LDS_AS f32x4*)(uintptr_t)(ra + v * (64 * 272) + 16);
    }
    u32x4 o = {pack_bf16(s0[0], s0[1]), pack_bf16(s0[2], s0[3]), pack_bf16(s1[0], s1[1]), pack_bf16(s1[2], s1[3])};
    *(u32x4*)((char*)out + (size_t)(m64 * 64 + row) * 1024 + n64 * 128 + (lane & 7) * 16) = o;
  }
  ST(20)
}

extern "C" void kernel_launch(void* const* d_in, const int* in_sizes, int n_in, void* d_out, int out_size,
                              void* d_ws, size_t ws_size, hipStream_t stream) {
  (void)in_sizes; (void)n_in; (void)out_size; (void)ws_size;
  (void)hipFuncSetAttribute((const void*)k_fused, hipFuncAttributeMaxDynamicSharedMemorySize, LDS_TOTAL);
  k_fused<<<256, 512, LDS_TOTAL, stream>>>(d_in[0], d_in[1], d_in[2], d_in[3], d_out, d_ws);
}
```

```cpp
#include <hip/hip_runtime.h>
#include <hip/hip_bf16.h>
#include <stdint.h>

typedef __attribute__((ext_vector_type(8))) __bf16 bf16x8;
typedef __attribute__((ext_vector_type(4))) short s16x4;
typedef __attribute__((ext_vector_type(8))) short s16x8;
typedef __attribute__((ext_vector_type(2))) __bf16 bf16x2;
typedef __attribute__((ext_vector_type(2))) float f32x2;
typedef __attribute__((ext_vector_type(4))) float f32x4;
typedef __attribute__((ext_vector_type(4))) unsigned u32x4;
typedef __attribute__((ext_vector_type(2))) unsigned u32x2;
#define LDS_AS __attribute__((address_space(3)))
#define ST_DECL
#define ST(i)
#define ST_FLUSH(a,b)

constexpr int M_ = 2048, D_ = 512, F_ = 1792;
constexpr unsigned SYNC_OFF = 8u << 20;

__device__ __forceinline__ unsigned rfl(unsigned v) { return __builtin_amdgcn_readfirstlane(v); }

__device__ __forceinline__ u32x4 make_srd(const void* p, unsigned bytes) {
  uint64_t a = (uint64_t)p;
  u32x4 r;
  r[0] = rfl((unsigned)a);
  r[1] = rfl((unsigned)(a >> 32)) & 0xffffu;
  r[2] = bytes;
  r[3] = 0x00020000u;
  return r;
}

template <int IMM>
__device__ __forceinline__ void dma16(unsigned m0v, unsigned voff, u32x4 srd, unsigned soff) {
#define NTS ""
  asm volatile("s_mov_b32 m0, %0\n\ts_nop 0\n\tbuffer_load_dwordx4 %1, %2, %3 offen offset:%4" NTS " lds"
               ::"s"(m0v), "v"(voff), "s"(srd), "s"(soff), "i"(IMM) : "memory");
}
__device__ __forceinline__ void gload16_sc1(u32x4& dst, unsigned voff, u32x4 srd, unsigned soff) {
  asm volatile("buffer_load_dwordx4 %0, %1, %2, %3 offen sc1" : "=v"(dst) : "v"(voff), "s"(srd), "s"(soff) : "memory");
}
__device__ __forceinline__ void gstore16_sc1(u32x4 v, unsigned voff, u32x4 srd, unsigned soff) {
  asm volatile("buffer_store_dwordx4 %0, %1, %2, %3 offen sc1\n\ts_nop 1" ::"v"(v), "v"(voff), "s"(srd), "s"(soff) : "memory");
}
__device__ __forceinline__ void gload8_sc1(u32x2& dst, unsigned voff, u32x4 srd, unsigned soff) {
  asm volatile("buffer_load_dwordx2 %0, %1, %2, %3 offen sc1" : "=v"(dst) : "v"(voff), "s"(srd), "s"(soff) : "memory");
}
__device__ __forceinline__ void gstore8_sc1(u32x2 v, unsigned voff, u32x4 srd, unsigned soff) {
  asm volatile("buffer_store_dwordx2 %0, %1, %2, %3 offen sc1" ::"v"(v), "v"(voff), "s"(srd), "s"(soff) : "memory");
}

__device__ __forceinline__ s16x4 tr_read(unsigned addr) {
  return __builtin_amdgcn_ds_read_tr16_b64_v4i16((LDS_AS s16x4*)(uintptr_t)addr);
}
__device__ __forceinline__ bf16x8 tr_frag(unsigned a0, unsigned a1) {
  s16x4 lo = tr_read(a0), hi = tr_read(a1);
  s16x8 v = __builtin_shufflevector(lo, hi, 0, 1, 2, 3, 4, 5, 6, 7);
  return __builtin_bit_cast(bf16x8, v);
}
__device__ __forceinline__ bf16x8 lds_read16(unsigned addr) { return *(LDS_AS bf16x8*)(uintptr_t)addr; }
__device__ __forceinline__ unsigned pack_bf16(float a, float b) {
  f32x2 v = {a, b};
  bf16x2 r = __builtin_convertvector(v, bf16x2);
  return __builtin_bit_cast(unsigned, r);
}
__device__ __forceinline__ float rnd_bf16(float a) {
  unsigned r;
  asm("v_cvt_pk_bf16_f32 %0, 0, %1" : "=v"(r) : "v"(a));
  return __builtin_bit_cast(float, r);
}

template <int N> __device__ __forceinline__ void wait_vm() { asm volatile("s_waitcnt vmcnt(%0)" ::"i"(N) : "memory"); }
template <int N> __device__ __forceinline__ void wait_vm_lgkm0() { asm volatile("s_waitcnt vmcnt(%0) lgkmcnt(0)" ::"i"(N) : "memory"); }
__device__ __forceinline__ void wait_lgkm0() { asm volatile("s_waitcnt lgkmcnt(0)" ::: "memory"); }
#define BAR() __builtin_amdgcn_s_barrier()
#define SB() __builtin_amdgcn_sched_barrier(0)
#define MFMA16(a, b, c) __builtin_amdgcn_mfma_f32_16x16x32_bf16(a, b, c, 0, 0, 0)

constexpr int SLOT = 49152, XOFF = 0, GOFF = 16384, LDS_TOTAL = 163840;
__host__ __device__ constexpr int wd_off(int s) {
  return s >= 10 ? wd_off(s - 10) : (s == 0 ? 3 * SLOT : (s < 4 ? 2 * SLOT + (s - 1) * 4096 : (s < 7 ? (s - 4) * 4096 : SLOT + (s - 7) * 4096)));
}
__host__ __device__ constexpr bool wd_spare(int s) { return (s % 10) == 0; }

#define P2_WIN 4

__global__ __launch_bounds__(512, 2) void k_fused(const void* __restrict__ x, const void* __restrict__ Wg,
                                                  const void* __restrict__ Wu, const void* __restrict__ Wd,
                                                  void* __restrict__ out, void* __restrict__ ws) {
  extern __shared__ __attribute__((aligned(16))) char smem[];
  const unsigned lds0 = (unsigned)(uintptr_t)(LDS_AS char*)smem;
  ST_DECL ST(0)
  const unsigned bid = blockIdx.x, xcd = bid & 7, r = bid >> 3;
  const unsigned half = xcd & 1, ml = r & 3, mi = (xcd >> 1) * 4 + ml, idx = r >> 2;
  const unsigned fj = half * 7 + idx, rank = half * 8 + idx;
  const unsigned m64 = 2 * mi + half, n64 = idx;
  const unsigned tid = threadIdx.x, lane = tid & 63;
  const unsigned w = rfl(tid >> 6);
  const unsigned g = lane >> 4, q = (lane & 15) >> 2, p = lane & 3;
#define ROTMODE 2
  const unsigned rotf = (2 * ml + idx) & 7;
  const unsigned rot = ROTMODE == 2 ? rotf : (ROTMODE == 1 ? ((ml + idx) & 1) * 4 : 0);

  const u32x4 srdH = make_srd(ws, SYNC_OFF + 65536);
  const u32x4 srdD = make_srd(Wd, F_ * D_ * 2);
  const u32x4 srdX = make_srd(x, M_ * D_ * 2);
  const u32x4 srdW = make_srd((w >> 2) ? Wu : Wg, D_ * F_ * 2);
  const unsigned syncb = SYNC_OFF + mi * 4096;

  const unsigned kq = w >> 1, mb = w & 1;
  const unsigned vd = (lane >> 3) * 1024 + (((lane & 7) ^ (g << 1)) << 4);
  const unsigned sd0 = (448 * kq + 16 * mb) * 1024 + n64 * 128;
  const unsigned md1 = lds0 + kq * 4096 + mb * 2048, md3 = lds0 + kq * 12288 + mb * 2048;
  const unsigned rot2 = (rotf * 7) >> 2;
  auto kap = [&](int s) { unsigned k = rot2 + s; return k >= 14 ? k - 14 : k; };
  auto issue_wd = [&](int s, int i) {
    const unsigned m0v = (wd_spare(s) ? md1 : md3) + wd_off(s);
    if (i == 0) dma16<0>(m0v, vd, srdD, sd0 + kap(s) * 32768);
    else dma16<1024>(m0v, vd, srdD, sd0 + kap(s) * 32768 + 8192 - 1024);
  };
  u32x2 ep = {0u, 0u};

  if (idx < 7) {
    const unsigned vx0 = (lane >> 3) * 1024 + (((lane & 7) ^ g) << 4);
    const unsigned vx1 = (vx0 ^ 64) + 8192 - 1024;
    const unsigned sx0 = (mi * 128 + 16 * w) * 1024;
    const unsigned vw0 = g * 3584 + (((lane & 15) ^ (g << 1)) << 4);
    const unsigned vw1 = g * 3584 + (((lane & 15) ^ (g << 1) ^ 8) << 4);
    const unsigned sw0 = (16 * (w & 3)) * 3584 + fj * 256;
    const unsigned mx0 = lds0 + XOFF + w * 2048;
    const unsigned mw0 = lds0 + GOFF + w * 4096;
    auto issue1 = [&](int s, int i) {
      const unsigned sl = (s % 3) * SLOT;
      if (s < 8) {
        const unsigned sg = (s + rot) & 7;
        if (i == 0) dma16<0>(mw0 + sl, vw0, srdW, sw0 + sg * (64 * 3584));
        if (i == 1) dma16<1024>(mw0 + sl, vw0, srdW, sw0 + sg * (64 * 3584) + 1 * (14336 - 1024));
        if (i == 2) dma16<2048>(mw0 + sl, vw1, srdW, sw0 + sg * (64 * 3584) + 2 * (14336 - 1024));
        if (i == 3) dma16<3072>(mw0 + sl, vw1, srdW, sw0 + sg * (64 * 3584) + 3 * (14336 - 1024));
        if (i == 4) dma16<0>(mx0 + sl, vx0, srdX, sx0 + sg * 128);
        if (i == 5) dma16<1024>(mx0 + sl, vx1, srdX, sx0 + sg * 128);
      } else {
        issue_wd(1 + (s - 8) * 3 + (i >> 1), i & 1);
      }
    };
#pragma unroll
    for (int i = 0; i < 6; ++i) issue1(0, i);
    if (w == 0) gload8_sc1(ep, 0u, srdH, syncb);
    else asm volatile("" : "+v"(ep));
#pragma unroll
    for (int i = 0; i < 6; ++i) issue1(1, i);
    ST(1)

    const unsigned wm = w & 1, wn = w >> 1;
    const unsigned xrow = 64 * wm + (lane & 15);
    const unsigned xa0 = lds0 + XOFF + xrow * 128 + ((g ^ ((xrow >> 1) & 7)) << 4);
    const unsigned wa0 = lds0 + GOFF + (8 * g + q) * 256 + (((2 * wn) ^ (q + 4 * (g & 1))) << 5) + p * 8;

    f32x4 acc[4][4];
#pragma unroll
    for (int b = 0; b < 4; ++b)
#pragma unroll
      for (int a = 0; a < 4; ++a) acc[b][a] = f32x4{0.f, 0.f, 0.f, 0.f};

    bf16x8 xf[4][2], wf[2][4][2];
    auto ldx = [&](int s, int b, int kk) { xf[b][kk] = lds_read16((xa0 ^ (kk << 6)) + (s % 3) * SLOT + b * 2048); };
    auto ldw = [&](int s, int a, int kk) {
      const unsigned ad = (wa0 ^ ((a & 1) << 5)) + (s % 3) * SLOT + (a >> 1) * 16384 + kk * 8192;
      wf[s & 1][a][kk] = tr_frag(ad, ad + 1024);
    };
    const unsigned sh = ((mi * 8 + wm * 4) * 56 + fj * 4 + wn) * 1024;
    auto epi = [&](int b) {
      u32x4 o;
#pragma unroll
      for (int t = 0; t < 2; ++t)
#pragma unroll
        for (int i = 0; i < 2; ++i) {
          float g0 = rnd_bf16(__builtin_elementwise_maximum(acc[b][t][2 * i], 0.f));
          float g1 = rnd_bf16(__builtin_elementwise_maximum(acc[b][t][2 * i + 1], 0.f));
          float u0 = rnd_bf16(acc[b][2 + t][2 * i]), u1 = rnd_bf16(acc[b][2 + t][2 * i + 1]);
          o[2 * t + i] = pack_bf16(g0 * u0, g1 * u1);
        }
      gstore16_sc1(o, lane * 16, srdH, sh + b * (56 * 1024));
    };

    wait_vm<6>();
    ST(2)
    BAR();
#pragma unroll
    for (int a = 0; a < 4; ++a) { ldw(0, a, 0); if (a == 0) { ldx(0, 0, 0); ldx(0, 0, 1); } ldw(0, a, 1); }
    ldx(0, 1, 0); ldx(0, 1, 1);
    SB();
#pragma unroll
    for (int s = 0; s < 8; ++s) {
#pragma unroll
      for (int b = 0; b < 4; ++b) {
        if (b == 2) {
          if (s == 0) wait_vm_lgkm0<4>(); else if (s == 1) wait_vm_lgkm0<8>(); else if (s < 7) wait_vm_lgkm0<6>(); else wait_lgkm0();
          BAR();
          if (s == 0 || s == 3 || s == 7) { ST(3 + s) }
        }
#pragma unroll
        for (int a = 0; a < 4; ++a) {
          acc[b][a] = MFMA16(wf[s & 1][a][0], xf[b][0], acc[b][a]);
          acc[b][a] = MFMA16(wf[s & 1][a][1], xf[b][1], acc[b][a]);
          SB();
          if (b < 2) { if (a < 2) ldx(s, b + 2, a); }
          else if (s < 7) {
            ldw(s + 1, 2 * (b - 2) + (a >> 1), a & 1);
            if (a >= 2) ldx(s + 1, b - 2, a - 2);
          }
          if (s == 0 && b < 2) { if (a & 1) issue1(2, 2 * b + (a >> 1)); }
          else if (s == 0 && b == 2) { if (a < 2) issue1(2, 4 + a); else issue_wd(0, a - 2); }
          else {
            const int ds = (b >= 2) ? s + 3 : s + 2;
            int di = -1;
            if (ds == 3) { if (b == 3 && (a & 1)) di = a >> 1; if (b < 2 && (a & 1)) di = 2 + 2 * b + (a >> 1); }
            else if (ds < 10) { if (b == 2 && (a & 1)) di = a >> 1; if (b == 3 && a == 1) di = 2; if (b == 0 && (a & 1)) di = 3 + (a >> 1); if (b == 1 && a == 1) di = 5; }
            else { if (b >= 2 && a < 3) di = 3 * (b - 2) + a; }
            if (di >= 0) issue1(ds, di);
          }
          if (s == 7 && b > 0 && a == 1) epi(b - 1);
          SB();
        }
      }
    }
    ST(11)
    epi(3);
    ST(12)
  } else {
    gload8_sc1(ep, 0u, srdH, syncb);
#pragma unroll
    for (int s = 0; s < 10; ++s) { issue_wd(s, 0); issue_wd(s, 1); }
  }
  asm volatile("s_waitcnt vmcnt(0)" : "+v"(ep)::"memory");
  ST(13)
  BAR();
  const uint64_t tag64 = (((uint64_t)ep[1] << 32) | ep[0]) * 0x9E3779B97F4A7C15ull + 1ull;
  const u32x2 tag = {(unsigned)tag64, (unsigned)(tag64 >> 32)};
  if (w == 0 && lane == 0) gstore8_sc1(tag, 0u, srdH, syncb + 128 * (1 + rank));

  const unsigned dl = (4 * g + q) * 128 + (((2 * g + (q >> 1)) & 3) << 5) + p * 8;
  const unsigned da1 = lds0 + kq * 4096 + dl, da3 = lds0 + kq * 12288 + dl;
  auto ldd = [&](int s, int a) {
    const unsigned ad = ((wd_spare(s) ? da1 : da3) ^ (a << 5)) + wd_off(s);
    return tr_frag(ad, ad + 2048);
  };
  bf16x8 dr[4][4];
#pragma unroll
  for (int s = 0; s < 4; ++s)
#pragma unroll
    for (int a = 0; a < 4; ++a) dr[s][a] = ldd(s, a);
  wait_lgkm0();
  SB();
  BAR();
#pragma unroll
  for (int s = 10; s < 14; ++s) { issue_wd(s, 0); issue_wd(s, 1); }
  ST(14)
  if (w == 0) {
    const unsigned fo = 128 * (1 + (lane & 15));
    for (unsigned spin = 0; spin < (1u << 20); ++spin) {
      u32x2 f;
      gload8_sc1(f, fo, srdH, syncb);
      asm volatile("s_waitcnt vmcnt(0)" : "+v"(f)::"memory");
      if (__all(f[0] == tag[0] && f[1] == tag[1])) break;
      __builtin_amdgcn_s_sleep(1);
    }
    if (rank == 0 && lane == 0) gstore8_sc1(tag, 0u, srdH, syncb);
  } else {
    wait_vm<0>();
  }
  ST(15)
  BAR();
  u32x4 hf[14][2];
  const unsigned sh2 = ((m64 * 4 + mb * 2) * 56 + 14 * kq) * 1024;
  auto issue_h = [&](int s) {
    gload16_sc1(hf[s][0], lane * 16, srdH, sh2 + kap(s) * 1024);
    gload16_sc1(hf[s][1], lane * 16, srdH, sh2 + kap(s) * 1024 + 56 * 1024);
  };
#pragma unroll
  for (int s = 0; s < P2_WIN; ++s) issue_h(s);
  ST(16)
  f32x4 acc2[2][4];
#pragma unroll
  for (int b = 0; b < 2; ++b)
#pragma unroll
    for (int a = 0; a < 4; ++a) acc2[b][a] = f32x4{0.f, 0.f, 0.f, 0.f};
  bf16x8 df[2][4];
#pragma unroll
  for (int s = 0; s < 14; ++s) {
    if (s + 1 >= 4 && s + 1 < 14) {
#pragma unroll
      for (int a = 0; a < 4; ++a) df[(s + 1) & 1][a] = ldd(s + 1, a);
    }
    const int inflight = ((s + P2_WIN < 14) ? P2_WIN : 14 - s) - 1;
    switch (inflight) {
#define C(k) case k: asm volatile("s_waitcnt vmcnt(%2)" : "+v"(hf[s][0]), "+v"(hf[s][1]) : "i"(2 * k) : "memory"); break;
      C(0) C(1) C(2) C(3) C(4) C(5) C(6) C(7) C(8) C(9) C(10) C(11) C(12) C(13)
#undef C
    }
    if (s == 0) { ST(17) }
    if (s + P2_WIN < 14) issue_h(s + P2_WIN);
#pragma unroll
    for (int a = 0; a < 4; ++a)
#pragma unroll
      for (int b = 0; b < 2; ++b)
        acc2[b][a] = MFMA16(s < 4 ? dr[s][a] : df[s & 1][a], __builtin_bit_cast(bf16x8, hf[s][b]), acc2[b][a]);
    SB();
  }
  ST(18)
  BAR();
  {
    const unsigned pa = lds0 + kq * (64 * 272) + (mb * 32 + (lane & 15)) * 272 + g * 16;
#pragma unroll
    for (int b = 0; b < 2; ++b)
#pragma unroll
      for (int a = 0; a < 4; ++a) *(LDS_AS f32x4*)(uintptr_t)(pa + b * (16 * 272) + a * 64) = acc2[b][a];
  }
  wait_lgkm0();
  BAR();
  ST(19)
  {
    const unsigned row = w * 8 + (lane >> 3);
    const unsigned ra = lds0 + row * 272 + (lane & 7) * 32;
    f32x4 s0 = *(LDS_AS f32x4*)(uintptr_t)(ra), s1 = *(LDS_AS f32x4*)(uintptr_t)(ra + 16);
#pragma unroll
    for (int v = 1; v < 4; ++v) {
      s0 += *(LDS_AS f32x4*)(uintptr_t)(ra + v * (64 * 272));
      s1 += *(LDS_AS f32x4*)(uintptr_t)(ra + v * (64 * 272) + 16);
    }
    u32x4 o = {pack_bf16(s0[0], s0[1]), pack_bf16(s0[2], s0[3]), pack_bf16(s1[0], s1[1]), pack_bf16(s1[2], s1[3])};
#define OUTPOL ""
    const u32x4 srdO = make_srd(out, M_ * D_ * 2);
    asm volatile("buffer_store_dwordx4 %0, %1, %2, %3 offen" OUTPOL ::"v"(o), "v"(row * 1024 + (lane & 7) * 16), "s"(srdO), "s"(m64 * 65536 + n64 * 128) : "memory");
  }
  ST(20)
}

__global__ void k_null(void* p) { if (p == nullptr) __builtin_trap(); }
extern "C" void kernel_launch(void* const* d_in, const int* in_sizes, int n_in, void* d_out, int out_size,
                              void* d_ws, size_t ws_size, hipStream_t stream) {
  (void)in_sizes; (void)n_in; (void)out_size; (void)ws_size;
  (void)hipFuncSetAttribute((const void*)k_fused, hipFuncAttributeMaxDynamicSharedMemorySize, LDS_TOTAL);
  k_null<<<256, 64, 0, stream>>>(d_ws);
  k_fused<<<256, 512, LDS_TOTAL, stream>>>(d_in[0], d_in[1], d_in[2], d_in[3], d_out, d_ws);
}
```

```cpp
#include <hip/hip_runtime.h>
#include <hip/hip_bf16.h>
#include <stdint.h>

typedef __attribute__((ext_vector_type(8))) __bf16 bf16x8;
typedef __attribute__((ext_vector_type(4))) short s16x4;
typedef __attribute__((ext_vector_type(8))) short s16x8;
typedef __attribute__((ext_vector_type(2))) __bf16 bf16x2;
typedef __attribute__((ext_vector_type(2))) float f32x2;
typedef __attribute__((ext_vector_type(4))) float f32x4;
typedef __attribute__((ext_vector_type(4))) unsigned u32x4;
typedef __attribute__((ext_vector_type(2))) unsigned u32x2;
#define LDS_AS __attribute__((address_space(3)))
#define ST_DECL
#define ST(i)
#define ST_FLUSH(a,b)

constexpr int M_ = 2048, D_ = 512, F_ = 1792;
constexpr unsigned SYNC_OFF = 8u << 20;

__device__ __forceinline__ unsigned rfl(unsigned v) { return __builtin_amdgcn_readfirstlane(v); }

__device__ __forceinline__ u32x4 make_srd(const void* p, unsigned bytes) {
  uint64_t a = (uint64_t)p;
  u32x4 r;
  r[0] = rfl((unsigned)a);
  r[1] = rfl((unsigned)(a >> 32)) & 0xffffu;
  r[2] = bytes;
  r[3] = 0x00020000u;
  return r;
}

template <int IMM>
__device__ __forceinline__ void dma16(unsigned m0v, unsigned voff, u32x4 srd, unsigned soff) {
#define NTS ""
  asm volatile("s_mov_b32 m0, %0\n\ts_nop 0\n\tbuffer_load_dwordx4 %1, %2, %3 offen offset:%4" NTS " lds"
               ::"s"(m0v), "v"(voff), "s"(srd), "s"(soff), "i"(IMM) : "memory");
}
__device__ __forceinline__ void set_m0(unsigned v) { asm volatile("s_mov_b32 m0, %0" ::"s"(v) : "memory"); }
template <int IMM>
__device__ __forceinline__ void dma16n(unsigned voff, u32x4 srd, unsigned soff) {
  asm volatile("buffer_load_dwordx4 %0, %1, %2 offen offset:%3 lds" ::"v"(voff), "s"(srd), "s"(soff), "i"(IMM) : "memory");
}
__device__ __forceinline__ unsigned opq(unsigned v) { asm volatile("" : "+v"(v)); return v; }
__device__ __forceinline__ void gload16_sc1(u32x4& dst, unsigned voff, u32x4 srd, unsigned soff) {
#define HPOL " sc1"
  asm volatile("buffer_load_dwordx4 %0, %1, %2, %3 offen" HPOL : "=v"(dst) : "v"(voff), "s"(srd), "s"(soff) : "memory");
}
__device__ __forceinline__ void gstore16_sc1(u32x4 v, unsigned voff, u32x4 srd, unsigned soff) {
  asm volatile("buffer_store_dwordx4 %0, %1, %2, %3 offen sc1\n\ts_nop 1" ::"v"(v), "v"(voff), "s"(srd), "s"(soff) : "memory");
}
__device__ __forceinline__ void gload8_sc1(u32x2& dst, unsigned voff, u32x4 srd, unsigned soff) {
  asm volatile("buffer_load_dwordx2 %0, %1, %2, %3 offen sc1" : "=v"(dst) : "v"(voff), "s"(srd), "s"(soff) : "memory");
}
__device__ __forceinline__ void gstore8_sc1(u32x2 v, unsigned voff, u32x4 srd, unsigned soff) {
  asm volatile("buffer_store_dwordx2 %0, %1, %2, %3 offen sc1" ::"v"(v), "v"(voff), "s"(srd), "s"(soff) : "memory");
}

__device__ __forceinline__ s16x4 tr_read(unsigned addr) {
  return __builtin_amdgcn_ds_read_tr16_b64_v4i16((LDS_AS s16x4*)(uintptr_t)addr);
}
__device__ __forceinline__ bf16x8 tr_frag(unsigned a0, unsigned a1) {
  s16x4 lo = tr_read(a0), hi = tr_read(a1);
  s16x8 v = __builtin_shufflevector(lo, hi, 0, 1, 2, 3, 4, 5, 6, 7);
  return __builtin_bit_cast(bf16x8, v);
}
__device__ __forceinline__ bf16x8 lds_read16(unsigned addr) { return *(LDS_AS bf16x8*)(uintptr_t)addr; }
__device__ __forceinline__ unsigned pack_bf16(float a, float b) {
  f32x2 v = {a, b};
  bf16x2 r = __builtin_convertvector(v, bf16x2);
  return __builtin_bit_cast(unsigned, r);
}
__device__ __forceinline__ float rnd_bf16(float a) {
  unsigned r;
  asm("v_cvt_pk_bf16_f32 %0, 0, %1" : "=v"(r) : "v"(a));
  return __builtin_bit_cast(float, r);
}

template <int I> struct IC { static constexpr int v = I; };
template <int I, int N, class F> __device__ __forceinline__ void sfor(F&& f) {
  if constexpr (I < N) { f(IC<I>{}); sfor<I + 1, N>(f); }
}
template <int N> __device__ __forceinline__ void wait_vm() { asm volatile("s_waitcnt vmcnt(%0)" ::"i"(N) : "memory"); }
template <int N> __device__ __forceinline__ void wait_vm_lgkm0() { asm volatile("s_waitcnt vmcnt(%0) lgkmcnt(0)" ::"i"(N) : "memory"); }
__device__ __forceinline__ void wait_lgkm0() { asm volatile("s_waitcnt lgkmcnt(0)" ::: "memory"); }
#define BAR() __builtin_amdgcn_s_barrier()
#define SB() __builtin_amdgcn_sched_barrier(0)
#define MFMA16(a, b, c) __builtin_amdgcn_mfma_f32_16x16x32_bf16(a, b, c, 0, 0, 0)

constexpr int SLOT = 49152, XOFF = 0, GOFF = 16384, LDS_TOTAL = 163840;
__host__ __device__ constexpr int wd_off(int s) {
  return s >= 10 ? wd_off(s - 10) : (s == 0 ? 3 * SLOT : (s < 4 ? 2 * SLOT + (s - 1) * 4096 : (s < 7 ? (s - 4) * 4096 : SLOT + (s - 7) * 4096)));
}
__host__ __device__ constexpr bool wd_spare(int s) { return (s % 10) == 0; }

#define P2_WIN 3
#define ROTMODE 3

__global__ __launch_bounds__(512, 2) void k_fused(const void* __restrict__ x, const void* __restrict__ Wg,
                                                  const void* __restrict__ Wu, const void* __restrict__ Wd,
                                                  void* __restrict__ out, void* __restrict__ ws) {
  extern __shared__ __attribute__((aligned(16))) char smem[];
  const unsigned lds0 = (unsigned)(uintptr_t)(LDS_AS char*)smem;
  ST_DECL ST(0)
  const unsigned bid = blockIdx.x, xcd = bid & 7, r = bid >> 3;
  const unsigned half = xcd & 1, ml = r & 3, mi = (xcd >> 1) * 4 + ml, idx = r >> 2;
  const unsigned fj = half * 7 + idx, rank = half * 8 + idx;
  const unsigned m64 = 2 * mi + half, n64 = idx;
  const unsigned tid = threadIdx.x, lane = tid & 63;
  const unsigned w = rfl(tid >> 6);
  const unsigned g = lane >> 4, q = (lane & 15) >> 2, p = lane & 3;
  const unsigned rotf = (2 * ml + idx) & 7;
  const unsigned rot = ROTMODE == 3 ? ((xcd & 6) + 4 * half) & 7 : (ROTMODE == 2 ? rotf : 0);

  const u32x4 srdH = make_srd(ws, SYNC_OFF + 65536);
  const u32x4 srdD = make_srd(Wd, F_ * D_ * 2);
  const u32x4 srdX = make_srd(x, M_ * D_ * 2);
  const u32x4 srdW = make_srd((w >> 2) ? Wu : Wg, D_ * F_ * 2);
  const unsigned syncb = SYNC_OFF + mi * 4096;

  const unsigned kq = w >> 1, mb = w & 1;
  const unsigned vd0 = (lane >> 3) * 1024 + (((lane & 7) ^ (g << 1)) << 4);
  const unsigned vd1 = vd0 + 8192 - 1024;
  const unsigned sd0 = (448 * kq + 16 * mb) * 1024 + n64 * 128;
  const unsigned md1 = lds0 + kq * 4096 + mb * 2048, md3 = lds0 + kq * 12288 + mb * 2048;
  const unsigned rot2 = (rotf * 7) >> 2;
  auto kap = [&](int s) { unsigned k = rot2 + s; return k >= 14 ? k - 14 : k; };
  auto issue_wd = [&](int s, int i) {
    if (i == 0) dma16<0>((wd_spare(s) ? md1 : md3) + wd_off(s), vd0, srdD, sd0 + kap(s) * 32768);
    else dma16n<1024>(vd1, srdD, sd0 + kap(s) * 32768);
  };
  const unsigned dl = (4 * g + q) * 128 + (((2 * g + (q >> 1)) & 3) << 5) + p * 8;
  bf16x8 dr[4][4];
  u32x2 ep;

  if (idx < 7) {
    const unsigned vx0 = (lane >> 3) * 1024 + (((lane & 7) ^ g) << 4);
    const unsigned vx1 = (vx0 ^ 64) + 8192 - 1024;
    const unsigned sx0 = (mi * 128 + 16 * w) * 1024;
    const unsigned vw0 = g * 3584 + (((lane & 15) ^ (g << 1)) << 4);
    const unsigned vw1 = vw0 + (14336 - 1024), vw2 = (vw0 ^ 128) + 2 * (14336 - 1024), vw3 = vw2 + (14336 - 1024);
    const unsigned sw0 = (16 * (w & 3)) * 3584 + fj * 256;
    const unsigned mx0 = lds0 + XOFF + w * 2048;
    const unsigned mw0 = lds0 + GOFF + w * 4096;
    auto issue1 = [&](int s, int i) {
      const unsigned sl = (s % 3) * SLOT;
      if (s < 8) {
        const unsigned sg = (s + rot) & 7;
        if (i == 0) dma16<0>(mw0 + sl, vw0, srdW, sw0 + sg * (64 * 3584));
        if (i == 1) dma16n<1024>(vw1, srdW, sw0 + sg * (64 * 3584));
        if (i == 2) dma16n<2048>(vw2, srdW, sw0 + sg * (64 * 3584));
        if (i == 3) dma16n<3072>(vw3, srdW, sw0 + sg * (64 * 3584));
        if (i == 4) dma16<0>(mx0 + sl, vx0, srdX, sx0 + sg * 128);
        if (i == 5) dma16n<1024>(vx1, srdX, sx0 + sg * 128);
      } else {
        issue_wd(1 + (s - 8) * 3 + (i >> 1), i & 1);
      }
    };
#pragma unroll
    for (int i = 0; i < 6; ++i) issue1(0, i);
    BAR();
#pragma unroll
    for (int i = 0; i < 6; ++i) issue1(1, i);
    gload8_sc1(ep, 0u, srdH, syncb);
    ST(1)

    const unsigned wm = w & 1, wn = w >> 1;
    const unsigned xrow = 64 * wm + (lane & 15);
    const unsigned xa0 = lds0 + XOFF + xrow * 128 + ((g ^ ((xrow >> 1) & 7)) << 4);
    const unsigned wa0 = lds0 + GOFF + (8 * g + q) * 256 + (((2 * wn) ^ (q + 4 * (g & 1))) << 5) + p * 8;
    unsigned xa[3][2], wa[3][2];
#pragma unroll
    for (int sl = 0; sl < 3; ++sl)
#pragma unroll
      for (int k = 0; k < 2; ++k) { xa[sl][k] = opq((xa0 ^ (k << 6)) + sl * SLOT); wa[sl][k] = opq((wa0 ^ (k << 5)) + sl * SLOT); }

    f32x4 acc[4][4];
#pragma unroll
    for (int b = 0; b < 4; ++b)
#pragma unroll
      for (int a = 0; a < 4; ++a) acc[b][a] = f32x4{0.f, 0.f, 0.f, 0.f};

    bf16x8 xf[2][4][2], wf[2][4][2];
    auto ldx = [&](int s, int b, int kk) { xf[s & 1][b][kk] = lds_read16(xa[s % 3][kk] + b * 2048); };
    auto ldw = [&](int s, int a, int kk) {
      const unsigned ad = wa[s % 3][a & 1] + (a >> 1) * 16384 + kk * 8192;
      wf[s & 1][a][kk] = tr_frag(ad, ad + 1024);
    };
    unsigned dA[4], dB[4];
    auto lddr = [&](int s, int a) {
      const unsigned ad = (s == 0) ? dA[a] : dB[a] + (s - 1) * 4096;
      dr[s][a] = tr_frag(ad, ad + 2048);
    };
    const unsigned sh = ((mi * 8 + wm * 4) * 56 + fj * 4 + wn) * 1024;
    auto epi = [&](int b) {
      u32x4 o;
#pragma unroll
      for (int t = 0; t < 2; ++t)
#pragma unroll
        for (int i = 0; i < 2; ++i) {
          float g0 = rnd_bf16(__builtin_elementwise_maximum(acc[b][t][2 * i], 0.f));
          float g1 = rnd_bf16(__builtin_elementwise_maximum(acc[b][t][2 * i + 1], 0.f));
          float u0 = rnd_bf16(acc[b][2 + t][2 * i]), u1 = rnd_bf16(acc[b][2 + t][2 * i + 1]);
          o[2 * t + i] = pack_bf16(g0 * u0, g1 * u1);
        }
      gstore16_sc1(o, lane * 16, srdH, sh + b * (56 * 1024));
    };

    wait_vm<7>();
    ST(2)
    BAR();
    ldw(0, 0, 0); ldx(0, 0, 0); ldw(0, 0, 1); ldx(0, 0, 1);
#pragma unroll
    for (int a = 1; a < 4; ++a) { ldw(0, a, 0); ldw(0, a, 1); }
    SB();
    sfor<0, 128>([&](auto Z) __attribute__((always_inline)) {
      constexpr int s = Z.v >> 4, z = Z.v & 15, b = z >> 2, a = z & 3;
      constexpr int bb = (s == 0) ? 2 : (s == 1 ? 1 : 0);
      if constexpr (s == 6 && z == 15) {
#pragma unroll
        for (int c = 0; c < 4; ++c) { dA[c] = opq((lds0 + kq * 4096 + wd_off(0) + dl) ^ (c << 5)); dB[c] = opq((lds0 + kq * 12288 + wd_off(1) + dl) ^ (c << 5)); }
      }
      if constexpr (z == 4 * bb) {
        if constexpr (s == 0) wait_vm_lgkm0<7>(); else if constexpr (s == 4) wait_vm_lgkm0<8>(); else wait_vm_lgkm0<6>();
        BAR();
        if (s == 0 || s >= 3) { ST(3 + s) }
      }
      acc[b][a] = MFMA16(wf[s & 1][a][0], xf[s & 1][b][0], acc[b][a]);
      acc[b][a] = MFMA16(wf[s & 1][a][1], xf[s & 1][b][1], acc[b][a]);
      SB();
      if constexpr (s == 0 && b < 2) {
        if constexpr (z < 6) ldx(0, 1 + (z >> 1), z & 1);
      } else if constexpr (s == 7) {
        lddr(b, a);
      } else {
        constexpr int n = 15 - 4 * bb, zz = z - 4 * bb;
        if constexpr (zz >= 0 && zz < n) {
          sfor<(zz * 16) / n, ((zz + 1) * 16) / n>([&](auto F) __attribute__((always_inline)) {
            constexpr int u = F.v >> 1, kk = F.v & 1;
            if constexpr (u == 0) ldw(s + 1, 0, kk); else if constexpr (u == 1) ldx(s + 1, 0, kk); else if constexpr (u < 5) ldw(s + 1, u - 1, kk); else ldx(s + 1, u - 4, kk);
          });
        }
      }
      if constexpr (s == 0 && b < 2) { if constexpr (z >= 1 && z < 7) issue1(2, z - 1); }
      else if constexpr (s == 0) { if constexpr (z & 1) issue1(3, (z - 8) >> 1); }
      else if constexpr (s == 1 && b == 0) { if constexpr (z & 1) issue1(3, 4 + (z >> 1)); }
      else if constexpr (s == 1) { if constexpr ((z & 1) == 0) issue1(4, (z - 4) >> 1); }
      else if constexpr (s == 3) { if constexpr (z < 2) issue_wd(0, z); if constexpr (z >= 3 && (z & 1) && z < 15) issue1(s + 3, (z - 3) >> 1); }
      else if constexpr (s == 7) { if constexpr (z < 6) issue1(10, z); }
      else { if constexpr ((z & 1) == 0 && z < 12) issue1(s + 3, z >> 1); }
      if constexpr (s == 7 && b > 0 && a == 1) epi(b - 1);
      SB();
    });
    ST(11)
    epi(3);
    ST(12)
    asm volatile("s_waitcnt vmcnt(0) lgkmcnt(0)" : "+v"(ep)::"memory");
  } else {
    gload8_sc1(ep, 0u, srdH, syncb);
    asm volatile("s_waitcnt vmcnt(0)" : "+v"(ep)::"memory");
  }
  ST(13)
  BAR();
  const uint64_t tag64 = (((uint64_t)ep[1] << 32) | ep[0]) * 0x9E3779B97F4A7C15ull + 1ull;
  const u32x2 tag = {(unsigned)tag64, (unsigned)(tag64 >> 32)};
  if (w == 0 && lane == 0) gstore8_sc1(tag, 0u, srdH, syncb + 128 * (1 + rank));

  const unsigned da1 = lds0 + kq * 4096 + dl, da3 = lds0 + kq * 12288 + dl;
  auto ldd = [&](int s, int a) {
    const unsigned ad = ((wd_spare(s) ? da1 : da3) ^ (a << 5)) + wd_off(s);
    return tr_frag(ad, ad + 2048);
  };
  if (idx == 7) {
    {
      unsigned t;
#pragma unroll
      for (int i = 0; i < 7; ++i) {
        unsigned c = i + xcd; c = c >= 7 ? c - 7 : c; c = c >= 7 ? c - 7 : c;
        asm volatile("buffer_load_dword %0, %1, %2, %3 offen" : "=v"(t) : "v"(lane * 128), "s"(srdD), "s"(((c * 4 + ml) * 8 + w) * 8192) : "memory");
      }
      asm volatile("s_waitcnt vmcnt(0)" : "+v"(t)::"memory");
    }
#pragma unroll
    for (int s = 0; s < 10; ++s) { issue_wd(s, 0); issue_wd(s, 1); }
    wait_vm<0>();
    BAR();
#pragma unroll
    for (int s = 0; s < 4; ++s)
#pragma unroll
      for (int a = 0; a < 4; ++a) dr[s][a] = ldd(s, a);
    wait_lgkm0();
    SB();
    BAR();
  }
#pragma unroll
  for (int s = 10; s < 14; ++s) { issue_wd(s, 0); issue_wd(s, 1); }
  ST(14)
  if (w == 0) {
    const unsigned fo = 128 * (1 + (lane & 15));
    for (unsigned spin = 0; spin < (1u << 20); ++spin) {
      u32x2 f;
      gload8_sc1(f, fo, srdH, syncb);
      asm volatile("s_waitcnt vmcnt(0)" : "+v"(f)::"memory");
      if (__all(f[0] == tag[0] && f[1] == tag[1])) break;
    }
    if (rank == 0 && lane == 0) gstore8_sc1(tag, 0u, srdH, syncb);
  } else {
    wait_vm<0>();
  }
  ST(15)
  BAR();
  u32x4 hf[14][2];
  const unsigned sh2 = ((m64 * 4 + mb * 2) * 56 + 14 * kq) * 1024;
  auto issue_h = [&](int s) {
    gload16_sc1(hf[s][0], lane * 16, srdH, sh2 + kap(s) * 1024);
    gload16_sc1(hf[s][1], lane * 16, srdH, sh2 + kap(s) * 1024 + 56 * 1024);
  };
#pragma unroll
  for (int s = 0; s < P2_WIN; ++s) issue_h(s);
  ST(16)
  f32x4 acc2[2][4];
#pragma unroll
  for (int b = 0; b < 2; ++b)
#pragma unroll
    for (int a = 0; a < 4; ++a) acc2[b][a] = f32x4{0.f, 0.f, 0.f, 0.f};
  bf16x8 df[2][4];
#pragma unroll
  for (int s = 0; s < 14; ++s) {
    if (s + 1 >= 4 && s + 1 < 14) {
#pragma unroll
      for (int a = 0; a < 4; ++a) df[(s + 1) & 1][a] = ldd(s + 1, a);
    }
    const int inflight = ((s + P2_WIN < 14) ? P2_WIN : 14 - s) - 1;
    switch (inflight) {
#define C(k) case k: asm volatile("s_waitcnt vmcnt(%2)" : "+v"(hf[s][0]), "+v"(hf[s][1]) : "i"(2 * k) : "memory"); break;
      C(0) C(1) C(2) C(3) C(4) C(5) C(6) C(7) C(8) C(9) C(10) C(11) C(12) C(13)
#undef C
    }
    if (s == 0) { ST(17) }
    if (s + P2_WIN < 14) issue_h(s + P2_WIN);
#pragma unroll
    for (int a = 0; a < 4; ++a)
#pragma unroll
      for (int b = 0; b < 2; ++b)
        acc2[b][a] = MFMA16(s < 4 ? dr[s][a] : df[s & 1][a], __builtin_bit_cast(bf16x8, hf[s][b]), acc2[b][a]);
    SB();
  }
  ST(18)
  BAR();
  {
    const unsigned pa = lds0 + kq * (64 * 272) + (mb * 32 + (lane & 15)) * 272 + g * 16;
#pragma unroll
    for (int b = 0; b < 2; ++b)
#pragma unroll
      for (int a = 0; a < 4; ++a) *(LDS_AS f32x4*)(uintptr_t)(pa + b * (16 * 272) + a * 64) = acc2[b][a];
  }
  wait_lgkm0();
  BAR();
  ST(19)
  {
    const unsigned row = w * 8 + (lane >> 3);
    const unsigned ra = lds0 + row * 272 + (lane & 7) * 32;
    f32x4 s0 = *(LDS_AS f32x4*)(uintptr_t)(ra), s1 = *(LDS_AS f32x4*)(uintptr_t)(ra + 16);
#pragma unroll
    for (int v = 1; v < 4; ++v) {
      s0 += *(LDS_AS f32x4*)(uintptr_t)(ra + v * (64 * 272));
      s1 += *(LDS_AS f32x4*)(uintptr_t)(ra + v * (64 * 272) + 16);
    }
    u32x4 o = {pack_bf16(s0[0], s0[1]), pack_bf16(s0[2], s0[3]), pack_bf16(s1[0], s1[1]), pack_bf16(s1[2], s1[3])};
#define OUTPOL ""
    const u32x4 srdO = make_srd(out, M_ * D_ * 2);
    asm volatile("buffer_store_dwordx4 %0, %1, %2, %3 offen" OUTPOL ::"v"(o), "v"(row * 1024 + (lane & 7) * 16), "s"(srdO), "s"(m64 * 65536 + n64 * 128) : "memory");
  }
  ST(20)
}

extern "C" void kernel_launch(void* const* d_in, const int* in_sizes, int n_in, void* d_out, int out_size,
                              void* d_ws, size_t ws_size, hipStream_t stream) {
  (void)in_sizes; (void)n_in; (void)out_size; (void)ws_size;
  (void)hipFuncSetAttribute((const void*)k_fused, hipFuncAttributeMaxDynamicSharedMemorySize, LDS_TOTAL);
  k_fused<<<256, 512, LDS_TOTAL, stream>>>(d_in[0], d_in[1], d_in[2], d_in[3], d_out, d_ws);
}
```

```cpp
#include <hip/hip_runtime.h>
#include <hip/hip_bf16.h>
#include <stdint.h>

typedef __attribute__((ext_vector_type(8))) __bf16 bf16x8;
typedef __attribute__((ext_vector_type(4))) short s16x4;
typedef __attribute__((ext_vector_type(8))) short s16x8;
typedef __attribute__((ext_vector_type(2))) __bf16 bf16x2;
typedef __attribute__((ext_vector_type(2))) float f32x2;
typedef __attribute__((ext_vector_type(4))) float f32x4;
typedef __attribute__((ext_vector_type(4))) unsigned u32x4;
typedef __attribute__((ext_vector_type(2))) unsigned u32x2;
#define LDS_AS __attribute__((address_space(3)))
#define ST_DECL
#define ST(i)
#define ST_FLUSH(a,b)

constexpr int M_ = 2048, D_ = 512, F_ = 1792;
constexpr unsigned SYNC_OFF = 8u << 20;
constexpr unsigned SPIN_CAP = 1u << 17;

__device__ __forceinline__ unsigned rfl(unsigned v) { return __builtin_amdgcn_readfirstlane(v); }

__device__ __forceinline__ u32x4 make_srd(const void* p, unsigned bytes) {
  uint64_t a = (uint64_t)p;
  u32x4 r;
  r[0] = rfl((unsigned)a);
  r[1] = rfl((unsigned)(a >> 32)) & 0xffffu;
  r[2] = bytes;
  r[3] = 0x00020000u;
  return r;
}

template <int IMM>
__device__ __forceinline__ void dma16(unsigned m0v, unsigned voff, u32x4 srd, unsigned soff) {
  asm volatile("s_mov_b32 m0, %0\n\ts_nop 0\n\tbuffer_load_dwordx4 %1, %2, %3 offen offset:%4 lds"
               ::"s"(m0v), "v"(voff), "s"(srd), "s"(soff), "i"(IMM) : "memory");
}
template <int IMM>
__device__ __forceinline__ void dma16n(unsigned voff, u32x4 srd, unsigned soff) {
  asm volatile("buffer_load_dwordx4 %0, %1, %2 offen offset:%3 lds" ::"v"(voff), "s"(srd), "s"(soff), "i"(IMM) : "memory");
}
__device__ __forceinline__ unsigned opq(unsigned v) { asm volatile("" : "+v"(v)); return v; }
__device__ __forceinline__ unsigned opqs(unsigned v) { asm volatile("" : "+s"(v)); return v; }
__device__ __forceinline__ void gload16_sc1(u32x4& dst, unsigned voff, u32x4 srd, unsigned soff) {
  asm volatile("buffer_load_dwordx4 %0, %1, %2, %3 offen sc1" : "=v"(dst) : "v"(voff), "s"(srd), "s"(soff) : "memory");
}
__device__ __forceinline__ void greload16_sc1(u32x4& dst, unsigned voff, u32x4 srd, unsigned soff) {
  asm volatile("buffer_load_dwordx4 %0, %1, %2, %3 offen sc1" : "+v"(dst) : "v"(voff), "s"(srd), "s"(soff) : "memory");
}
__device__ __forceinline__ void gstore16_sc1(u32x4 v, unsigned voff, u32x4 srd, unsigned soff) {
  asm volatile("buffer_store_dwordx4 %0, %1, %2, %3 offen sc1\n\ts_nop 1" ::"v"(v), "v"(voff), "s"(srd), "s"(soff) : "memory");
}
__device__ __forceinline__ void gload8_sc1(u32x2& dst, unsigned voff, u32x4 srd, unsigned soff) {
  asm volatile("buffer_load_dwordx2 %0, %1, %2, %3 offen sc1" : "=v"(dst) : "v"(voff), "s"(srd), "s"(soff) : "memory");
}
__device__ __forceinline__ void gstore8_sc1(u32x2 v, unsigned voff, u32x4 srd, unsigned soff) {
  asm volatile("buffer_store_dwordx2 %0, %1, %2, %3 offen sc1" ::"v"(v), "v"(voff), "s"(srd), "s"(soff) : "memory");
}

__device__ __forceinline__ s16x4 tr_read(unsigned addr) {
  return __builtin_amdgcn_ds_read_tr16_b64_v4i16((LDS_AS s16x4*)(uintptr_t)addr);
}
__device__ __forceinline__ bf16x8 tr_frag(unsigned a0, unsigned a1) {
  s16x4 lo = tr_read(a0), hi = tr_read(a1);
  s16x8 v = __builtin_shufflevector(lo, hi, 0, 1, 2, 3, 4, 5, 6, 7);
  return __builtin_bit_cast(bf16x8, v);
}
__device__ __forceinline__ bf16x8 lds_read16(unsigned addr) { return *(LDS_AS bf16x8*)(uintptr_t)addr; }
__device__ __forceinline__ unsigned pack_bf16(float a, float b) {
  f32x2 v = {a, b};
  bf16x2 r = __builtin_convertvector(v, bf16x2);
  return __builtin_bit_cast(unsigned, r);
}
__device__ __forceinline__ float rnd_bf16(float a) { return __builtin_bit_cast(float, pack_bf16(0.f, a)); }

template <class TB>
__device__ __forceinline__ void mfma_acc(f32x4& c, bf16x8 a, TB b) {
  asm volatile("v_mfma_f32_16x16x32_bf16 %0, %1, %2, %0" : "+v"(c) : "v"(a), "v"(b));
}
template <class TB>
__device__ __forceinline__ void mfma_new(f32x4& c, bf16x8 a, TB b) {
  asm volatile("v_mfma_f32_16x16x32_bf16 %0, %1, %2, 0" : "=&v"(c) : "v"(a), "v"(b));
}

__device__ __forceinline__ void mfma2_acc(f32x4& c, bf16x8 a0, bf16x8 b0, bf16x8 a1, bf16x8 b1) {
  asm volatile("v_mfma_f32_16x16x32_bf16 %0, %1, %2, %0\n\tv_mfma_f32_16x16x32_bf16 %0, %3, %4, %0" : "+v"(c) : "v"(a0), "v"(b0), "v"(a1), "v"(b1));
}
__device__ __forceinline__ void mfma2_new(f32x4& c, bf16x8 a0, bf16x8 b0, bf16x8 a1, bf16x8 b1) {
  asm volatile("v_mfma_f32_16x16x32_bf16 %0, %1, %2, 0\n\tv_mfma_f32_16x16x32_bf16 %0, %3, %4, %0" : "=&v"(c) : "v"(a0), "v"(b0), "v"(a1), "v"(b1));
}

template <int I> struct IC { static constexpr int v = I; };
template <int I, int N, class F> __device__ __forceinline__ void sfor(F&& f) {
  if constexpr (I < N) { f(IC<I>{}); sfor<I + 1, N>(f); }
}
template <int N> __device__ __forceinline__ void wait_vm() { asm volatile("s_waitcnt vmcnt(%0)" ::"i"(N) : "memory"); }
template <int N> __device__ __forceinline__ void wait_vm_lgkm0() { asm volatile("s_waitcnt vmcnt(%0) lgkmcnt(0)" ::"i"(N) : "memory"); }
__device__ __forceinline__ void wait_lgkm0() { asm volatile("s_waitcnt lgkmcnt(0)" ::: "memory"); }
#define BAR() __builtin_amdgcn_s_barrier()
#define SB() __builtin_amdgcn_sched_barrier(0)
#define INL __attribute__((always_inline))

constexpr int UNIT = 16384, LDS_TOTAL = 10 * UNIT;
constexpr int uaddr(int n) { return (n % 10) * UNIT; }

enum { K_DMA = 1, K_STORE, K_FLAG, K_POLL, K_H };
struct Op { int pos, kind, n, i; };
struct Sched { int cnt; Op op[200]; };
constexpr void put(Sched& s, int pos, int kind, int n, int i) { s.op[s.cnt++] = Op{pos, kind, n, i}; }
constexpr void spread(Sched& s, int p0, int p1, int u0, int nu) {
  for (int k = 0; k < 2 * nu; ++k) put(s, p0 + (k * (p1 - p0)) / (2 * nu), K_DMA, u0 + (k >> 1), k & 1);
}
constexpr int Q(int step, int slot) { return 200 + 8 * step + slot; }
constexpr void pair(Sched& s, int step, int slot, int kind, int n) { put(s, Q(step, slot), kind, n, 0); put(s, Q(step, slot == 0 ? 0 : slot + 1), kind, n, 1); }
#define HB 3
constexpr int Y1 = 2, Y2 = 5, FCHK = 7, Y3 = 10;
constexpr Sched make_sched() {
  Sched s{};
  for (int n = 0; n < 4; ++n) { put(s, -1, K_DMA, n, 0); put(s, -1, K_DMA, n, 1); }
  put(s, -1, K_POLL, 0, 0);
  spread(s, 0, 2, 4, 2);
  for (int t = 0; t < 7; ++t) spread(s, 8 * t + 2, 8 * t + 9, 2 * t + 6, 2);
  spread(s, 58, 66, 20, 2);
  put(s, 67, K_STORE, 0, 0);
  put(s, 69, K_STORE, 0, 1);
  spread(s, 72, 80, 22, 1);
  spread(s, 82, 89, 23, 2);
  put(s, 90, K_FLAG, 0, 0);
  spread(s, 91, 103, 25, 2);
  spread(s, 103, 111, 27, 2);
  put(s, 111, K_POLL, 1, 0);
  spread(s, 112, 121, 29, 2);
  for (int k = 0; k < 4; ++k) put(s, 122 + k, K_H, k >> 1, k & 1);
  put(s, 128, K_STORE, 1, 0);
  put(s, 128, K_STORE, 1, 1);
  put(s, 128, K_H, 2, 0);
  put(s, 128, K_H, 2, 1);
  put(s, Q(Y1, 0), K_FLAG, 1, 0);
  pair(s, 2, 0, K_H, 3); pair(s, 2, 0, K_H, 4);
  pair(s, 2, 3, K_DMA, 31);
  pair(s, 3, 1, K_H, 5); pair(s, 3, 3, K_H, 6);
  pair(s, 4, 1, K_DMA, 32); pair(s, 4, 3, K_DMA, 33);
  pair(s, 5, 1, K_DMA, 34); pair(s, 5, 3, K_DMA, 35);
  put(s, Q(6, 0), K_POLL, 2, 0);
  pair(s, 6, 1, K_DMA, 36); pair(s, 6, 3, K_DMA, 37);
  for (int k = 0; k < HB; ++k) pair(s, 7, 0, K_H, 7 + k);
  for (int k = 7 + HB; k < 14; ++k) pair(s, k - HB, 1, K_H, k);
  return s;
}
constexpr Sched SCH = make_sched();
constexpr int younger_than(int kind, int n, int i, int P) {
  int c = -1;
  for (int k = 0; k < SCH.cnt; ++k) {
    if (SCH.op[k].pos >= P) break;
    if (SCH.op[k].kind == kind && SCH.op[k].n == n && SCH.op[k].i == i) c = 0; else if (c >= 0) ++c;
  }
  return c;
}
constexpr int first_op(int P) { int k = 0; while (k < SCH.cnt && SCH.op[k].pos < P) ++k; return k; }
constexpr bool is_bar(int P) { return P < 64 ? (P & 7) == 2 : (P & 15) == 10; }
constexpr int bar_wait(int P) {
  if (P < 64) return younger_than(K_DMA, 2 * (P >> 3) + 3, 1, P);
  if (P == 74) return younger_than(K_DMA, 19, 1, P);
  if (P == 90) return younger_than(K_STORE, 0, 1, P);
  if (P == 106) return younger_than(K_DMA, 23, 1, P);
  if (P == 122) return younger_than(K_POLL, 1, 0, P);
  if (P == Q(Y1, 0)) return younger_than(K_STORE, 1, 1, P);
  if (P == Q(Y2, 0)) return younger_than(K_DMA, 31, 1, P);
  if (P == Q(FCHK, 0)) return younger_than(K_POLL, 2, 0, P);
  return younger_than(K_DMA, 37, 1, P);
}
constexpr bool sched_sorted() { for (int k = 1; k < SCH.cnt; ++k) if (SCH.op[k].pos < SCH.op[k - 1].pos) return false; return true; }
static_assert(sched_sorted(), "schedule order");

__global__ __launch_bounds__(512, 2) void k_fused(const void* __restrict__ x, const void* __restrict__ Wg,
                                                  const void* __restrict__ Wu, const void* __restrict__ Wd,
                                                  void* __restrict__ out, void* __restrict__ ws) {
  extern __shared__ __attribute__((aligned(16))) char smem[];
  const unsigned lds0 = (unsigned)(uintptr_t)(LDS_AS char*)smem;
  ST_DECL ST(0)
  const unsigned bid = blockIdx.x;
  const unsigned w = rfl(threadIdx.x >> 6);
  const bool busy = bid < 224;
#define CPF 0
  const unsigned mi = (bid & 6) * 2 + ((bid >> 3) & 3), half = bid & 1, idx = bid >> 5;
  const unsigned rot = ((bid & 6) + 4 * half) & 7;
  unsigned vw0, vw1, vx0, vx1;
  {
    const unsigned lane = threadIdx.x & 63, g = lane >> 4;
    vw0 = (lane >> 3) * 3584 + (((lane & 7) ^ ((g & 1) << 1)) << 4);
    vw1 = (vw0 ^ 64) + 8 * 3584 - 1024;
    vx0 = (lane >> 3) * 1024 + (((lane & 7) ^ g) << 4);
    vx1 = (vx0 ^ 64) + 8192 - 1024;
  }
  unsigned sx0 = (mi * 128 + 16 * w) * 1024;
  unsigned sw0 = (16 * (w & 3)) * 3584 + (half * 14 + idx) * 128;
  unsigned mu0 = lds0 + w * 2048;
  unsigned sw00 = sw0 + rot * (64 * 3584), sx00 = sx0 + rot * 128;
  asm volatile("" : "+v"(vw0), "+v"(vw1), "+v"(vx0), "+v"(vx1));
  asm volatile("" : "+s"(sx0), "+s"(sw0), "+s"(mu0), "+s"(sw00), "+s"(sx00));
  SB();
  const u32x4 srdX = make_srd(x, M_ * D_ * 2);
  const u32x4 srdW = make_srd((w >> 2) ? Wu : Wg, D_ * F_ * 2);
  auto issue_xw = [&](int n, int i) {
    const int s = n < 16 ? n >> 1 : n - 16;
    const unsigned sg = (s + rot) & 7;
    if (n < 16 && (n & 1) == 0) {
      const unsigned so = n == 0 ? sx00 : sx0 + sg * 128;
      if (i == 0) dma16<0>(mu0 + uaddr(n), vx0, srdX, so); else dma16n<1024>(vx1, srdX, so);
    } else {
      const unsigned so = n == 1 ? sw00 : sw0 + sg * (64 * 3584) + (n >= 16 ? 896 : 0);
      if (i == 0) dma16<0>(mu0 + uaddr(n), vw0, srdW, so); else dma16n<1024>(vw1, srdW, so);
    }
  };
  asm volatile("" ::"s"(Wd), "s"(out), "s"(ws));
  if (busy) {
    issue_xw(0, 0); issue_xw(0, 1); issue_xw(1, 0); issue_xw(1, 1);
    BAR();
    issue_xw(2, 0); issue_xw(2, 1); issue_xw(3, 0); issue_xw(3, 1);
  }
  SB();
  const unsigned tid = opq(threadIdx.x), lane = tid & 63;
  const unsigned bid2 = opqs(bid), xcd = bid2 & 7, ml = (bid2 >> 3) & 3;
  unsigned rank = half * 8 + idx;
  const unsigned m64 = 2 * mi + half, n64 = idx;
  const unsigned g = lane >> 4, q = (lane & 15) >> 2, p = lane & 3;
  const u32x4 srdH = make_srd(ws, SYNC_OFF + 16 * 8192);
  const u32x4 srdD = make_srd(Wd, F_ * D_ * 2);
  unsigned syncb = SYNC_OFF + mi * 8192;
  const unsigned lane16 = lane * 16;

  const unsigned kq = w >> 1, mb = w & 1;
  unsigned rot2 = 2 * ml + idx; rot2 = rot2 >= 7 ? rot2 - 7 : rot2;
  unsigned kb[7];
  auto set_kb = [&]() {
    unsigned e = opqs(rot2);
#pragma unroll
    for (int j = 0; j < 7; ++j) {
      kb[j] = opqs((e + 7 * kq + (kq >> 1) * 14) * 1024);
      e = e == 6 ? 0 : e + 1;
    }
  };
  auto kap = [&](int s) { return kb[s % 7] + (s >= 7 ? 14 * 1024 : 0); };
  const unsigned vd0 = (lane >> 3) * 1024 + (((lane & 7) ^ (g << 1)) << 4);
  const unsigned vd1 = vd0 + 8192 - 1024;
  unsigned sd0 = (16 * mb) * 1024 + n64 * 128;
  auto issue_wd = [&](int s, int i) {
    if (i == 0) dma16<0>(mu0 + uaddr(24 + s), vd0, srdD, sd0 + kap(s) * 32);
    else dma16n<1024>(vd1, srdD, sd0 + kap(s) * 32);
  };
  const unsigned dl = lds0 + kq * 4096 + (4 * g + q) * 128 + (((2 * g + (q >> 1)) & 3) << 5) + p * 8;
  unsigned db[3][4];
  auto set_db = [&](int k) {
#pragma unroll
    for (int c = 0; c < 4; ++c) db[k][c] = opq((dl ^ (c << 5)) + k * 4 * UNIT);
  };
  auto ldd = [&](int s, int a) {
    const int ua = uaddr(24 + s);
    const unsigned ad = db[ua >> 16][a] + (ua & 65535);
    return tr_frag(ad, ad + 2048);
  };
  u32x2 ep, tag, fl;
  auto mktag = [&]() {
    const uint64_t t = (((uint64_t)ep[1] << 32) | ep[0]) * 0x9E3779B97F4A7C15ull + 1ull;
    tag = u32x2{(unsigned)t, (unsigned)(t >> 32)};
  };
  const unsigned one = tid == 0 ? 0u : 0x40000000u;
  const unsigned fo = tid < 16 ? 128 * (1 + tid) : 0x40000000u;
  unsigned fmask = 0x7F7Fu;
  auto flags_ok = [&]() { return (rfl((unsigned)__builtin_amdgcn_ballot_w64(fl[0] == tag[0] && fl[1] == tag[1])) & fmask) == fmask; };
  auto poll = [&](unsigned off) {
    unsigned cnt = SPIN_CAP, t;
#define PLD(r) "buffer_load_dwordx2 " r ", %2, %3, %4 offen sc1\n\t"
#define PCK(r) "s_waitcnt vmcnt(2)\n\tv_cmp_eq_u64 vcc, " r ", %5\n\ts_nop 1\n\ts_and_b32 %1, vcc_lo, %6\n\ts_cmp_eq_u32 %1, %6\n\ts_cbranch_scc1 2f\n\t"
    asm volatile(PLD("v[250:251]") "s_sleep 3\n\t" PLD("v[252:253]") "s_sleep 3\n\t" PLD("v[254:255]")
                 "1:\n\t"
                 PCK("v[250:251]") PLD("v[250:251]") PCK("v[252:253]") PLD("v[252:253]") PCK("v[254:255]") PLD("v[254:255]")
                 "s_sub_u32 %0, %0, 1\n\ts_cbranch_scc0 1b\n\t"
                 "2:"
                 : "+s"(cnt), "=&s"(t) : "v"(fo), "s"(srdH), "s"(syncb + off), "v"(tag), "s"(fmask)
                 : "v250", "v251", "v252", "v253", "v254", "v255", "vcc", "scc", "memory");
#undef PLD
#undef PCK
  };
  auto poll_slow = [&](unsigned off) {
#pragma nounroll
    for (unsigned spin = 0; spin < SPIN_CAP; ++spin) {
      gload8_sc1(fl, fo, srdH, syncb + off);
      asm volatile("s_waitcnt vmcnt(0)" : "+v"(fl)::"memory");
      if (flags_ok()) break;
      __builtin_amdgcn_s_sleep(8);
    }
  };
  bf16x8 df[2][4];
  u32x4 hf[14][2];
  unsigned sh2 = ((m64 * 4 + mb * 2) * 56) * 1024;
  const unsigned lane16b = lane16 + 56 * 1024;
  auto issue_h = [&](int s, int b) { gload16_sc1(hf[s][b], b ? lane16b : lane16, srdH, sh2 + kap(s)); };

  set_kb();
  if (__builtin_expect(busy, 1)) {
    gload8_sc1(ep, 0u, srdH, syncb);
    const unsigned wm = w & 3, wn = w >> 2;
    const unsigned xrow = 32 * wm + (lane & 15);
    const unsigned xa0 = lds0 + xrow * 128 + ((g ^ ((xrow >> 1) & 7)) << 4);
    const unsigned wa0 = lds0 + (8 * g + q) * 128 + (((2 * wn) ^ ((q >> 1) | ((g & 1) << 1))) << 5) + p * 8;
    unsigned xa[3][2], wa[3][2];
#pragma unroll
    for (int k = 0; k < 3; ++k)
#pragma unroll
      for (int c = 0; c < 2; ++c) { xa[k][c] = opq((xa0 ^ (c << 6)) + k * 4 * UNIT); wa[k][c] = opq((wa0 ^ (c << 5)) + k * 4 * UNIT); }
    ST(1)

    f32x4 acc[2][2][4];
    bf16x8 xf[8][2][2];
    bf16x8 wf[4][2];
    auto ldx = [&](int s, int b, int kk) { const int ua = uaddr(2 * s); xf[s][b][kk] = lds_read16(xa[ua >> 16][kk] + (ua & 65535) + b * 2048); };
    auto ldw = [&](int G, int kk) {
      const int st = G >> 2, a = G & 3, ua = uaddr(st < 8 ? 2 * st + 1 : 8 + st);
      const unsigned ad = wa[ua >> 16][a & 1] + (ua & 65535) + (a >> 1) * 8192 + kk * 4096;
      wf[G & 3][kk] = tr_frag(ad, ad + 512);
    };
    const unsigned sh = ((mi * 8 + wm * 2) * 56 + half * 28 + idx * 2 + wn) * 1024;
    u32x4 ho[2];
    auto epi = [&](int ps, int b, int c) {
      const int t = c >> 1, i = c & 1;
      float g0 = __builtin_elementwise_maximum(acc[ps][b][t][2 * i], 0.f), g1 = __builtin_elementwise_maximum(acc[ps][b][t][2 * i + 1], 0.f);
      float p0 = g0 * acc[ps][b][2 + t][2 * i];
      asm volatile("" : "+v"(p0));
      ho[b][c] = pack_bf16(p0, g1 * acc[ps][b][2 + t][2 * i + 1]);
    };
    auto sth = [&](int ps, int b) { gstore16_sc1(ho[b], lane16, srdH, sh + b * (56 * 1024) + ps * (14 * 1024)); };

    auto vmem = [&](auto PP) INL {
      sfor<first_op(PP.v), first_op(PP.v + 1)>([&](auto K) INL {
        constexpr Op o = SCH.op[K.v];
        if constexpr (o.pos == PP.v) {
          if constexpr (o.kind == K_DMA) { if constexpr (o.n < 24) issue_xw(o.n, o.i); else issue_wd(o.n - 24, o.i); }
          if constexpr (o.kind == K_STORE) sth(o.n, o.i);
          if constexpr (o.kind == K_FLAG) gstore8_sc1(tag, one, srdH, syncb + 128 * (1 + rank));
          if constexpr (o.kind == K_POLL) gload8_sc1(fl, fo, srdH, syncb);
          if constexpr (o.kind == K_H) issue_h(o.n, o.i);
        }
      });
    };
    wait_vm<5>();
    ST(2)
    BAR();
    ldw(0, 0); ldx(0, 0, 0); ldw(0, 1); ldx(0, 0, 1); ldx(0, 1, 0); ldx(0, 1, 1); ldw(1, 0); ldw(1, 1); ldw(2, 0); ldw(2, 1);
    SB();
    sfor<0, 128>([&](auto Z) INL {
      constexpr int P = Z.v, ps = P >> 6, s = (P >> 3) & 7, G = P >> 1, a = G & 3, b = P & 1;
      if constexpr (is_bar(P)) {
        constexpr int n = bar_wait(P);
        static_assert(n >= 0 && n < 24, "wait count");
        if constexpr (P == 10) asm volatile("s_waitcnt vmcnt(%1)" : "+v"(ep) : "i"(n) : "memory");
        else if constexpr (P == 122) {
          asm volatile("s_waitcnt vmcnt(%1)" : "+v"(fl) : "i"(n) : "memory");
          if (w == 0) { if (__builtin_expect(!flags_ok(), 0)) poll(0); }
        } else wait_vm<n>();
        BAR();
        if (P == 2) { ST(3) } if (P == 26) { ST(4) } if (P == 58) { ST(5) } if (P == 74) { ST(6) } if (P == 90) { ST(7) } if (P == 106) { ST(8) } if (P == 122) { ST(9) }
      }
      if constexpr (P < 58 ? (P & 1) == 0 : (P & 3) == 2) __builtin_amdgcn_s_setprio(
P < 58 ? 3 - (((P + 6) & 7) >> 1) : 3 - (((P + 6) & 15) >> 2));
      if constexpr (s == 0) mfma2_new(acc[ps][b][a], wf[G & 3][0], xf[s][b][0], wf[G & 3][1], xf[s][b][1]);
      else mfma2_acc(acc[ps][b][a], wf[G & 3][0], xf[s][b][0], wf[G & 3][1], xf[s][b][1]);
      SB();
      if constexpr (G + 3 < 64) ldw(G + 3, b);
      if constexpr (P < 56 && (P & 7) >= 2 && (P & 7) < 6) ldx(s + 1, ((P - 2) >> 1) & 1, P & 1);
      if constexpr (P == 120) set_db(1);
      if constexpr (P >= 124) df[0][P - 124] = ldd(0, P - 124);
      if constexpr (P == 65) { epi(0, 0, 0); epi(0, 0, 1); }
      if constexpr (P == 66) { epi(0, 1, 0); epi(0, 1, 1); }
      if constexpr (P == 67) { epi(0, 0, 2); epi(0, 0, 3); }
      if constexpr (P == 68 || P == 69) epi(0, 1, P - 66);
      if constexpr (P == 84) mktag();
      vmem(Z);
      SB();
    });
    ST(11)
    set_db(0); set_db(2);
    epi(1, 0, 0); epi(1, 0, 1); epi(1, 1, 0); epi(1, 1, 1);
    SB();
    epi(1, 0, 2); epi(1, 0, 3); epi(1, 1, 2); epi(1, 1, 3);
    vmem(IC<128>{});
    __builtin_amdgcn_s_setprio(0);
    ST(12)
  } else {
    gload8_sc1(ep, 0u, srdH, syncb);
    set_db(0); set_db(1); set_db(2);
    {
      unsigned t = 0;
#define NPF 7
#define PFX 0
#define PFB 0
#pragma unroll
      for (int i = 0; i < NPF; ++i) {
        unsigned c = i + xcd; c = c >= 7 ? c - 7 : c; c = c >= 7 ? c - 7 : c;
        asm volatile("buffer_load_dword %0, %1, %2, %3 offen" : "+v"(t) : "v"(lane * 128), "s"(srdD), "s"(((c * 4 + ml) * 8 + w) * 8192) : "memory");
      }
      asm volatile("s_waitcnt vmcnt(0)" : "+v"(t), "+v"(ep)::"memory");
      mktag();
    }
#pragma unroll
    for (int s = 0; s < 7; ++s) { issue_wd(s, 0); issue_wd(s, 1); }
    if (w == 0) poll_slow(0); else wait_vm<0>();
    BAR();
#pragma unroll
    for (int s = 0; s < 3; ++s) { issue_h(s, 0); issue_h(s, 1); }
#pragma unroll
    for (int a = 0; a < 4; ++a) df[0][a] = ldd(0, a);
    wait_vm<0>();
  }
  asm volatile("" : "+s"(mu0), "+s"(sd0), "+s"(sh2), "+s"(syncb), "+s"(rank));
  asm volatile("" : "+s"(kb[0]), "+s"(kb[1]), "+s"(kb[2]), "+s"(kb[3]), "+s"(kb[4]), "+s"(kb[5]), "+s"(kb[6]));
  fmask = rank == 7 ? 0xFFFFu : 0x7F7Fu;
  f32x4 acc2[2][4];
  auto vmem2 = [&](auto PP) INL {
    sfor<first_op(PP.v), first_op(PP.v + 1)>([&](auto K) INL {
      constexpr Op o = SCH.op[K.v];
      if constexpr (o.pos == PP.v) {
        if constexpr (o.kind == K_DMA) issue_wd(o.n - 24, o.i);
        if constexpr (o.kind == K_FLAG) gstore8_sc1(tag, one, srdH, syncb + 128 * (17 + rank));
        if constexpr (o.kind == K_POLL) gload8_sc1(fl, fo, srdH, syncb + 2048);
        if constexpr (o.kind == K_H) issue_h(o.n, o.i);
      }
    });
  };
  sfor<0, 14>([&](auto S) INL {
    constexpr int s = S.v;
    if constexpr (s == Y1 || s == FCHK || s == Y2 || s == Y3) {
      constexpr int n = bar_wait(Q(s, 0));
      static_assert(n >= 0 && n < 24, "wait count");
      if constexpr (s == FCHK) {
        asm volatile("s_waitcnt vmcnt(%1)" : "+v"(fl) : "i"(n) : "memory");
        if (w == 0) { if (__builtin_expect(!flags_ok(), 0)) poll(2048); }
      } else wait_vm<n>();
      BAR();
      if (s == Y1) { ST(13) } if (s == FCHK) { ST(14) } if (s == Y2) { ST(15) } if (s == Y3) { ST(16) }
      if constexpr (s == Y3) { if (rank == 7 && w == 0) gstore8_sc1(tag, one, srdH, syncb); }
    }
    vmem2(IC<Q(s, 0)>{});
    asm volatile("s_waitcnt vmcnt(%2)" : "+v"(hf[s][0]), "+v"(hf[s][1]) : "i"(younger_than(K_H, s, 1, Q(s, 1))) : "memory");
    if (s == 0) { ST(17) }
    SB();
    sfor<0, 4>([&](auto A) INL {
      constexpr int a = A.v;
      if constexpr (s == 0) { mfma_new(acc2[0][a], df[0][a], hf[0][0]); mfma_new(acc2[1][a], df[0][a], hf[0][1]); }
      else { mfma_acc(acc2[0][a], df[s & 1][a], hf[s][0]); mfma_acc(acc2[1][a], df[s & 1][a], hf[s][1]); }
      SB();
      vmem2(IC<Q(s, a + 1)>{});
      if constexpr (s + 1 < 14) df[(s + 1) & 1][a] = ldd(s + 1, a);
      SB();
    });
  });
  ST(18)
  asm volatile("s_nop 7" ::: "memory");
  SB();
  {
    const unsigned pa = lds0 + (kq == 3 ? 8 * UNIT : kq * (64 * 272)) + (mb * 32 + (lane & 15)) * 272 + g * 16;
#pragma unroll
    for (int a = 0; a < 4; ++a)
#pragma unroll
      for (int b = 0; b < 2; ++b) *(LDS_AS f32x4*)(uintptr_t)(pa + b * (16 * 272) + a * 64) = acc2[b][a];
  }
  wait_lgkm0();
  BAR();
  ST(19)
  {
    const unsigned row = w * 8 + (lane >> 3);
    const unsigned ra = lds0 + row * 272 + (lane & 7) * 32;
    f32x4 s0 = *(LDS_AS f32x4*)(uintptr_t)(ra), s1 = *(LDS_AS f32x4*)(uintptr_t)(ra + 16);
#pragma unroll
    for (int v = 1; v < 4; ++v) {
      const unsigned o = v == 3 ? 8 * UNIT : v * (64 * 272);
      s0 += *(LDS_AS f32x4*)(uintptr_t)(ra + o);
      s1 += *(LDS_AS f32x4*)(uintptr_t)(ra + o + 16);
    }
    u32x4 o = {pack_bf16(s0[0], s0[1]), pack_bf16(s0[2], s0[3]), pack_bf16(s1[0], s1[1]), pack_bf16(s1[2], s1[3])};
    *(u32x4*)((char*)out + (size_t)(m64 * 64 + row) * 1024 + n64 * 128 + (lane & 7) * 16) = o;
  }
  ST(20)
}

extern "C" void kernel_launch(void* const* d_in, const int* in_sizes, int n_in, void* d_out, int out_size,
                              void* d_ws, size_t ws_size, hipStream_t stream) {
  (void)in_sizes; (void)n_in; (void)out_size; (void)ws_size;
  (void)hipFuncSetAttribute((const void*)k_fused, hipFuncAttributeMaxDynamicSharedMemorySize, LDS_TOTAL);
  k_fused<<<256, 512, LDS_TOTAL, stream>>>(d_in[0], d_in[1], d_in[2], d_in[3], d_out, d_ws);
}
```

```cpp
#include <hip/hip_runtime.h>
#include <hip/hip_bf16.h>
#include <stdint.h>

typedef __attribute__((ext_vector_type(8))) __bf16 bf16x8;
typedef __attribute__((ext_vector_type(4))) short s16x4;
typedef __attribute__((ext_vector_type(8))) short s16x8;
typedef __attribute__((ext_vector_type(2))) __bf16 bf16x2;
typedef __attribute__((ext_vector_type(2))) float f32x2;
typedef __attribute__((ext_vector_type(4))) float f32x4;
typedef __attribute__((ext_vector_type(4))) unsigned u32x4;
typedef __attribute__((ext_vector_type(2))) unsigned u32x2;
#define LDS_AS __attribute__((address_space(3)))
#define ST_DECL
#define ST(i)
#define ST_FLUSH(a,b)

constexpr int M_ = 2048, D_ = 512, F_ = 1792;
constexpr unsigned SYNC_OFF = 8u << 20;
constexpr unsigned SPIN_CAP = 1u << 17;

__device__ __forceinline__ unsigned rfl(unsigned v) { return __builtin_amdgcn_readfirstlane(v); }

__device__ __forceinline__ u32x4 make_srd(const void* p, unsigned bytes) {
  uint64_t a = (uint64_t)p;
  u32x4 r;
  r[0] = rfl((unsigned)a);
  r[1] = rfl((unsigned)(a >> 32)) & 0xffffu;
  r[2] = bytes;
  r[3] = 0x00020000u;
  return r;
}

template <int IMM>
__device__ __forceinline__ void dma16(unsigned m0v, unsigned voff, u32x4 srd, unsigned soff) {
  asm volatile("s_mov_b32 m0, %0\n\ts_nop 0\n\tbuffer_load_dwordx4 %1, %2, %3 offen offset:%4 lds"
               ::"s"(m0v), "v"(voff), "s"(srd), "s"(soff), "i"(IMM) : "memory");
}
template <int IMM>
__device__ __forceinline__ void dma16n(unsigned voff, u32x4 srd, unsigned soff) {
  asm volatile("buffer_load_dwordx4 %0, %1, %2 offen offset:%3 lds" ::"v"(voff), "s"(srd), "s"(soff), "i"(IMM) : "memory");
}
__device__ __forceinline__ unsigned opq(unsigned v) { asm volatile("" : "+v"(v)); return v; }
__device__ __forceinline__ unsigned opqs(unsigned v) { asm volatile("" : "+s"(v)); return v; }
__device__ __forceinline__ void gload16_sc1(u32x4& dst, unsigned voff, u32x4 srd, unsigned soff) {
  asm volatile("buffer_load_dwordx4 %0, %1, %2, %3 offen sc1" : "=v"(dst) : "v"(voff), "s"(srd), "s"(soff) : "memory");
}
__device__ __forceinline__ void greload16_sc1(u32x4& dst, unsigned voff, u32x4 srd, unsigned soff) {
  asm volatile("buffer_load_dwordx4 %0, %1, %2, %3 offen sc1" : "+v"(dst) : "v"(voff), "s"(srd), "s"(soff) : "memory");
}
__device__ __forceinline__ void gstore16_sc1(u32x4 v, unsigned voff, u32x4 srd, unsigned soff) {
  asm volatile("buffer_store_dwordx4 %0, %1, %2, %3 offen sc1\n\ts_nop 1" ::"v"(v), "v"(voff), "s"(srd), "s"(soff) : "memory");
}
__device__ __forceinline__ void gload8_sc1(u32x2& dst, unsigned voff, u32x4 srd, unsigned soff) {
  asm volatile("buffer_load_dwordx2 %0, %1, %2, %3 offen sc1" : "=v"(dst) : "v"(voff), "s"(srd), "s"(soff) : "memory");
}
__device__ __forceinline__ void gstore8_sc1(u32x2 v, unsigned voff, u32x4 srd, unsigned soff) {
  asm volatile("buffer_store_dwordx2 %0, %1, %2, %3 offen sc1" ::"v"(v), "v"(voff), "s"(srd), "s"(soff) : "memory");
}

__device__ __forceinline__ s16x4 tr_read(unsigned addr) {
  return __builtin_amdgcn_ds_read_tr16_b64_v4i16((LDS_AS s16x4*)(uintptr_t)addr);
}
__device__ __forceinline__ bf16x8 tr_frag(unsigned a0, unsigned a1) {
  s16x4 lo = tr_read(a0), hi = tr_read(a1);
  s16x8 v = __builtin_shufflevector(lo, hi, 0, 1, 2, 3, 4, 5, 6, 7);
  return __builtin_bit_cast(bf16x8, v);
}
__device__ __forceinline__ bf16x8 lds_read16(unsigned addr) { return *(LDS_AS bf16x8*)(uintptr_t)addr; }
__device__ __forceinline__ unsigned pack_bf16(float a, float b) {
  f32x2 v = {a, b};
  bf16x2 r = __builtin_convertvector(v, bf16x2);
  return __builtin_bit_cast(unsigned, r);
}
__device__ __forceinline__ float rnd_bf16(float a) { return __builtin_bit_cast(float, pack_bf16(0.f, a)); }

template <class TB>
__device__ __forceinline__ void mfma_acc(f32x4& c, bf16x8 a, TB b) {
  asm volatile("v_mfma_f32_16x16x32_bf16 %0, %1, %2, %0" : "+v"(c) : "v"(a), "v"(b));
}
template <class TB>
__device__ __forceinline__ void mfma_new(f32x4& c, bf16x8 a, TB b) {
  asm volatile("v_mfma_f32_16x16x32_bf16 %0, %1, %2, 0" : "=&v"(c) : "v"(a), "v"(b));
}

__device__ __forceinline__ void mfma2_acc(f32x4& c, bf16x8 a0, bf16x8 b0, bf16x8 a1, bf16x8 b1) {
  asm volatile("v_mfma_f32_16x16x32_bf16 %0, %1, %2, %0\n\tv_mfma_f32_16x16x32_bf16 %0, %3, %4, %0" : "+v"(c) : "v"(a0), "v"(b0), "v"(a1), "v"(b1));
}
__device__ __forceinline__ void mfma2_new(f32x4& c, bf16x8 a0, bf16x8 b0, bf16x8 a1, bf16x8 b1) {
  asm volatile("v_mfma_f32_16x16x32_bf16 %0, %1, %2, 0\n\tv_mfma_f32_16x16x32_bf16 %0, %3, %4, %0" : "=&v"(c) : "v"(a0), "v"(b0), "v"(a1), "v"(b1));
}

template <int I> struct IC { static constexpr int v = I; };
template <int I, int N, class F> __device__ __forceinline__ void sfor(F&& f) {
  if constexpr (I < N) { f(IC<I>{}); sfor<I + 1, N>(f); }
}
template <int N> __device__ __forceinline__ void wait_vm() { asm volatile("s_waitcnt vmcnt(%0)" ::"i"(N) : "memory"); }
template <int N> __device__ __forceinline__ void wait_vm_lgkm0() { asm volatile("s_waitcnt vmcnt(%0) lgkmcnt(0)" ::"i"(N) : "memory"); }
__device__ __forceinline__ void wait_lgkm0() { asm volatile("s_waitcnt lgkmcnt(0)" ::: "memory"); }
#define BAR() __builtin_amdgcn_s_barrier()
#define SB() __builtin_amdgcn_sched_barrier(0)
#define INL __attribute__((always_inline))

constexpr int UNIT = 16384, LDS_TOTAL = 10 * UNIT;
constexpr int uaddr(int n) { return (n % 10) * UNIT; }

enum { K_DMA = 1, K_STORE, K_FLAG, K_POLL, K_H };
struct Op { int pos, kind, n, i; };
struct Sched { int cnt; Op op[200]; };
constexpr void put(Sched& s, int pos, int kind, int n, int i) { s.op[s.cnt++] = Op{pos, kind, n, i}; }
constexpr void spread(Sched& s, int p0, int p1, int u0, int nu) {
  for (int k = 0; k < 2 * nu; ++k) put(s, p0 + (k * (p1 - p0)) / (2 * nu), K_DMA, u0 + (k >> 1), k & 1);
}
constexpr int Q(int step, int slot) { return 200 + 8 * step + slot; }
constexpr void pair(Sched& s, int step, int slot, int kind, int n) { put(s, Q(step, slot), kind, n, 0); put(s, Q(step, slot == 0 ? 0 : slot + 1), kind, n, 1); }
#define HB 3
constexpr int Y1 = 2, Y2 = 5, FCHK = 7, Y3 = 10;
constexpr Sched make_sched() {
  Sched s{};
  for (int n = 0; n < 4; ++n) { put(s, -1, K_DMA, n, 0); put(s, -1, K_DMA, n, 1); }
  put(s, -1, K_POLL, 0, 0);
  spread(s, 0, 2, 4, 2);
  for (int t = 0; t < 7; ++t) spread(s, 8 * t + 2, 8 * t + 9, 2 * t + 6, 2);
  spread(s, 58, 66, 20, 2);
  put(s, 67, K_STORE, 0, 0);
  put(s, 69, K_STORE, 0, 1);
  spread(s, 72, 80, 22, 1);
  spread(s, 82, 89, 23, 2);
  put(s, 90, K_FLAG, 0, 0);
  spread(s, 91, 103, 25, 2);
  spread(s, 103, 111, 27, 2);
  put(s, 111, K_POLL, 1, 0);
  spread(s, 112, 121, 29, 2);
  for (int k = 0; k < 4; ++k) put(s, 122 + k, K_H, k >> 1, k & 1);
  put(s, 128, K_STORE, 1, 0);
  put(s, 128, K_STORE, 1, 1);
  put(s, 128, K_H, 2, 0);
  put(s, 128, K_H, 2, 1);
  put(s, Q(Y1, 0), K_FLAG, 1, 0);
  pair(s, 2, 0, K_H, 3); pair(s, 2, 0, K_H, 4);
  pair(s, 2, 3, K_DMA, 31);
  pair(s, 3, 1, K_H, 5); pair(s, 3, 3, K_H, 6);
  pair(s, 4, 1, K_DMA, 32); pair(s, 4, 3, K_DMA, 33);
  pair(s, 5, 1, K_DMA, 34); pair(s, 5, 3, K_DMA, 35);
  put(s, Q(6, 0), K_POLL, 2, 0);
  pair(s, 6, 1, K_DMA, 36); pair(s, 6, 3, K_DMA, 37);
  for (int k = 0; k < HB; ++k) pair(s, 7, 0, K_H, 7 + k);
  for (int k = 7 + HB; k < 14; ++k) pair(s, k - HB, 1, K_H, k);
  return s;
}
constexpr Sched SCH = make_sched();
constexpr int younger_than(int kind, int n, int i, int P) {
  int c = -1;
  for (int k = 0; k < SCH.cnt; ++k) {
    if (SCH.op[k].pos >= P) break;
    if (SCH.op[k].kind == kind && SCH.op[k].n == n && SCH.op[k].i == i) c = 0; else if (c >= 0) ++c;
  }
  return c;
}
constexpr int first_op(int P) { int k = 0; while (k < SCH.cnt && SCH.op[k].pos < P) ++k; return k; }
constexpr bool is_bar(int P) { return P < 64 ? (P & 7) == 2 : (P & 15) == 10; }
constexpr int bar_wait(int P) {
  if (P < 64) return younger_than(K_DMA, 2 * (P >> 3) + 3, 1, P);
  if (P == 74) return younger_than(K_DMA, 19, 1, P);
  if (P == 90) return younger_than(K_STORE, 0, 1, P);
  if (P == 106) return younger_than(K_DMA, 23, 1, P);
  if (P == 122) return younger_than(K_POLL, 1, 0, P);
  if (P == Q(Y1, 0)) return younger_than(K_STORE, 1, 1, P);
  if (P == Q(Y2, 0)) return younger_than(K_DMA, 31, 1, P);
  if (P == Q(FCHK, 0)) return younger_than(K_POLL, 2, 0, P);
  return younger_than(K_DMA, 37, 1, P);
}
constexpr bool sched_sorted() { for (int k = 1; k < SCH.cnt; ++k) if (SCH.op[k].pos < SCH.op[k - 1].pos) return false; return true; }
static_assert(sched_sorted(), "schedule order");

__global__ __launch_bounds__(512, 2) void k_fused(const void* __restrict__ x, const void* __restrict__ Wg,
                                                  const void* __restrict__ Wu, const void* __restrict__ Wd,
                                                  void* __restrict__ out, void* __restrict__ ws) {
  extern __shared__ __attribute__((aligned(16))) char smem[];
  const unsigned lds0 = (unsigned)(uintptr_t)(LDS_AS char*)smem;
  ST_DECL ST(0)
  const unsigned bid = blockIdx.x;
  const unsigned w = rfl(threadIdx.x >> 6);
  const bool busy = bid < 224;
#define CPF 0
  const unsigned mi = (bid & 6) * 2 + ((bid >> 3) & 3), half = bid & 1, idx = bid >> 5;
  const unsigned rot = ((bid & 6) + 4 * half) & 7;
  unsigned vw0, vw1, vx0, vx1;
  {
    const unsigned lane = threadIdx.x & 63, g = lane >> 4;
    vw0 = (lane >> 3) * 3584 + (((lane & 7) ^ ((g & 1) << 1)) << 4);
    vw1 = (vw0 ^ 64) + 8 * 3584 - 1024;
    vx0 = (lane >> 3) * 1024 + (((lane & 7) ^ g) << 4);
    vx1 = (vx0 ^ 64) + 8192 - 1024;
  }
  unsigned sx0 = (mi * 128 + 16 * w) * 1024;
  unsigned sw0 = (16 * (w & 3)) * 3584 + (half * 14 + idx) * 128;
  unsigned mu0 = lds0 + w * 2048;
  unsigned sw00 = sw0 + rot * (64 * 3584), sx00 = sx0 + rot * 128;
  asm volatile("" : "+v"(vw0), "+v"(vw1), "+v"(vx0), "+v"(vx1));
  asm volatile("" : "+s"(sx0), "+s"(sw0), "+s"(mu0), "+s"(sw00), "+s"(sx00));
  SB();
  const u32x4 srdX = make_srd(x, M_ * D_ * 2);
  const u32x4 srdW = make_srd((w >> 2) ? Wu : Wg, D_ * F_ * 2);
  auto issue_xw = [&](int n, int i) {
    const int s = n < 16 ? n >> 1 : n - 16;
    const unsigned sg = (s + rot) & 7;
    if (n < 16 && (n & 1) == 0) {
      const unsigned so = n == 0 ? sx00 : sx0 + sg * 128;
      if (i == 0) dma16<0>(mu0 + uaddr(n), vx0, srdX, so); else dma16n<1024>(vx1, srdX, so);
    } else {
      const unsigned so = n == 1 ? sw00 : sw0 + sg * (64 * 3584) + (n >= 16 ? 896 : 0);
      if (i == 0) dma16<0>(mu0 + uaddr(n), vw0, srdW, so); else dma16n<1024>(vw1, srdW, so);
    }
  };
  asm volatile("" ::"s"(Wd), "s"(out), "s"(ws));
  if (busy) {
    issue_xw(0, 0); issue_xw(0, 1); issue_xw(1, 0); issue_xw(1, 1);
    BAR();
    issue_xw(2, 0); issue_xw(2, 1); issue_xw(3, 0); issue_xw(3, 1);
  }
  SB();
  const unsigned tid = opq(threadIdx.x), lane = tid & 63;
  const unsigned bid2 = opqs(bid), xcd = bid2 & 7, ml = (bid2 >> 3) & 3;
  unsigned rank = half * 8 + idx;
  const unsigned m64 = 2 * mi + half, n64 = idx;
  const unsigned g = lane >> 4, q = (lane & 15) >> 2, p = lane & 3;
  const u32x4 srdH = make_srd(ws, SYNC_OFF + 16 * 8192);
  const u32x4 srdD = make_srd(Wd, F_ * D_ * 2);
  unsigned syncb = SYNC_OFF + mi * 8192;
  const unsigned lane16 = lane * 16;

  const unsigned kq = w >> 1, mb = w & 1;
  unsigned rot2 = 2 * ml + idx; rot2 = rot2 >= 7 ? rot2 - 7 : rot2;
  unsigned kb[7];
  auto set_kb = [&]() {
    unsigned e = opqs(rot2);
#pragma unroll
    for (int j = 0; j < 7; ++j) {
      kb[j] = opqs((e + 7 * kq + (kq >> 1) * 14) * 1024);
      e = e == 6 ? 0 : e + 1;
    }
  };
  auto kap = [&](int s) { return kb[s % 7] + (s >= 7 ? 14 * 1024 : 0); };
  const unsigned vd0 = (lane >> 3) * 1024 + (((lane & 7) ^ (g << 1)) << 4);
  const unsigned vd1 = vd0 + 8192 - 1024;
  unsigned sd0 = (16 * mb) * 1024 + n64 * 128;
  auto issue_wd = [&](int s, int i) {
    if (i == 0) dma16<0>(mu0 + uaddr(24 + s), vd0, srdD, sd0 + kap(s) * 32);
    else dma16n<1024>(vd1, srdD, sd0 + kap(s) * 32);
  };
  const unsigned dl = lds0 + kq * 4096 + (4 * g + q) * 128 + (((2 * g + (q >> 1)) & 3) << 5) + p * 8;
  unsigned db[3][4];
  auto set_db = [&](int k) {
#pragma unroll
    for (int c = 0; c < 4; ++c) db[k][c] = opq((dl ^ (c << 5)) + k * 4 * UNIT);
  };
  auto ldd = [&](int s, int a) {
    const int ua = uaddr(24 + s);
    const unsigned ad = db[ua >> 16][a] + (ua & 65535);
    return tr_frag(ad, ad + 2048);
  };
  u32x2 ep, tag, fl;
  auto mktag = [&]() {
    const uint64_t t = (((uint64_t)ep[1] << 32) | ep[0]) * 0x9E3779B97F4A7C15ull + 1ull;
    tag = u32x2{(unsigned)t, (unsigned)(t >> 32)};
  };
  const unsigned one = tid == 0 ? 0u : 0x40000000u;
  const unsigned fo = tid < 16 ? 128 * (1 + tid) : 0x40000000u;
  unsigned fmask = 0x7F7Fu;
  auto flags_ok = [&]() { return (rfl((unsigned)__builtin_amdgcn_ballot_w64(fl[0] == tag[0] && fl[1] == tag[1])) & fmask) == fmask; };
  auto poll = [&](unsigned off) {
    unsigned cnt = SPIN_CAP, t;
#define PLD(r) "buffer_load_dwordx2 " r ", %2, %3, %4 offen sc1\n\t"
#define PCK(r) "s_waitcnt vmcnt(2)\n\tv_cmp_eq_u64 vcc, " r ", %5\n\ts_nop 1\n\ts_and_b32 %1, vcc_lo, %6\n\ts_cmp_eq_u32 %1, %6\n\ts_cbranch_scc1 2f\n\t"
    asm volatile(PLD("v[250:251]") "s_sleep 3\n\t" PLD("v[252:253]") "s_sleep 3\n\t" PLD("v[254:255]")
                 "1:\n\t"
                 PCK("v[250:251]") PLD("v[250:251]") PCK("v[252:253]") PLD("v[252:253]") PCK("v[254:255]") PLD("v[254:255]")
                 "s_sub_u32 %0, %0, 1\n\ts_cbranch_scc0 1b\n\t"
                 "2:"
                 : "+s"(cnt), "=&s"(t) : "v"(fo), "s"(srdH), "s"(syncb + off), "v"(tag), "s"(fmask)
                 : "v250", "v251", "v252", "v253", "v254", "v255", "vcc", "scc", "memory");
#undef PLD
#undef PCK
  };
  auto poll_slow = [&](unsigned off) {
#pragma nounroll
    for (unsigned spin = 0; spin < SPIN_CAP; ++spin) {
      gload8_sc1(fl, fo, srdH, syncb + off);
      asm volatile("s_waitcnt vmcnt(0)" : "+v"(fl)::"memory");
      if (flags_ok()) break;
      __builtin_amdgcn_s_sleep(8);
    }
  };
  bf16x8 df[2][4];
  u32x4 hf[14][2];
  unsigned sh2 = ((m64 * 4 + mb * 2) * 56) * 1024;
  const unsigned lane16b = lane16 + 56 * 1024;
  auto issue_h = [&](int s, int b) { gload16_sc1(hf[s][b], b ? lane16b : lane16, srdH, sh2 + kap(s)); };

  set_kb();
  if (__builtin_expect(busy, 1)) {
    gload8_sc1(ep, 0u, srdH, syncb);
    const unsigned wm = w & 3, wn = w >> 2;
    const unsigned xrow = 32 * wm + (lane & 15);
    const unsigned xa0 = lds0 + xrow * 128 + ((g ^ ((xrow >> 1) & 7)) << 4);
    const unsigned wa0 = lds0 + (8 * g + q) * 128 + (((2 * wn) ^ ((q >> 1) | ((g & 1) << 1))) << 5) + p * 8;
    unsigned xa[3][2], wa[3][2];
#pragma unroll
    for (int k = 0; k < 3; ++k)
#pragma unroll
      for (int c = 0; c < 2; ++c) { xa[k][c] = opq((xa0 ^ (c << 6)) + k * 4 * UNIT); wa[k][c] = opq((wa0 ^ (c << 5)) + k * 4 * UNIT); }
    ST(1)

    f32x4 acc[2][2][4];
    bf16x8 xf[8][2][2];
    bf16x8 wf[4][2];
    auto ldx = [&](int s, int b, int kk) { const int ua = uaddr(2 * s); xf[s][b][kk] = lds_read16(xa[ua >> 16][kk] + (ua & 65535) + b * 2048); };
    auto ldw = [&](int G, int kk) {
      const int st = G >> 2, a = G & 3, ua = uaddr(st < 8 ? 2 * st + 1 : 8 + st);
      const unsigned ad = wa[ua >> 16][a & 1] + (ua & 65535) + (a >> 1) * 8192 + kk * 4096;
      wf[G & 3][kk] = tr_frag(ad, ad + 512);
    };
    const unsigned sh = ((mi * 8 + wm * 2) * 56 + half * 28 + idx * 2 + wn) * 1024;
    u32x4 ho[2];
    auto epi = [&](int ps, int b, int c) {
      const int t = c >> 1, i = c & 1;
      float g0 = __builtin_elementwise_maximum(acc[ps][b][t][2 * i], 0.f), g1 = __builtin_elementwise_maximum(acc[ps][b][t][2 * i + 1], 0.f);
      float p0 = g0 * acc[ps][b][2 + t][2 * i];
      asm volatile("" : "+v"(p0));
      ho[b][c] = pack_bf16(p0, g1 * acc[ps][b][2 + t][2 * i + 1]);
    };
    auto sth = [&](int ps, int b) { gstore16_sc1(ho[b], lane16, srdH, sh + b * (56 * 1024) + ps * (14 * 1024)); };

    auto vmem = [&](auto PP) INL {
      sfor<first_op(PP.v), first_op(PP.v + 1)>([&](auto K) INL {
        constexpr Op o = SCH.op[K.v];
        if constexpr (o.pos == PP.v) {
          if constexpr (o.kind == K_DMA) { if constexpr (o.n < 24) issue_xw(o.n, o.i); else issue_wd(o.n - 24, o.i); }
          if constexpr (o.kind == K_STORE) sth(o.n, o.i);
          if constexpr (o.kind == K_FLAG) gstore8_sc1(tag, one, srdH, syncb + 128 * (1 + rank));
          if constexpr (o.kind == K_POLL) gload8_sc1(fl, fo, srdH, syncb);
          if constexpr (o.kind == K_H) issue_h(o.n, o.i);
        }
      });
    };
    wait_vm<5>();
    ST(2)
    BAR();
    ldw(0, 0); ldx(0, 0, 0); ldw(0, 1); ldx(0, 0, 1); ldx(0, 1, 0); ldx(0, 1, 1); ldw(1, 0); ldw(1, 1); ldw(2, 0); ldw(2, 1);
    SB();
    sfor<0, 128>([&](auto Z) INL {
      constexpr int P = Z.v, ps = P >> 6, s = (P >> 3) & 7, G = P >> 1, a = G & 3, b = P & 1;
      if constexpr (is_bar(P)) {
        constexpr int n = bar_wait(P);
        static_assert(n >= 0 && n < 24, "wait count");
        if constexpr (P == 10) asm volatile("s_waitcnt vmcnt(%1)" : "+v"(ep) : "i"(n) : "memory");
        else if constexpr (P == 122) {
          asm volatile("s_waitcnt vmcnt(%1)" : "+v"(fl) : "i"(n) : "memory");
          if (w == 0) { if (__builtin_expect(!flags_ok(), 0)) poll(0); }
        } else wait_vm<n>();
        BAR();
        if (P == 2) { ST(3) } if (P == 26) { ST(4) } if (P == 58) { ST(5) } if (P == 74) { ST(6) } if (P == 90) { ST(7) } if (P == 106) { ST(8) } if (P == 122) { ST(9) }
      }
      if constexpr (P < 58 ? (P & 1) == 0 : (P & 3) == 2) __builtin_amdgcn_s_setprio(
P < 58 ? 3 - (((P + 6) & 7) >> 1) : 3 - (((P + 6) & 15) >> 2));
      if constexpr (s == 0) mfma2_new(acc[ps][b][a], wf[G & 3][0], xf[s][b][0], wf[G & 3][1], xf[s][b][1]);
      else mfma2_acc(acc[ps][b][a], wf[G & 3][0], xf[s][b][0], wf[G & 3][1], xf[s][b][1]);
      SB();
      if constexpr (G + 3 < 64) ldw(G + 3, b);
      if constexpr (P < 56 && (P & 7) >= 2 && (P & 7) < 6) ldx(s + 1, ((P - 2) >> 1) & 1, P & 1);
      if constexpr (P == 120) set_db(1);
      if constexpr (P >= 124) df[0][P - 124] = ldd(0, P - 124);
      if constexpr (P == 65) { epi(0, 0, 0); epi(0, 0, 1); }
      if constexpr (P == 66) { epi(0, 1, 0); epi(0, 1, 1); }
      if constexpr (P == 67) { epi(0, 0, 2); epi(0, 0, 3); }
      if constexpr (P == 68 || P == 69) epi(0, 1, P - 66);
      if constexpr (P == 84) mktag();
      vmem(Z);
      SB();
    });
    ST(11)
    set_db(0); set_db(2);
    epi(1, 0, 0); epi(1, 0, 1); epi(1, 1, 0); epi(1, 1, 1);
    SB();
    epi(1, 0, 2); epi(1, 0, 3); epi(1, 1, 2); epi(1, 1, 3);
    vmem(IC<128>{});
    __builtin_amdgcn_s_setprio(0);
    ST(12)
  } else {
    gload8_sc1(ep, 0u, srdH, syncb);
    set_db(0); set_db(1); set_db(2);
    {
      unsigned t = 0;
#define NPF 2
#define PFX 2
#define PFB 1
      {
        const unsigned st = (rot + 2 + (lane & 3)) & 7;
        const unsigned vxo = (lane & 3) < PFX ? (mi * 128 + w * 16 + (lane >> 2)) * 1024 + st * 128 : 0x40000000u;
        asm volatile("buffer_load_dword %0, %1, %2, 0 offen" : "+v"(t) : "v"(vxo), "s"(srdX) : "memory");
      }
      {
        const unsigned li = (ml * 4 + (w & 3)) * 56 + lane;
        const unsigned row = li / 7, c = li - row * 7;
        const unsigned vo = lane < 56 ? (rot * 64 + row) * 3584 + (half * 14 + 7 + c) * 128 : 0x40000000u;
        asm volatile("buffer_load_dword %0, %1, %2, 0 offen" : "+v"(t) : "v"(vo), "s"(srdW) : "memory");
      }
#pragma unroll
      for (int i = 0; i < NPF; ++i) {
        unsigned c = i + xcd; c = c >= 7 ? c - 7 : c; c = c >= 7 ? c - 7 : c;
        asm volatile("buffer_load_dword %0, %1, %2, %3 offen" : "+v"(t) : "v"(lane * 128), "s"(srdD), "s"(((c * 4 + ml) * 8 + w) * 8192) : "memory");
      }
      asm volatile("s_waitcnt vmcnt(0)" : "+v"(t), "+v"(ep)::"memory");
      mktag();
    }
#pragma unroll
    for (int s = 0; s < 7; ++s) { issue_wd(s, 0); issue_wd(s, 1); }
    if (w == 0) poll_slow(0); else wait_vm<0>();
    BAR();
#pragma unroll
    for (int s = 0; s < 3; ++s) { issue_h(s, 0); issue_h(s, 1); }
#pragma unroll
    for (int a = 0; a < 4; ++a) df[0][a] = ldd(0, a);
    wait_vm<0>();
  }
  asm volatile("" : "+s"(mu0), "+s"(sd0), "+s"(sh2), "+s"(syncb), "+s"(rank));
  asm volatile("" : "+s"(kb[0]), "+s"(kb[1]), "+s"(kb[2]), "+s"(kb[3]), "+s"(kb[4]), "+s"(kb[5]), "+s"(kb[6]));
  fmask = rank == 7 ? 0xFFFFu : 0x7F7Fu;
  f32x4 acc2[2][4];
  auto vmem2 = [&](auto PP) INL {
    sfor<first_op(PP.v), first_op(PP.v + 1)>([&](auto K) INL {
      constexpr Op o = SCH.op[K.v];
      if constexpr (o.pos == PP.v) {
        if constexpr (o.kind == K_DMA) issue_wd(o.n - 24, o.i);
        if constexpr (o.kind == K_FLAG) gstore8_sc1(tag, one, srdH, syncb + 128 * (17 + rank));
        if constexpr (o.kind == K_POLL) gload8_sc1(fl, fo, srdH, syncb + 2048);
        if constexpr (o.kind == K_H) issue_h(o.n, o.i);
      }
    });
  };
  sfor<0, 14>([&](auto S) INL {
    constexpr int s = S.v;
    if constexpr (s == Y1 || s == FCHK || s == Y2 || s == Y3) {
      constexpr int n = bar_wait(Q(s, 0));
      static_assert(n >= 0 && n < 24, "wait count");
      if constexpr (s == FCHK) {
        asm volatile("s_waitcnt vmcnt(%1)" : "+v"(fl) : "i"(n) : "memory");
        if (w == 0) { if (__builtin_expect(!flags_ok(), 0)) poll(2048); }
      } else wait_vm<n>();
      BAR();
      if (s == Y1) { ST(13) } if (s == FCHK) { ST(14) } if (s == Y2) { ST(15) } if (s == Y3) { ST(16) }
      if constexpr (s == Y3) { if (rank == 7 && w == 0) gstore8_sc1(tag, one, srdH, syncb); }
    }
    vmem2(IC<Q(s, 0)>{});
    asm volatile("s_waitcnt vmcnt(%2)" : "+v"(hf[s][0]), "+v"(hf[s][1]) : "i"(younger_than(K_H, s, 1, Q(s, 1))) : "memory");
    if (s == 0) { ST(17) }
    SB();
    sfor<0, 4>([&](auto A) INL {
      constexpr int a = A.v;
      if constexpr (s == 0) { mfma_new(acc2[0][a], df[0][a], hf[0][0]); mfma_new(acc2[1][a], df[0][a], hf[0][1]); }
      else { mfma_acc(acc2[0][a], df[s & 1][a], hf[s][0]); mfma_acc(acc2[1][a], df[s & 1][a], hf[s][1]); }
      SB();
      vmem2(IC<Q(s, a + 1)>{});
      if constexpr (s + 1 < 14) df[(s + 1) & 1][a] = ldd(s + 1, a);
      SB();
    });
  });
  ST(18)
  asm volatile("s_nop 7" ::: "memory");
  SB();
  {
    const unsigned pa = lds0 + (kq == 3 ? 8 * UNIT : kq * (64 * 272)) + (mb * 32 + (lane & 15)) * 272 + g * 16;
#pragma unroll
    for (int a = 0; a < 4; ++a)
#pragma unroll
      for (int b = 0; b < 2; ++b) *(LDS_AS f32x4*)(uintptr_t)(pa + b * (16 * 272) + a * 64) = acc2[b][a];
  }
  wait_lgkm0();
  BAR();
  ST(19)
  {
    const unsigned row = w * 8 + (lane >> 3);
    const unsigned ra = lds0 + row * 272 + (lane & 7) * 32;
    f32x4 s0 = *(LDS_AS f32x4*)(uintptr_t)(ra), s1 = *(LDS_AS f32x4*)(uintptr_t)(ra + 16);
#pragma unroll
    for (int v = 1; v < 4; ++v) {
      const unsigned o = v == 3 ? 8 * UNIT : v * (64 * 272);
      s0 += *(LDS_AS f32x4*)(uintptr_t)(ra + o);
      s1 += *(LDS_AS f32x4*)(uintptr_t)(ra + o + 16);
    }
    u32x4 o = {pack_bf16(s0[0], s0[1]), pack_bf16(s0[2], s0[3]), pack_bf16(s1[0], s1[1]), pack_bf16(s1[2], s1[3])};
    *(u32x4*)((char*)out + (size_t)(m64 * 64 + row) * 1024 + n64 * 128 + (lane & 7) * 16) = o;
  }
  ST(20)
}

extern "C" void kernel_launch(void* const* d_in, const int* in_sizes, int n_in, void* d_out, int out_size,
                              void* d_ws, size_t ws_size, hipStream_t stream) {
  (void)in_sizes; (void)n_in; (void)out_size; (void)ws_size;
  (void)hipFuncSetAttribute((const void*)k_fused, hipFuncAttributeMaxDynamicSharedMemorySize, LDS_TOTAL);
  k_fused<<<256, 512, LDS_TOTAL, stream>>>(d_in[0], d_in[1], d_in[2], d_in[3], d_out, d_ws);
}
```

```cpp
#include <hip/hip_runtime.h>
#include <hip/hip_bf16.h>
#include <stdint.h>

typedef __attribute__((ext_vector_type(8))) __bf16 bf16x8;
typedef __attribute__((ext_vector_type(4))) short s16x4;
typedef __attribute__((ext_vector_type(8))) short s16x8;
typedef __attribute__((ext_vector_type(2))) __bf16 bf16x2;
typedef __attribute__((ext_vector_type(2))) float f32x2;
typedef __attribute__((ext_vector_type(4))) float f32x4;
typedef __attribute__((ext_vector_type(4))) unsigned u32x4;
typedef __attribute__((ext_vector_type(2))) unsigned u32x2;
#define LDS_AS __attribute__((address_space(3)))
#define ST_DECL
#define ST(i)
#define ST_FLUSH(a,b)

constexpr int M_ = 2048, D_ = 512, F_ = 1792;
constexpr unsigned SYNC_OFF = 8u << 20;
constexpr unsigned SPIN_CAP = 1u << 17;

__device__ __forceinline__ unsigned rfl(unsigned v) { return __builtin_amdgcn_readfirstlane(v); }

__device__ __forceinline__ u32x4 make_srd(const void* p, unsigned bytes) {
  uint64_t a = (uint64_t)p;
  u32x4 r;
  r[0] = rfl((unsigned)a);
  r[1] = rfl((unsigned)(a >> 32)) & 0xffffu;
  r[2] = bytes;
  r[3] = 0x00020000u;
  return r;
}

template <int IMM>
__device__ __forceinline__ void dma16(unsigned m0v, unsigned voff, u32x4 srd, unsigned soff) {
  asm volatile("s_mov_b32 m0, %0\n\ts_nop 0\n\tbuffer_load_dwordx4 %1, %2, %3 offen offset:%4 lds"
               ::"s"(m0v), "v"(voff), "s"(srd), "s"(soff), "i"(IMM) : "memory");
}
template <int IMM>
__device__ __forceinline__ void dma16n(unsigned voff, u32x4 srd, unsigned soff) {
  asm volatile("buffer_load_dwordx4 %0, %1, %2 offen offset:%3 lds" ::"v"(voff), "s"(srd), "s"(soff), "i"(IMM) : "memory");
}
__device__ __forceinline__ void set_m0(unsigned v) { asm volatile("s_mov_b32 m0, %0" ::"s"(v) : "memory"); }
__device__ __forceinline__ unsigned opq(unsigned v) { asm volatile("" : "+v"(v)); return v; }
__device__ __forceinline__ unsigned opqs(unsigned v) { asm volatile("" : "+s"(v)); return v; }
__device__ __forceinline__ void gload16_sc1(u32x4& dst, unsigned voff, u32x4 srd, unsigned soff) {
  asm volatile("buffer_load_dwordx4 %0, %1, %2, %3 offen sc1" : "=v"(dst) : "v"(voff), "s"(srd), "s"(soff) : "memory");
}
__device__ __forceinline__ void greload16_sc1(u32x4& dst, unsigned voff, u32x4 srd, unsigned soff) {
  asm volatile("buffer_load_dwordx4 %0, %1, %2, %3 offen sc1" : "+v"(dst) : "v"(voff), "s"(srd), "s"(soff) : "memory");
}
__device__ __forceinline__ void gstore16_sc1(u32x4 v, unsigned voff, u32x4 srd, unsigned soff) {
  asm volatile("buffer_store_dwordx4 %0, %1, %2, %3 offen sc1\n\ts_nop 1" ::"v"(v), "v"(voff), "s"(srd), "s"(soff) : "memory");
}
__device__ __forceinline__ void gload8_sc1(u32x2& dst, unsigned voff, u32x4 srd, unsigned soff) {
  asm volatile("buffer_load_dwordx2 %0, %1, %2, %3 offen sc1" : "=v"(dst) : "v"(voff), "s"(srd), "s"(soff) : "memory");
}
__device__ __forceinline__ void gstore8_sc1(u32x2 v, unsigned voff, u32x4 srd, unsigned soff) {
  asm volatile("buffer_store_dwordx2 %0, %1, %2, %3 offen sc1" ::"v"(v), "v"(voff), "s"(srd), "s"(soff) : "memory");
}

__device__ __forceinline__ s16x4 tr_read(unsigned addr) {
  return __builtin_amdgcn_ds_read_tr16_b64_v4i16((LDS_AS s16x4*)(uintptr_t)addr);
}
__device__ __forceinline__ bf16x8 tr_frag(unsigned a0, unsigned a1) {
  s16x4 lo = tr_read(a0), hi = tr_read(a1);
  s16x8 v = __builtin_shufflevector(lo, hi, 0, 1, 2, 3, 4, 5, 6, 7);
  return __builtin_bit_cast(bf16x8, v);
}
struct Frag { s16x4 lo, hi; };
__device__ __forceinline__ bf16x8 join(const Frag& f) {
  s16x8 v = __builtin_shufflevector(f.lo, f.hi, 0, 1, 2, 3, 4, 5, 6, 7);
  return __builtin_bit_cast(bf16x8, v);
}
__device__ __forceinline__ bf16x8 lds_read16(unsigned addr) { return *(LDS_AS bf16x8*)(uintptr_t)addr; }
__device__ __forceinline__ unsigned pack_bf16(float a, float b) {
  f32x2 v = {a, b};
  bf16x2 r = __builtin_convertvector(v, bf16x2);
  return __builtin_bit_cast(unsigned, r);
}
__device__ __forceinline__ float rnd_bf16(float a) { return __builtin_bit_cast(float, pack_bf16(0.f, a)); }

template <class TB>
__device__ __forceinline__ void mfma_acc(f32x4& c, bf16x8 a, TB b) {
  asm volatile("v_mfma_f32_16x16x32_bf16 %0, %1, %2, %0" : "+v"(c) : "v"(a), "v"(b));
}
template <class TB>
__device__ __forceinline__ void mfma_new(f32x4& c, bf16x8 a, TB b) {
  asm volatile("v_mfma_f32_16x16x32_bf16 %0, %1, %2, 0" : "=&v"(c) : "v"(a), "v"(b));
}

__device__ __forceinline__ void mfma2_acc(f32x4& c, bf16x8 a0, bf16x8 b0, bf16x8 a1, bf16x8 b1) {
  asm volatile("v_mfma_f32_16x16x32_bf16 %0, %1, %2, %0\n\tv_mfma_f32_16x16x32_bf16 %0, %3, %4, %0" : "+v"(c) : "v"(a0), "v"(b0), "v"(a1), "v"(b1));
}
__device__ __forceinline__ void mfma2_new(f32x4& c, bf16x8 a0, bf16x8 b0, bf16x8 a1, bf16x8 b1) {
  asm volatile("v_mfma_f32_16x16x32_bf16 %0, %1, %2, 0\n\tv_mfma_f32_16x16x32_bf16 %0, %3, %4, %0" : "=&v"(c) : "v"(a0), "v"(b0), "v"(a1), "v"(b1));
}

template <int I> struct IC { static constexpr int v = I; };
template <int I, int N, class F> __device__ __forceinline__ void sfor(F&& f) {
  if constexpr (I < N) { f(IC<I>{}); sfor<I + 1, N>(f); }
}
template <int N> __device__ __forceinline__ void wait_vm() { asm volatile("s_waitcnt vmcnt(%0)" ::"i"(N) : "memory"); }
template <int N> __device__ __forceinline__ void wait_vm_lgkm0() { asm volatile("s_waitcnt vmcnt(%0) lgkmcnt(0)" ::"i"(N) : "memory"); }
__device__ __forceinline__ void wait_lgkm0() { asm volatile("s_waitcnt lgkmcnt(0)" ::: "memory"); }
#define BAR() __builtin_amdgcn_s_barrier()
#define SB() __builtin_amdgcn_sched_barrier(0)
#define INL __attribute__((always_inline))

constexpr int UNIT = 16384, LDS_TOTAL = 10 * UNIT;
constexpr int uaddr(int n) { return (n % 10) * UNIT; }

enum { K_DMA = 1, K_STORE, K_FLAG, K_POLL, K_H };
struct Op { int pos, kind, n, i; };
struct Sched { int cnt; Op op[200]; };
constexpr void put(Sched& s, int pos, int kind, int n, int i) { s.op[s.cnt++] = Op{pos, kind, n, i}; }
constexpr void spread(Sched& s, int p0, int p1, int u0, int nu) {
  for (int k = 0; k < 2 * nu; ++k) put(s, p0 + (k * (p1 - p0)) / (2 * nu), K_DMA, u0 + (k >> 1), k & 1);
}
constexpr int Q(int step, int slot) { return 200 + 8 * step + slot; }
constexpr void pair(Sched& s, int step, int slot, int kind, int n) { put(s, Q(step, slot), kind, n, 0); put(s, Q(step, slot == 0 ? 0 : slot + 1), kind, n, 1); }
#define LEAD 1
#define HB 3
constexpr int Y1 = 2, Y2 = 5, FCHK = 7, Y3 = 10;
constexpr Sched make_sched() {
  Sched s{};
  for (int n = 0; n < 4; ++n) { put(s, -1, K_DMA, n, 0); put(s, -1, K_DMA, n, 1); }
  put(s, -1, K_POLL, 0, 0);
  for (int n = 4; n < 6; ++n) { put(s, -1, K_DMA, n, 0); put(s, -1, K_DMA, n, 1); }
  spread(s, 0, 2, 6, 1);
  for (int t = 0; t < 7; ++t) spread(s, 8 * t + 2, 8 * t + 9, 2 * t + 7, 2);
  spread(s, 58, 66, 21, 2);
  put(s, 67, K_STORE, 0, 0);
  put(s, 69, K_STORE, 0, 1);
  spread(s, 78, 89, 23, 2);
  put(s, 90, K_FLAG, 0, 0);
  spread(s, 91, 103, 25, 2);
  spread(s, 103, 111, 27, 2);
  put(s, 111, K_POLL, 1, 0);
  spread(s, 112, 121, 29, 2);
  for (int k = 0; k < 4; ++k) put(s, 122 + k, K_H, k >> 1, k & 1);
  put(s, 128, K_STORE, 1, 0);
  put(s, 128, K_STORE, 1, 1);
  put(s, 128, K_H, 2, 0);
  put(s, 128, K_H, 2, 1);
  put(s, Q(Y1, 0), K_FLAG, 1, 0);
  pair(s, 2, 0, K_H, 3); pair(s, 2, 0, K_H, 4);
  pair(s, 2, 3, K_DMA, 31);
  pair(s, 3, 1, K_H, 5); pair(s, 3, 3, K_H, 6);
  pair(s, 4, 1, K_DMA, 32); pair(s, 4, 3, K_DMA, 33);
  pair(s, 5, 1, K_DMA, 34); pair(s, 5, 3, K_DMA, 35);
  put(s, Q(6, 0), K_POLL, 2, 0);
  pair(s, 6, 1, K_DMA, 36); pair(s, 6, 3, K_DMA, 37);
  for (int k = 0; k < HB; ++k) pair(s, 7, 0, K_H, 7 + k);
  for (int k = 7 + HB; k < 14; ++k) pair(s, k - HB, 1, K_H, k);
  return s;
}
constexpr Sched SCH = make_sched();
constexpr int younger_than(int kind, int n, int i, int P) {
  int c = -1;
  for (int k = 0; k < SCH.cnt; ++k) {
    if (SCH.op[k].pos >= P) break;
    if (SCH.op[k].kind == kind && SCH.op[k].n == n && SCH.op[k].i == i) c = 0; else if (c >= 0) ++c;
  }
  return c;
}
constexpr int first_op(int P) { int k = 0; while (k < SCH.cnt && SCH.op[k].pos < P) ++k; return k; }
constexpr bool is_bar(int P) { return P < 64 ? (P & 7) == 2 : (P & 15) == 10; }
constexpr int bar_wait(int P) {
  if (P < 64) return younger_than(K_DMA, 2 * (P >> 3) + 3, 1, P);
  if (P == 74) return younger_than(K_DMA, 19, 1, P);
  if (P == 90) return younger_than(K_STORE, 0, 1, P);
  if (P == 106) return younger_than(K_DMA, 23, 1, P);
  if (P == 122) return younger_than(K_POLL, 1, 0, P);
  if (P == Q(Y1, 0)) return younger_than(K_STORE, 1, 1, P);
  if (P == Q(Y2, 0)) return younger_than(K_DMA, 31, 1, P);
  if (P == Q(FCHK, 0)) return younger_than(K_POLL, 2, 0, P);
  return younger_than(K_DMA, 37, 1, P);
}
constexpr bool sched_sorted() { for (int k = 1; k < SCH.cnt; ++k) if (SCH.op[k].pos < SCH.op[k - 1].pos) return false; return true; }
static_assert(sched_sorted(), "schedule order");

__global__ __launch_bounds__(512, 2) void k_fused(const void* __restrict__ x, const void* __restrict__ Wg,
                                                  const void* __restrict__ Wu, const void* __restrict__ Wd,
                                                  void* __restrict__ out, void* __restrict__ ws) {
  extern __shared__ __attribute__((aligned(16))) char smem[];
  const unsigned lds0 = (unsigned)(uintptr_t)(LDS_AS char*)smem;
  ST_DECL ST(0)
  const unsigned bid = blockIdx.x;
  const unsigned w = rfl(threadIdx.x >> 6);
  const bool busy = bid < 224;
#define CPF 18000
  const unsigned mi = (bid & 6) * 2 + ((bid >> 3) & 3), half = bid & 1, idx = bid >> 5;
  const unsigned rot = ((bid & 6) + 4 * half) & 7;
  unsigned vw0, vw1, vx0, vx1;
  {
    const unsigned lane = threadIdx.x & 63, g = lane >> 4;
    vw0 = (lane >> 3) * 3584 + (((lane & 7) ^ ((g & 1) << 1)) << 4);
    vw1 = (vw0 ^ 64) + 8 * 3584 - 1024;
    vx0 = (lane >> 3) * 1024 + (((lane & 7) ^ g) << 4);
    vx1 = (vx0 ^ 64) + 8192 - 1024;
  }
  unsigned sx0 = (mi * 128 + 16 * w) * 1024;
  unsigned sw0 = (16 * (w & 3)) * 3584 + (half * 14 + idx) * 128;
  unsigned mu0 = lds0 + w * 2048;
  unsigned sw00 = sw0 + rot * (64 * 3584), sx00 = sx0 + rot * 128;
  asm volatile("" : "+v"(vw0), "+v"(vw1), "+v"(vx0), "+v"(vx1));
  asm volatile("" : "+s"(sx0), "+s"(sw0), "+s"(mu0), "+s"(sw00), "+s"(sx00));
  SB();
  const u32x4 srdX = make_srd(x, M_ * D_ * 2);
  const u32x4 srdW = make_srd((w >> 2) ? Wu : Wg, D_ * F_ * 2);
  auto issue_xw = [&](int n, int i, bool pre = false) {
    const int s = n < 16 ? n >> 1 : n - 16;
    const unsigned sg = (s + rot) & 7;
    if (n < 16 && (n & 1) == 0) {
      const unsigned so = n == 0 ? sx00 : sx0 + sg * 128;
      if (i == 0) { if (pre) dma16n<0>(vx0, srdX, so); else dma16<0>(mu0 + uaddr(n), vx0, srdX, so); } else dma16n<1024>(vx1, srdX, so);
    } else {
      const unsigned so = n == 1 ? sw00 : sw0 + sg * (64 * 3584) + (n >= 16 ? 896 : 0);
      if (i == 0) { if (pre) dma16n<0>(vw0, srdW, so); else dma16<0>(mu0 + uaddr(n), vw0, srdW, so); } else dma16n<1024>(vw1, srdW, so);
    }
  };
  asm volatile("" ::"s"(Wd), "s"(out), "s"(ws));
  if (busy) {
    issue_xw(0, 0); issue_xw(0, 1); issue_xw(1, 0); issue_xw(1, 1);
    BAR();
    issue_xw(2, 0); issue_xw(2, 1); issue_xw(3, 0); issue_xw(3, 1);
  } else {
    uint64_t pc;
    asm volatile("s_getpc_b64 %0" : "=s"(pc));
    const unsigned o = min(threadIdx.x * 64u, (unsigned)CPF);
    asm volatile("global_load_dword v249, %0, %1" ::"v"(o), "s"(pc) : "v249", "memory");
  }
  SB();
  const unsigned tid = opq(threadIdx.x), lane = tid & 63;
  const unsigned bid2 = opqs(bid), xcd = bid2 & 7, ml = (bid2 >> 3) & 3;
  unsigned rank = half * 8 + idx;
  const unsigned m64 = 2 * mi + half, n64 = idx;
  const unsigned g = lane >> 4, q = (lane & 15) >> 2, p = lane & 3;
  const u32x4 srdH = make_srd(ws, SYNC_OFF + 16 * 8192);
  const u32x4 srdD = make_srd(Wd, F_ * D_ * 2);
  unsigned syncb = SYNC_OFF + mi * 8192;
  const unsigned lane16 = lane * 16;

  const unsigned kq = w >> 1, mb = w & 1;
  unsigned rot2 = 2 * ml + idx; rot2 = rot2 >= 7 ? rot2 - 7 : rot2;
  unsigned kb[7];
  auto set_kb = [&]() {
    unsigned e = opqs(rot2);
#pragma unroll
    for (int j = 0; j < 7; ++j) {
      kb[j] = opqs((e + 7 * kq + (kq >> 1) * 14) * 1024);
      e = e == 6 ? 0 : e + 1;
    }
  };
  auto kap = [&](int s) { return kb[s % 7] + (s >= 7 ? 14 * 1024 : 0); };
  const unsigned vd0 = (lane >> 3) * 1024 + (((lane & 7) ^ (g << 1)) << 4);
  const unsigned vd1 = vd0 + 8192 - 1024;
  unsigned sd0 = (16 * mb) * 1024 + n64 * 128;
  auto issue_wd = [&](int s, int i, bool pre = false) {
    if (i == 0) { if (pre) dma16n<0>(vd0, srdD, sd0 + kap(s) * 32); else dma16<0>(mu0 + uaddr(24 + s), vd0, srdD, sd0 + kap(s) * 32); }
    else dma16n<1024>(vd1, srdD, sd0 + kap(s) * 32);
  };
  const unsigned dl = lds0 + kq * 4096 + (4 * g + q) * 128 + (((2 * g + (q >> 1)) & 3) << 5) + p * 8;
  unsigned db[3][4];
  auto set_db = [&](int k) {
#pragma unroll
    for (int c = 0; c < 4; ++c) db[k][c] = opq((dl ^ (c << 5)) + k * 4 * UNIT);
  };
  auto ldd = [&](int s, int a, int u) {
    const int ua = uaddr(24 + s);
    return tr_read(db[ua >> 16][a] + (ua & 65535) + u * 2048);
  };
  u32x2 ep, tag, fl;
  auto mktag = [&]() {
    const uint64_t t = (((uint64_t)ep[1] << 32) | ep[0]) * 0x9E3779B97F4A7C15ull + 1ull;
    tag = u32x2{(unsigned)t, (unsigned)(t >> 32)};
  };
  const unsigned one = tid == 0 ? 0u : 0x40000000u;
  const unsigned fo = tid < 16 ? 128 * (1 + tid) : 0x40000000u;
#define FM0 0x7F7Fu
  unsigned fmask = busy ? FM0 : 0x7F7Fu;
  auto flags_ok = [&]() { return (rfl((unsigned)__builtin_amdgcn_ballot_w64(fl[0] == tag[0] && fl[1] == tag[1])) & fmask) == fmask; };
  auto poll = [&](unsigned off) {
    unsigned cnt = SPIN_CAP, t;
#define PLD(r) "buffer_load_dwordx2 " r ", %2, %3, %4 offen sc1\n\t"
#define PCK(r) "s_waitcnt vmcnt(2)\n\tv_cmp_eq_u64 vcc, " r ", %5\n\ts_nop 1\n\ts_and_b32 %1, vcc_lo, %6\n\ts_cmp_eq_u32 %1, %6\n\ts_cbranch_scc1 2f\n\t"
    asm volatile(PLD("v[250:251]") "s_sleep 3\n\t" PLD("v[252:253]") "s_sleep 3\n\t" PLD("v[254:255]")
                 "1:\n\t"
                 PCK("v[250:251]") PLD("v[250:251]") PCK("v[252:253]") PLD("v[252:253]") PCK("v[254:255]") PLD("v[254:255]")
                 "s_sub_u32 %0, %0, 1\n\ts_cbranch_scc0 1b\n\t"
                 "2:"
                 : "+s"(cnt), "=&s"(t) : "v"(fo), "s"(srdH), "s"(syncb + off), "v"(tag), "s"(fmask)
                 : "v250", "v251", "v252", "v253", "v254", "v255", "vcc", "scc", "memory");
#undef PLD
#undef PCK
  };
  auto poll_slow = [&](unsigned off) {
#pragma nounroll
    for (unsigned spin = 0; spin < SPIN_CAP; ++spin) {
      gload8_sc1(fl, fo, srdH, syncb + off);
      asm volatile("s_waitcnt vmcnt(0)" : "+v"(fl)::"memory");
      if (flags_ok()) break;
      __builtin_amdgcn_s_sleep(8);
    }
  };
  Frag df[2][4];
  u32x4 hf[14][2];
  unsigned sh2 = ((m64 * 4 + mb * 2) * 56) * 1024;
  const unsigned lane16b = lane16 + 56 * 1024;
  auto issue_h = [&](int s, int b) { gload16_sc1(hf[s][b], b ? lane16b : lane16, srdH, sh2 + kap(s)); };

  set_kb();
  if (__builtin_expect(busy, 1)) {
    gload8_sc1(ep, 0u, srdH, syncb);
    const unsigned wm = w & 3, wn = w >> 2;
    const unsigned xrow = 32 * wm + (lane & 15);
    const unsigned xa0 = lds0 + xrow * 128 + ((g ^ ((xrow >> 1) & 7)) << 4);
    const unsigned wa0 = lds0 + (8 * g + q) * 128 + (((2 * wn) ^ ((q >> 1) | ((g & 1) << 1))) << 5) + p * 8;
    unsigned xa[3][2], wa[3][2];
#pragma unroll
    for (int k = 0; k < 3; ++k)
#pragma unroll
      for (int c = 0; c < 2; ++c) { xa[k][c] = opq((xa0 ^ (c << 6)) + k * 4 * UNIT); wa[k][c] = opq((wa0 ^ (c << 5)) + k * 4 * UNIT); }
    ST(1)

    f32x4 acc[2][2][4];
    bf16x8 xf[8][2][2];
    Frag wf[4][2];
    auto ldx = [&](int s, int b, int kk) { const int ua = uaddr(2 * s); xf[s][b][kk] = lds_read16(xa[ua >> 16][kk] + (ua & 65535) + b * 2048); };
    auto ldw = [&](int G, int kk, int u = 2) {
      const int st = G >> 2, a = G & 3, ua = uaddr(st < 8 ? 2 * st + 1 : 8 + st);
      const unsigned ad = wa[ua >> 16][a & 1] + (ua & 65535) + (a >> 1) * 8192 + kk * 4096;
      if (u != 1) wf[G & 3][kk].lo = tr_read(ad);
      if (u != 0) wf[G & 3][kk].hi = tr_read(ad + 512);
    };
    const unsigned sh = ((mi * 8 + wm * 2) * 56 + half * 28 + idx * 2 + wn) * 1024;
    u32x4 ho[2];
    auto epi = [&](int ps, int b, int c) {
      const int t = c >> 1, i = c & 1;
      float g0 = __builtin_elementwise_maximum(acc[ps][b][t][2 * i], 0.f), g1 = __builtin_elementwise_maximum(acc[ps][b][t][2 * i + 1], 0.f);
      float p0 = g0 * acc[ps][b][2 + t][2 * i];
      asm volatile("" : "+v"(p0));
      ho[b][c] = pack_bf16(p0, g1 * acc[ps][b][2 + t][2 * i + 1]);
    };
    auto sth = [&](int ps, int b) { gstore16_sc1(ho[b], lane16, srdH, sh + b * (56 * 1024) + ps * (14 * 1024)); };

    auto vmem = [&](auto PP) INL {
      sfor<first_op(PP.v), first_op(PP.v + 1)>([&](auto K) INL {
        constexpr Op o = SCH.op[K.v];
        if constexpr (o.pos == PP.v) {
          constexpr bool pre = PP.v < 128 && K.v == first_op(PP.v);
          if constexpr (o.kind == K_DMA) { if constexpr (o.n < 24) issue_xw(o.n, o.i, pre); else issue_wd(o.n - 24, o.i, pre); }
          if constexpr (o.kind == K_STORE) sth(o.n, o.i);
          if constexpr (o.kind == K_FLAG) gstore8_sc1(tag, one, srdH, syncb + 128 * (1 + rank));
          if constexpr (o.kind == K_POLL) gload8_sc1(fl, fo, srdH, syncb);
          if constexpr (o.kind == K_H) issue_h(o.n, o.i);
        }
      });
    };
    issue_xw(4, 0); issue_xw(4, 1); issue_xw(5, 0); issue_xw(5, 1);
    wait_vm<9>();
    ST(2)
    BAR();
    ldw(0, 0); ldx(0, 0, 0); ldw(0, 1); ldx(0, 0, 1); ldx(0, 1, 0); ldx(0, 1, 1); ldw(1, 0); ldw(1, 1); ldw(2, 0); ldw(2, 1);
    SB();
    sfor<0, 128>([&](auto Z) INL {
      constexpr int P = Z.v, ps = P >> 6, s = (P >> 3) & 7, G = P >> 1, a = G & 3, b = P & 1;
      if constexpr (is_bar(P)) {
        constexpr int n = bar_wait(P);
        static_assert(n >= 0 && n < 24, "wait count");
        if constexpr (P == 10) asm volatile("s_waitcnt vmcnt(%1)" : "+v"(ep) : "i"(n) : "memory");
        else if constexpr (P == 122) {
          asm volatile("s_waitcnt vmcnt(%1)" : "+v"(fl) : "i"(n) : "memory");
          if (w == 0) { if (__builtin_expect(!flags_ok(), 0)) poll(0); }
        } else wait_vm<n>();
        BAR();
        if (P == 2) { ST(3) } if (P == 26) { ST(4) } if (P == 58) { ST(5) } if (P == 74) { ST(6) } if (P == 90) { ST(7) } if (P == 106) { ST(8) } if (P == 122) { ST(9) }
      }
      if constexpr (P < 58 ? (P & 1) == 0 : (P & 3) == 2) __builtin_amdgcn_s_setprio(
P < 58 ? 3 - (((P + 6) & 7) >> 1) : 3 - (((P + 6) & 15) >> 2));
      if constexpr (s == 0) mfma_new(acc[ps][b][a], join(wf[G & 3][0]), xf[s][b][0]); else mfma_acc(acc[ps][b][a], join(wf[G & 3][0]), xf[s][b][0]);
      SB();
      if constexpr (G + 3 < 64) ldw(G + 3, b, 0);
      if constexpr (P >= 124) df[0][P - 124].lo = ldd(0, P - 124, 0);
      {
        constexpr int k0 = first_op(P);
        if constexpr (k0 < first_op(P + 1) && SCH.op[k0].kind == K_DMA && SCH.op[k0].i == 0) set_m0(mu0 + uaddr(SCH.op[k0].n));
        else if constexpr (P < 56 && (P & 7) >= 2 && (P & 7) < 6) ldx(s + 1, ((P - 2) >> 1) & 1, P & 1);
      }
      SB();
      mfma_acc(acc[ps][b][a], join(wf[G & 3][1]), xf[s][b][1]);
      SB();
      if constexpr (G + 3 < 64) ldw(G + 3, b, 1);
      if constexpr (P >= 124) df[0][P - 124].hi = ldd(0, P - 124, 1);
      {
        constexpr int k0 = first_op(P);
        if constexpr (k0 < first_op(P + 1) && SCH.op[k0].kind == K_DMA && SCH.op[k0].i == 0 && P < 56 && (P & 7) >= 2 && (P & 7) < 6) ldx(s + 1, ((P - 2) >> 1) & 1, P & 1);
      }
      if constexpr (P == 120) set_db(1);
      if constexpr (P == 65) { epi(0, 0, 0); epi(0, 0, 1); }
      if constexpr (P == 66) { epi(0, 1, 0); epi(0, 1, 1); }
      if constexpr (P == 67) { epi(0, 0, 2); epi(0, 0, 3); }
      if constexpr (P == 68 || P == 69) epi(0, 1, P - 66);
      if constexpr (P == 84) mktag();
      vmem(Z);
      SB();
    });
    ST(11)
    set_db(0); set_db(2);
    epi(1, 0, 0); epi(1, 0, 1); epi(1, 1, 0); epi(1, 1, 1);
    SB();
    epi(1, 0, 2); epi(1, 0, 3); epi(1, 1, 2); epi(1, 1, 3);
    vmem(IC<128>{});
    __builtin_amdgcn_s_setprio(0);
    ST(12)
  } else {
    gload8_sc1(ep, 0u, srdH, syncb);
    set_db(0); set_db(1); set_db(2);
    {
      unsigned t = 0;
#define NPF 3
#define PFX 2
#define PFB 1
      {
        const unsigned st = (rot + 2 + (lane & 3)) & 7;
        const unsigned vxo = (lane & 3) < PFX ? (mi * 128 + w * 16 + (lane >> 2)) * 1024 + st * 128 : 0x40000000u;
        asm volatile("buffer_load_dword %0, %1, %2, 0 offen" : "+v"(t) : "v"(vxo), "s"(srdX) : "memory");
      }
      {
        const unsigned li = (ml * 4 + (w & 3)) * 56 + lane;
        const unsigned row = li / 7, c = li - row * 7;
        const unsigned vo = lane < 56 ? (rot * 64 + row) * 3584 + (half * 14 + 7 + c) * 128 : 0x40000000u;
        asm volatile("buffer_load_dword %0, %1, %2, 0 offen" : "+v"(t) : "v"(vo), "s"(srdW) : "memory");
      }
#pragma unroll
      for (int i = 0; i < NPF; ++i) {
        unsigned c = i + xcd; c = c >= 7 ? c - 7 : c; c = c >= 7 ? c - 7 : c;
        asm volatile("buffer_load_dword %0, %1, %2, %3 offen" : "+v"(t) : "v"(lane * 128), "s"(srdD), "s"(((c * 4 + ml) * 8 + w) * 8192) : "memory");
      }
      asm volatile("s_waitcnt vmcnt(0)" : "+v"(t), "+v"(ep)::"memory");
      mktag();
    }
#pragma unroll
    for (int s = 0; s < 7; ++s) { issue_wd(s, 0); issue_wd(s, 1); }
    if (w == 0) poll_slow(0); else wait_vm<0>();
    BAR();
#pragma unroll
    for (int s = 0; s < 3; ++s) { issue_h(s, 0); issue_h(s, 1); }
#pragma unroll
    for (int a = 0; a < 4; ++a) { df[0][a].lo = ldd(0, a, 0); df[0][a].hi = ldd(0, a, 1); }
    wait_vm<0>();
  }
  asm volatile("" : "+s"(mu0), "+s"(sd0), "+s"(sh2), "+s"(syncb), "+s"(rank));
  asm volatile("" : "+s"(kb[0]), "+s"(kb[1]), "+s"(kb[2]), "+s"(kb[3]), "+s"(kb[4]), "+s"(kb[5]), "+s"(kb[6]));
  fmask = rank == 7 ? 0xFFFFu : FM0;
  f32x4 acc2[2][4];
  const unsigned pa = opq(lds0 + (kq == 3 ? 8 * UNIT : kq * (64 * 272)) + (mb * 32 + (lane & 15)) * 272 + g * 16);
  const unsigned ra = opq(lds0 + (w * 8 + (lane >> 3)) * 272 + (lane & 7) * 32), ra3 = opq(ra + 8 * UNIT);
  const unsigned vo = opq((w * 8 + (lane >> 3)) * 1024 + (lane & 7) * 16), so = opqs(m64 * 65536 + n64 * 128);
  const u32x4 srdO = make_srd(out, M_ * D_ * 2);
  auto wpart = [&](int a) {
    *(LDS_AS f32x4*)(uintptr_t)(pa + a * 64) = acc2[0][a];
    *(LDS_AS f32x4*)(uintptr_t)(pa + 16 * 272 + a * 64) = acc2[1][a];
  };
  auto vmem2 = [&](auto PP) INL {
    sfor<first_op(PP.v), first_op(PP.v + 1)>([&](auto K) INL {
      constexpr Op o = SCH.op[K.v];
      if constexpr (o.pos == PP.v) {
        if constexpr (o.kind == K_DMA) issue_wd(o.n - 24, o.i);
        if constexpr (o.kind == K_FLAG) gstore8_sc1(tag, one, srdH, syncb + 128 * (17 + rank));
        if constexpr (o.kind == K_POLL) gload8_sc1(fl, fo, srdH, syncb + 2048);
        if constexpr (o.kind == K_H) issue_h(o.n, o.i);
      }
    });
  };
  sfor<0, 14>([&](auto S) INL {
    constexpr int s = S.v;
    if constexpr (s == Y1 || s == FCHK || s == Y2 || s == Y3) {
      constexpr int n = bar_wait(Q(s, 0));
      static_assert(n >= 0 && n < 24, "wait count");
      if constexpr (s == FCHK) {
        asm volatile("s_waitcnt vmcnt(%1)" : "+v"(fl) : "i"(n) : "memory");
        if (w == 0) { if (__builtin_expect(!flags_ok(), 0)) poll(2048); }
      } else wait_vm<n>();
      BAR();
      if (s == Y1) { ST(13) } if (s == FCHK) { ST(14) } if (s == Y2) { ST(15) } if (s == Y3) { ST(16) }
      if constexpr (s == Y3) { if (rank == 7 && w == 0) gstore8_sc1(tag, one, srdH, syncb); }
    }
    vmem2(IC<Q(s, 0)>{});
    asm volatile("s_waitcnt vmcnt(%2)" : "+v"(hf[s][0]), "+v"(hf[s][1]) : "i"(younger_than(K_H, s, 1, Q(s, 1))) : "memory");
    if (s == 0) { ST(17) }
    SB();
    sfor<0, 4>([&](auto A) INL {
      constexpr int a = A.v;
      if constexpr (s == 0) mfma_new(acc2[0][a], join(df[0][a]), hf[0][0]); else mfma_acc(acc2[0][a], join(df[s & 1][a]), hf[s][0]);
      SB();
      if constexpr (s + 1 < 14) df[(s + 1) & 1][a].lo = ldd(s + 1, a, 0);
      SB();
      if constexpr (s == 0) mfma_new(acc2[1][a], join(df[0][a]), hf[0][1]); else mfma_acc(acc2[1][a], join(df[s & 1][a]), hf[s][1]);
      SB();
      if constexpr (s + 1 < 14) df[(s + 1) & 1][a].hi = ldd(s + 1, a, 1);
      vmem2(IC<Q(s, a + 1)>{});
      if constexpr (s == 13 && a == 3) { asm volatile("s_nop 1" ::: "memory"); SB(); wpart(0); SB(); wpart(1); }
      SB();
    });
  });
  ST(18)
  wpart(2);
  SB();
  asm volatile("s_nop 1" ::: "memory");
  SB();
  wpart(3);
  wait_lgkm0();
  BAR();
  ST(19)
  {
    f32x4 p0[4], p1[4];
#pragma unroll
    for (int v = 0; v < 4; ++v) {
      const unsigned ad = v == 3 ? ra3 : ra + v * (64 * 272);
      p0[v] = *(LDS_AS f32x4*)(uintptr_t)(ad);
      p1[v] = *(LDS_AS f32x4*)(uintptr_t)(ad + 16);
    }
    SB();
    const f32x4 s0 = (p0[0] + p0[1]) + (p0[2] + p0[3]), s1 = (p1[0] + p1[1]) + (p1[2] + p1[3]);
    u32x4 o = {pack_bf16(s0[0], s0[1]), pack_bf16(s0[2], s0[3]), pack_bf16(s1[0], s1[1]), pack_bf16(s1[2], s1[3])};
    asm volatile("buffer_store_dwordx4 %0, %1, %2, %3 offen" ::"v"(o), "v"(vo), "s"(srdO), "s"(so) : "memory");
  }
  ST(20)
}

extern "C" void kernel_launch(void* const* d_in, const int* in_sizes, int n_in, void* d_out, int out_size,
                              void* d_ws, size_t ws_size, hipStream_t stream) {
  (void)in_sizes; (void)n_in; (void)out_size; (void)ws_size;
  (void)hipFuncSetAttribute((const void*)k_fused, hipFuncAttributeMaxDynamicSharedMemorySize, LDS_TOTAL);
  k_fused<<<256, 512, LDS_TOTAL, stream>>>(d_in[0], d_in[1], d_in[2], d_in[3], d_out, d_ws);
}
```

```cpp
#include <hip/hip_runtime.h>
#include <hip/hip_bf16.h>
#include <stdint.h>

typedef __attribute__((ext_vector_type(8))) __bf16 bf16x8;
typedef __attribute__((ext_vector_type(4))) short s16x4;
typedef __attribute__((ext_vector_type(8))) short s16x8;
typedef __attribute__((ext_vector_type(2))) __bf16 bf16x2;
typedef __attribute__((ext_vector_type(2))) float f32x2;
typedef __attribute__((ext_vector_type(4))) float f32x4;
typedef __attribute__((ext_vector_type(4))) unsigned u32x4;
typedef __attribute__((ext_vector_type(2))) unsigned u32x2;
#define LDS_AS __attribute__((address_space(3)))
#define ST_DECL
#define ST(i)
#define ST_FLUSH(a,b)

constexpr int M_ = 2048, D_ = 512, F_ = 1792;
constexpr unsigned SYNC_OFF = 8u << 20;
constexpr unsigned SPIN_CAP = 1u << 17;

__device__ __forceinline__ unsigned rfl(unsigned v) { return __builtin_amdgcn_readfirstlane(v); }

__device__ __forceinline__ u32x4 make_srd(const void* p, unsigned bytes) {
  uint64_t a = (uint64_t)p;
  u32x4 r;
  r[0] = rfl((unsigned)a);
  r[1] = rfl((unsigned)(a >> 32)) & 0xffffu;
  r[2] = bytes;
  r[3] = 0x00020000u;
  return r;
}

template <int IMM>
__device__ __forceinline__ void dma16(unsigned m0v, unsigned voff, u32x4 srd, unsigned soff) {
  asm volatile("s_mov_b32 m0, %0\n\ts_nop 0\n\tbuffer_load_dwordx4 %1, %2, %3 offen offset:%4 lds"
               ::"s"(m0v), "v"(voff), "s"(srd), "s"(soff), "i"(IMM) : "memory");
}
template <int IMM>
__device__ __forceinline__ void dma16n(unsigned voff, u32x4 srd, unsigned soff) {
  asm volatile("buffer_load_dwordx4 %0, %1, %2 offen offset:%3 lds" ::"v"(voff), "s"(srd), "s"(soff), "i"(IMM) : "memory");
}
__device__ __forceinline__ void set_m0(unsigned v) { asm volatile("s_mov_b32 m0, %0" ::"s"(v) : "memory"); }
__device__ __forceinline__ unsigned opq(unsigned v) { asm volatile("" : "+v"(v)); return v; }
__device__ __forceinline__ unsigned opqs(unsigned v) { asm volatile("" : "+s"(v)); return v; }
__device__ __forceinline__ void gload16_sc1(u32x4& dst, unsigned voff, u32x4 srd, unsigned soff) {
  asm volatile("buffer_load_dwordx4 %0, %1, %2, %3 offen sc1" : "=v"(dst) : "v"(voff), "s"(srd), "s"(soff) : "memory");
}
__device__ __forceinline__ void greload16_sc1(u32x4& dst, unsigned voff, u32x4 srd, unsigned soff) {
  asm volatile("buffer_load_dwordx4 %0, %1, %2, %3 offen sc1" : "+v"(dst) : "v"(voff), "s"(srd), "s"(soff) : "memory");
}
__device__ __forceinline__ void gstore16_sc1(u32x4 v, unsigned voff, u32x4 srd, unsigned soff) {
  asm volatile("buffer_store_dwordx4 %0, %1, %2, %3 offen sc1\n\ts_nop 1" ::"v"(v), "v"(voff), "s"(srd), "s"(soff) : "memory");
}
__device__ __forceinline__ void gload8_sc1(u32x2& dst, unsigned voff, u32x4 srd, unsigned soff) {
  asm volatile("buffer_load_dwordx2 %0, %1, %2, %3 offen sc1" : "=v"(dst) : "v"(voff), "s"(srd), "s"(soff) : "memory");
}
__device__ __forceinline__ void gstore8_sc1(u32x2 v, unsigned voff, u32x4 srd, unsigned soff) {
  asm volatile("buffer_store_dwordx2 %0, %1, %2, %3 offen sc1" ::"v"(v), "v"(voff), "s"(srd), "s"(soff) : "memory");
}

__device__ __forceinline__ s16x4 tr_read(unsigned addr) {
  return __builtin_amdgcn_ds_read_tr16_b64_v4i16((LDS_AS s16x4*)(uintptr_t)addr);
}
__device__ __forceinline__ bf16x8 tr_frag(unsigned a0, unsigned a1) {
  s16x4 lo = tr_read(a0), hi = tr_read(a1);
  s16x8 v = __builtin_shufflevector(lo, hi, 0, 1, 2, 3, 4, 5, 6, 7);
  return __builtin_bit_cast(bf16x8, v);
}
struct Frag { s16x4 lo, hi; };
__device__ __forceinline__ bf16x8 join(const Frag& f) {
  s16x8 v = __builtin_shufflevector(f.lo, f.hi, 0, 1, 2, 3, 4, 5, 6, 7);
  return __builtin_bit_cast(bf16x8, v);
}
__device__ __forceinline__ bf16x8 lds_read16(unsigned addr) { return *(LDS_AS bf16x8*)(uintptr_t)addr; }
__device__ __forceinline__ unsigned pack_bf16(float a, float b) {
  f32x2 v = {a, b};
  bf16x2 r = __builtin_convertvector(v, bf16x2);
  return __builtin_bit_cast(unsigned, r);
}
__device__ __forceinline__ float rnd_bf16(float a) { return __builtin_bit_cast(float, pack_bf16(0.f, a)); }

template <class TB>
__device__ __forceinline__ void mfma_acc(f32x4& c, bf16x8 a, TB b) {
  asm volatile("v_mfma_f32_16x16x32_bf16 %0, %1, %2, %0" : "+v"(c) : "v"(a), "v"(b));
}
template <class TB>
__device__ __forceinline__ void mfma_new(f32x4& c, bf16x8 a, TB b) {
  asm volatile("v_mfma_f32_16x16x32_bf16 %0, %1, %2, 0" : "=&v"(c) : "v"(a), "v"(b));
}

__device__ __forceinline__ void mfma2_acc(f32x4& c, bf16x8 a0, bf16x8 b0, bf16x8 a1, bf16x8 b1) {
  asm volatile("v_mfma_f32_16x16x32_bf16 %0, %1, %2, %0\n\tv_mfma_f32_16x16x32_bf16 %0, %3, %4, %0" : "+v"(c) : "v"(a0), "v"(b0), "v"(a1), "v"(b1));
}
__device__ __forceinline__ void mfma2_new(f32x4& c, bf16x8 a0, bf16x8 b0, bf16x8 a1, bf16x8 b1) {
  asm volatile("v_mfma_f32_16x16x32_bf16 %0, %1, %2, 0\n\tv_mfma_f32_16x16x32_bf16 %0, %3, %4, %0" : "=&v"(c) : "v"(a0), "v"(b0), "v"(a1), "v"(b1));
}

template <int I> struct IC { static constexpr int v = I; };
template <int I, int N, class F> __device__ __forceinline__ void sfor(F&& f) {
  if constexpr (I < N) { f(IC<I>{}); sfor<I + 1, N>(f); }
}
template <int N> __device__ __forceinline__ void wait_vm() { asm volatile("s_waitcnt vmcnt(%0)" ::"i"(N) : "memory"); }
template <int N> __device__ __forceinline__ void wait_vm_lgkm0() { asm volatile("s_waitcnt vmcnt(%0) lgkmcnt(0)" ::"i"(N) : "memory"); }
__device__ __forceinline__ void wait_lgkm0() { asm volatile("s_waitcnt lgkmcnt(0)" ::: "memory"); }
#define BAR() __builtin_amdgcn_s_barrier()
#define SB() __builtin_amdgcn_sched_barrier(0)
#define INL __attribute__((always_inline))

constexpr int UNIT = 16384, LDS_TOTAL = 10 * UNIT;
constexpr int uaddr(int n) { return (n % 10) * UNIT; }

enum { K_DMA = 1, K_STORE, K_FLAG, K_POLL, K_H };
struct Op { int pos, kind, n, i; };
struct Sched { int cnt; Op op[200]; };
constexpr void put(Sched& s, int pos, int kind, int n, int i) { s.op[s.cnt++] = Op{pos, kind, n, i}; }
constexpr void spread(Sched& s, int p0, int p1, int u0, int nu) {
  for (int k = 0; k < 2 * nu; ++k) put(s, p0 + (k * (p1 - p0)) / (2 * nu), K_DMA, u0 + (k >> 1), k & 1);
}
constexpr int Q(int step, int slot) { return 200 + 8 * step + slot; }
constexpr void pair(Sched& s, int step, int slot, int kind, int n) { put(s, Q(step, slot), kind, n, 0); put(s, Q(step, slot == 0 ? 0 : slot + 1), kind, n, 1); }
#define LEAD 0
#define HB 3
constexpr int Y1 = 2, Y2 = 5, FCHK = 7, Y3 = 10;
constexpr Sched make_sched() {
  Sched s{};
  for (int n = 0; n < 4; ++n) { put(s, -1, K_DMA, n, 0); put(s, -1, K_DMA, n, 1); }
  put(s, -1, K_POLL, 0, 0);
  spread(s, 0, 2, 4, 2);
  for (int t = 0; t < 7; ++t) spread(s, 8 * t + 2, 8 * t + 9, 2 * t + 6, 2);
  spread(s, 58, 66, 20, 2);
  put(s, 67, K_STORE, 0, 0);
  put(s, 69, K_STORE, 0, 1);
  spread(s, 72, 80, 22, 1);
  spread(s, 82, 89, 23, 2);
  put(s, 90, K_FLAG, 0, 0);
  spread(s, 91, 103, 25, 2);
  spread(s, 103, 111, 27, 2);
  put(s, 111, K_POLL, 1, 0);
  spread(s, 112, 121, 29, 2);
  for (int k = 0; k < 4; ++k) put(s, 122 + k, K_H, k >> 1, k & 1);
  put(s, 128, K_STORE, 1, 0);
  put(s, 128, K_STORE, 1, 1);
  put(s, 128, K_H, 2, 0);
  put(s, 128, K_H, 2, 1);
  put(s, Q(Y1, 0), K_FLAG, 1, 0);
  pair(s, 2, 0, K_H, 3); pair(s, 2, 0, K_H, 4);
  pair(s, 2, 3, K_DMA, 31);
  pair(s, 3, 1, K_H, 5); pair(s, 3, 3, K_H, 6);
  pair(s, 4, 1, K_DMA, 32); pair(s, 4, 3, K_DMA, 33);
  pair(s, 5, 1, K_DMA, 34); pair(s, 5, 3, K_DMA, 35);
  put(s, Q(6, 0), K_POLL, 2, 0);
  pair(s, 6, 1, K_DMA, 36); pair(s, 6, 3, K_DMA, 37);
  for (int k = 0; k < HB; ++k) pair(s, 7, 0, K_H, 7 + k);
  for (int k = 7 + HB; k < 14; ++k) pair(s, k - HB, 1, K_H, k);
  return s;
}
constexpr Sched SCH = make_sched();
constexpr int younger_than(int kind, int n, int i, int P) {
  int c = -1;
  for (int k = 0; k < SCH.cnt; ++k) {
    if (SCH.op[k].pos >= P) break;
    if (SCH.op[k].kind == kind && SCH.op[k].n == n && SCH.op[k].i == i) c = 0; else if (c >= 0) ++c;
  }
  return c;
}
constexpr int first_op(int P) { int k = 0; while (k < SCH.cnt && SCH.op[k].pos < P) ++k; return k; }
constexpr bool is_bar(int P) { return P < 64 ? (P & 7) == 2 : (P & 15) == 10; }
constexpr int bar_wait(int P) {
  if (P < 64) return younger_than(K_DMA, 2 * (P >> 3) + 3, 1, P);
  if (P == 74) return younger_than(K_DMA, 19, 1, P);
  if (P == 90) return younger_than(K_STORE, 0, 1, P);
  if (P == 106) return younger_than(K_DMA, 23, 1, P);
  if (P == 122) return younger_than(K_POLL, 1, 0, P);
  if (P == Q(Y1, 0)) return younger_than(K_STORE, 1, 1, P);
  if (P == Q(Y2, 0)) return younger_than(K_DMA, 31, 1, P);
  if (P == Q(FCHK, 0)) return younger_than(K_POLL, 2, 0, P);
  return younger_than(K_DMA, 37, 1, P);
}
constexpr bool sched_sorted() { for (int k = 1; k < SCH.cnt; ++k) if (SCH.op[k].pos < SCH.op[k - 1].pos) return false; return true; }
static_assert(sched_sorted(), "schedule order");

__global__ __launch_bounds__(512, 2) void k_fused(const void* __restrict__ x, const void* __restrict__ Wg,
                                                  const void* __restrict__ Wu, const void* __restrict__ Wd,
                                                  void* __restrict__ out, void* __restrict__ ws) {
  extern __shared__ __attribute__((aligned(16))) char smem[];
  const unsigned lds0 = (unsigned)(uintptr_t)(LDS_AS char*)smem;
  ST_DECL ST(0)
  const unsigned bid = blockIdx.x;
  const unsigned w = rfl(threadIdx.x >> 6);
  const bool busy = bid < 224;
#define CPF 18000
  const unsigned mi = (bid & 6) * 2 + ((bid >> 3) & 3), half = bid & 1, idx = bid >> 5;
  const unsigned rot = ((bid & 6) + 4 * half) & 7;
  unsigned vw0, vw1, vx0, vx1;
  {
    const unsigned lane = threadIdx.x & 63, g = lane >> 4;
    vw0 = (lane >> 3) * 3584 + (((lane & 7) ^ ((g & 1) << 1)) << 4);
    vw1 = (vw0 ^ 64) + 8 * 3584 - 1024;
    vx0 = (lane >> 3) * 1024 + (((lane & 7) ^ g) << 4);
    vx1 = (vx0 ^ 64) + 8192 - 1024;
  }
  unsigned sx0 = (mi * 128 + 16 * w) * 1024;
  unsigned sw0 = (16 * (w & 3)) * 3584 + (half * 14 + idx) * 128;
  unsigned mu0 = lds0 + w * 2048;
  unsigned sw00 = sw0 + rot * (64 * 3584), sx00 = sx0 + rot * 128;
  asm volatile("" : "+v"(vw0), "+v"(vw1), "+v"(vx0), "+v"(vx1));
  asm volatile("" : "+s"(sx0), "+s"(sw0), "+s"(mu0), "+s"(sw00), "+s"(sx00));
  SB();
  const u32x4 srdX = make_srd(x, M_ * D_ * 2);
  const u32x4 srdW = make_srd((w >> 2) ? Wu : Wg, D_ * F_ * 2);
  auto issue_xw = [&](int n, int i, bool pre = false) {
    const int s = n < 16 ? n >> 1 : n - 16;
    const unsigned sg = (s + rot) & 7;
    if (n < 16 && (n & 1) == 0) {
      const unsigned so = n == 0 ? sx00 : sx0 + sg * 128;
      if (i == 0) { if (pre) dma16n<0>(vx0, srdX, so); else dma16<0>(mu0 + uaddr(n), vx0, srdX, so); } else dma16n<1024>(vx1, srdX, so);
    } else {
      const unsigned so = n == 1 ? sw00 : sw0 + sg * (64 * 3584) + (n >= 16 ? 896 : 0);
      if (i == 0) { if (pre) dma16n<0>(vw0, srdW, so); else dma16<0>(mu0 + uaddr(n), vw0, srdW, so); } else dma16n<1024>(vw1, srdW, so);
    }
  };
  asm volatile("" ::"s"(Wd), "s"(out), "s"(ws));
  if (busy) {
    issue_xw(0, 0); issue_xw(0, 1); issue_xw(1, 0); issue_xw(1, 1);
    BAR();
    issue_xw(2, 0); issue_xw(2, 1); issue_xw(3, 0); issue_xw(3, 1);
  } else {
    uint64_t pc;
    asm volatile("s_getpc_b64 %0" : "=s"(pc));
    const unsigned o = min(threadIdx.x * 64u, (unsigned)CPF);
    asm volatile("global_load_dword v249, %0, %1" ::"v"(o), "s"(pc) : "v249", "memory");
  }
  SB();
  const unsigned tid = opq(threadIdx.x), lane = tid & 63;
  const unsigned bid2 = opqs(bid), xcd = bid2 & 7, ml = (bid2 >> 3) & 3;
  unsigned rank = half * 8 + idx;
  const unsigned m64 = 2 * mi + half, n64 = idx;
  const unsigned g = lane >> 4, q = (lane & 15) >> 2, p = lane & 3;
  const u32x4 srdH = make_srd(ws, SYNC_OFF + 16 * 8192);
  const u32x4 srdD = make_srd(Wd, F_ * D_ * 2);
  unsigned syncb = SYNC_OFF + mi * 8192;
  const unsigned lane16 = lane * 16;

  const unsigned kq = w >> 1, mb = w & 1;
  unsigned rot2 = 2 * ml + idx; rot2 = rot2 >= 7 ? rot2 - 7 : rot2;
  unsigned kb[7];
  auto set_kb = [&]() {
    unsigned e = opqs(rot2);
#pragma unroll
    for (int j = 0; j < 7; ++j) {
      kb[j] = opqs((e + 7 * kq + (kq >> 1) * 14) * 1024);
      e = e == 6 ? 0 : e + 1;
    }
  };
  auto kap = [&](int s) { return kb[s % 7] + (s >= 7 ? 14 * 1024 : 0); };
  const unsigned vd0 = (lane >> 3) * 1024 + (((lane & 7) ^ (g << 1)) << 4);
  const unsigned vd1 = vd0 + 8192 - 1024;
  unsigned sd0 = (16 * mb) * 1024 + n64 * 128;
  auto issue_wd = [&](int s, int i, bool pre = false) {
    if (i == 0) { if (pre) dma16n<0>(vd0, srdD, sd0 + kap(s) * 32); else dma16<0>(mu0 + uaddr(24 + s), vd0, srdD, sd0 + kap(s) * 32); }
    else dma16n<1024>(vd1, srdD, sd0 + kap(s) * 32);
  };
  const unsigned dl = lds0 + kq * 4096 + (4 * g + q) * 128 + (((2 * g + (q >> 1)) & 3) << 5) + p * 8;
  unsigned db[3][4];
  auto set_db = [&](int k) {
#pragma unroll
    for (int c = 0; c < 4; ++c) db[k][c] = opq((dl ^ (c << 5)) + k * 4 * UNIT);
  };
  auto ldd = [&](int s, int a, int u) {
    const int ua = uaddr(24 + s);
    return tr_read(db[ua >> 16][a] + (ua & 65535) + u * 2048);
  };
  u32x2 ep, tag, fl;
  auto mktag = [&]() {
    const uint64_t t = (((uint64_t)ep[1] << 32) | ep[0]) * 0x9E3779B97F4A7C15ull + 1ull;
    tag = u32x2{(unsigned)t, (unsigned)(t >> 32)};
  };
  const unsigned one = tid == 0 ? 0u : 0x40000000u;
  const unsigned fo = tid < 16 ? 128 * (1 + tid) : 0x40000000u;
#define FM0 0x7F7Fu
  unsigned fmask = busy ? FM0 : 0x7F7Fu;
  auto flags_ok = [&]() { return (rfl((unsigned)__builtin_amdgcn_ballot_w64(fl[0] == tag[0] && fl[1] == tag[1])) & fmask) == fmask; };
  auto poll = [&](unsigned off) {
    unsigned cnt = SPIN_CAP, t;
#define PLD(r) "buffer_load_dwordx2 " r ", %2, %3, %4 offen sc1\n\t"
#define PCK(r) "s_waitcnt vmcnt(2)\n\tv_cmp_eq_u64 vcc, " r ", %5\n\ts_nop 1\n\ts_and_b32 %1, vcc_lo, %6\n\ts_cmp_eq_u32 %1, %6\n\ts_cbranch_scc1 2f\n\t"
    asm volatile(PLD("v[250:251]") "s_sleep 3\n\t" PLD("v[252:253]") "s_sleep 3\n\t" PLD("v[254:255]")
                 "1:\n\t"
                 PCK("v[250:251]") PLD("v[250:251]") PCK("v[252:253]") PLD("v[252:253]") PCK("v[254:255]") PLD("v[254:255]")
                 "s_sub_u32 %0, %0, 1\n\ts_cbranch_scc0 1b\n\t"
                 "2:"
                 : "+s"(cnt), "=&s"(t) : "v"(fo), "s"(srdH), "s"(syncb + off), "v"(tag), "s"(fmask)
                 : "v250", "v251", "v252", "v253", "v254", "v255", "vcc", "scc", "memory");
#undef PLD
#undef PCK
  };
  auto poll_slow = [&](unsigned off) {
#pragma nounroll
    for (unsigned spin = 0; spin < SPIN_CAP; ++spin) {
      gload8_sc1(fl, fo, srdH, syncb + off);
      asm volatile("s_waitcnt vmcnt(0)" : "+v"(fl)::"memory");
      if (flags_ok()) break;
      __builtin_amdgcn_s_sleep(8);
    }
  };
  Frag df[2][4];
  u32x4 hf[14][2];
  unsigned sh2 = ((m64 * 4 + mb * 2) * 56) * 1024;
  const unsigned lane16b = lane16 + 56 * 1024;
  auto issue_h = [&](int s, int b) { gload16_sc1(hf[s][b], b ? lane16b : lane16, srdH, sh2 + kap(s)); };

  set_kb();
  if (__builtin_expect(busy, 1)) {
    gload8_sc1(ep, 0u, srdH, syncb);
    const unsigned wm = w & 3, wn = w >> 2;
    const unsigned xrow = 32 * wm + (lane & 15);
    const unsigned xa0 = lds0 + xrow * 128 + ((g ^ ((xrow >> 1) & 7)) << 4);
    const unsigned wa0 = lds0 + (8 * g + q) * 128 + (((2 * wn) ^ ((q >> 1) | ((g & 1) << 1))) << 5) + p * 8;
    unsigned xa[3][2], wa[3][2];
#pragma unroll
    for (int k = 0; k < 3; ++k)
#pragma unroll
      for (int c = 0; c < 2; ++c) { xa[k][c] = opq((xa0 ^ (c << 6)) + k * 4 * UNIT); wa[k][c] = opq((wa0 ^ (c << 5)) + k * 4 * UNIT); }
    ST(1)

    f32x4 acc[2][2][4];
    bf16x8 xf[8][2][2];
    Frag wf[4][2];
    auto ldx = [&](int s, int b, int kk) { const int ua = uaddr(2 * s); xf[s][b][kk] = lds_read16(xa[ua >> 16][kk] + (ua & 65535) + b * 2048); };
    auto ldw = [&](int G, int kk, int u = 2) {
      const int st = G >> 2, a = G & 3, ua = uaddr(st < 8 ? 2 * st + 1 : 8 + st);
      const unsigned ad = wa[ua >> 16][a & 1] + (ua & 65535) + (a >> 1) * 8192 + kk * 4096;
      if (u != 1) wf[G & 3][kk].lo = tr_read(ad);
      if (u != 0) wf[G & 3][kk].hi = tr_read(ad + 512);
    };
    const unsigned sh = ((mi * 8 + wm * 2) * 56 + half * 28 + idx * 2 + wn) * 1024;
    u32x4 ho[2];
    auto epi = [&](int ps, int b, int c) {
      const int t = c >> 1, i = c & 1;
      float g0 = __builtin_elementwise_maximum(acc[ps][b][t][2 * i], 0.f), g1 = __builtin_elementwise_maximum(acc[ps][b][t][2 * i + 1], 0.f);
      float p0 = g0 * acc[ps][b][2 + t][2 * i];
      asm volatile("" : "+v"(p0));
      ho[b][c] = pack_bf16(p0, g1 * acc[ps][b][2 + t][2 * i + 1]);
    };
    auto sth = [&](int ps, int b) { gstore16_sc1(ho[b], lane16, srdH, sh + b * (56 * 1024) + ps * (14 * 1024)); };

    auto vmem = [&](auto PP) INL {
      sfor<first_op(PP.v), first_op(PP.v + 1)>([&](auto K) INL {
        constexpr Op o = SCH.op[K.v];
        if constexpr (o.pos == PP.v) {
          constexpr bool pre = PP.v < 128 && K.v == first_op(PP.v);
          if constexpr (o.kind == K_DMA) { if constexpr (o.n < 24) issue_xw(o.n, o.i, pre); else issue_wd(o.n - 24, o.i, pre); }
          if constexpr (o.kind == K_STORE) sth(o.n, o.i);
          if constexpr (o.kind == K_FLAG) gstore8_sc1(tag, one, srdH, syncb + 128 * (1 + rank));
          if constexpr (o.kind == K_POLL) gload8_sc1(fl, fo, srdH, syncb);
          if constexpr (o.kind == K_H) issue_h(o.n, o.i);
        }
      });
    };
    wait_vm<5>();
    ST(2)
    BAR();
    ldw(0, 0); ldx(0, 0, 0); ldw(0, 1); ldx(0, 0, 1); ldx(0, 1, 0); ldx(0, 1, 1); ldw(1, 0); ldw(1, 1); ldw(2, 0); ldw(2, 1);
    SB();
    sfor<0, 128>([&](auto Z) INL {
      constexpr int P = Z.v, ps = P >> 6, s = (P >> 3) & 7, G = P >> 1, a = G & 3, b = P & 1;
      if constexpr (is_bar(P)) {
        constexpr int n = bar_wait(P);
        static_assert(n >= 0 && n < 24, "wait count");
        if constexpr (P == 10) asm volatile("s_waitcnt vmcnt(%1)" : "+v"(ep) : "i"(n) : "memory");
        else if constexpr (P == 122) {
          asm volatile("s_waitcnt vmcnt(%1)" : "+v"(fl) : "i"(n) : "memory");
          if (w == 0) { if (__builtin_expect(!flags_ok(), 0)) poll(0); }
        } else wait_vm<n>();
        BAR();
        if (P == 2) { ST(3) } if (P == 26) { ST(4) } if (P == 58) { ST(5) } if (P == 74) { ST(6) } if (P == 90) { ST(7) } if (P == 106) { ST(8) } if (P == 122) { ST(9) }
      }
      if constexpr (P < 58 ? (P & 1) == 0 : (P & 3) == 2) __builtin_amdgcn_s_setprio(
P < 58 ? 3 - (((P + 6) & 7) >> 1) : 3 - (((P + 6) & 15) >> 2));
      if constexpr (s == 0) mfma_new(acc[ps][b][a], join(wf[G & 3][0]), xf[s][b][0]); else mfma_acc(acc[ps][b][a], join(wf[G & 3][0]), xf[s][b][0]);
      SB();
      if constexpr (G + 3 < 64) ldw(G + 3, b, 0);
      if constexpr (P >= 124) df[0][P - 124].lo = ldd(0, P - 124, 0);
      {
        constexpr int k0 = first_op(P);
        if constexpr (k0 < first_op(P + 1) && SCH.op[k0].kind == K_DMA && SCH.op[k0].i == 0) set_m0(mu0 + uaddr(SCH.op[k0].n));
        else if constexpr (P < 56 && (P & 7) >= 2 && (P & 7) < 6) ldx(s + 1, ((P - 2) >> 1) & 1, P & 1);
      }
      SB();
      mfma_acc(acc[ps][b][a], join(wf[G & 3][1]), xf[s][b][1]);
      SB();
      if constexpr (G + 3 < 64) ldw(G + 3, b, 1);
      if constexpr (P >= 124) df[0][P - 124].hi = ldd(0, P - 124, 1);
      {
        constexpr int k0 = first_op(P);
        if constexpr (k0 < first_op(P + 1) && SCH.op[k0].kind == K_DMA && SCH.op[k0].i == 0 && P < 56 && (P & 7) >= 2 && (P & 7) < 6) ldx(s + 1, ((P - 2) >> 1) & 1, P & 1);
      }
      if constexpr (P == 120) set_db(1);
      if constexpr (P == 65) { epi(0, 0, 0); epi(0, 0, 1); }
      if constexpr (P == 66) { epi(0, 1, 0); epi(0, 1, 1); }
      if constexpr (P == 67) { epi(0, 0, 2); epi(0, 0, 3); }
      if constexpr (P == 68 || P == 69) epi(0, 1, P - 66);
      if constexpr (P == 84) mktag();
      vmem(Z);
      SB();
    });
    ST(11)
    set_db(0); set_db(2);
    epi(1, 0, 0); epi(1, 0, 1); epi(1, 1, 0); epi(1, 1, 1);
    SB();
    epi(1, 0, 2); epi(1, 0, 3); epi(1, 1, 2); epi(1, 1, 3);
    vmem(IC<128>{});
    __builtin_amdgcn_s_setprio(0);
    ST(12)
  } else {
    gload8_sc1(ep, 0u, srdH, syncb);
    set_db(0); set_db(1); set_db(2);
    {
      unsigned t = 0;
#define NPF 3
#define PFX 2
#define PFB 1
      {
        const unsigned st = (rot + 2 + (lane & 3)) & 7;
        const unsigned vxo = (lane & 3) < PFX ? (mi * 128 + w * 16 + (lane >> 2)) * 1024 + st * 128 : 0x40000000u;
        asm volatile("buffer_load_dword %0, %1, %2, 0 offen" : "+v"(t) : "v"(vxo), "s"(srdX) : "memory");
      }
      {
        const unsigned li = (ml * 4 + (w & 3)) * 56 + lane;
        const unsigned row = li / 7, c = li - row * 7;
        const unsigned vo = lane < 56 ? (rot * 64 + row) * 3584 + (half * 14 + 7 + c) * 128 : 0x40000000u;
        asm volatile("buffer_load_dword %0, %1, %2, 0 offen" : "+v"(t) : "v"(vo), "s"(srdW) : "memory");
      }
#pragma unroll
      for (int i = 0; i < NPF; ++i) {
        unsigned c = i + xcd; c = c >= 7 ? c - 7 : c; c = c >= 7 ? c - 7 : c;
        asm volatile("buffer_load_dword %0, %1, %2, %3 offen" : "+v"(t) : "v"(lane * 128), "s"(srdD), "s"(((c * 4 + ml) * 8 + w) * 8192) : "memory");
      }
      asm volatile("s_waitcnt vmcnt(0)" : "+v"(t), "+v"(ep)::"memory");
      mktag();
    }
#pragma unroll
    for (int s = 0; s < 7; ++s) { issue_wd(s, 0); issue_wd(s, 1); }
    if (w == 0) poll_slow(0); else wait_vm<0>();
    BAR();
#pragma unroll
    for (int s = 0; s < 3; ++s) { issue_h(s, 0); issue_h(s, 1); }
#pragma unroll
    for (int a = 0; a < 4; ++a) { df[0][a].lo = ldd(0, a, 0); df[0][a].hi = ldd(0, a, 1); }
    wait_vm<0>();
  }
  asm volatile("" : "+s"(mu0), "+s"(sd0), "+s"(sh2), "+s"(syncb), "+s"(rank));
  asm volatile("" : "+s"(kb[0]), "+s"(kb[1]), "+s"(kb[2]), "+s"(kb[3]), "+s"(kb[4]), "+s"(kb[5]), "+s"(kb[6]));
  fmask = rank == 7 ? 0xFFFFu : FM0;
  f32x4 acc2[2][4];
  const unsigned pa = opq(lds0 + (kq == 3 ? 8 * UNIT : kq * (64 * 272)) + (mb * 32 + (lane & 15)) * 272 + g * 16);
  const unsigned ra = opq(lds0 + (w * 8 + (lane >> 3)) * 272 + (lane & 7) * 32), ra3 = opq(ra + 8 * UNIT);
  const unsigned vo = opq((w * 8 + (lane >> 3)) * 1024 + (lane & 7) * 16), so = opqs(m64 * 65536 + n64 * 128);
  const u32x4 srdO = make_srd(out, M_ * D_ * 2);
  auto wpart = [&](int a) {
    *(LDS_AS f32x4*)(uintptr_t)(pa + a * 64) = acc2[0][a];
    *(LDS_AS f32x4*)(uintptr_t)(pa + 16 * 272 + a * 64) = acc2[1][a];
  };
  auto vmem2 = [&](auto PP) INL {
    sfor<first_op(PP.v), first_op(PP.v + 1)>([&](auto K) INL {
      constexpr Op o = SCH.op[K.v];
      if constexpr (o.pos == PP.v) {
        if constexpr (o.kind == K_DMA) issue_wd(o.n - 24, o.i);
        if constexpr (o.kind == K_FLAG) gstore8_sc1(tag, one, srdH, syncb + 128 * (17 + rank));
        if constexpr (o.kind == K_POLL) gload8_sc1(fl, fo, srdH, syncb + 2048);
        if constexpr (o.kind == K_H) issue_h(o.n, o.i);
      }
    });
  };
  sfor<0, 14>([&](auto S) INL {
    constexpr int s = S.v;
    if constexpr (s == Y1 || s == FCHK || s == Y2 || s == Y3) {
      constexpr int n = bar_wait(Q(s, 0));
      static_assert(n >= 0 && n < 24, "wait count");
      if constexpr (s == FCHK) {
        asm volatile("s_waitcnt vmcnt(%1)" : "+v"(fl) : "i"(n) : "memory");
        if (w == 0) { if (__builtin_expect(!flags_ok(), 0)) poll(2048); }
      } else wait_vm<n>();
      BAR();
      if (s == Y1) { ST(13) } if (s == FCHK) { ST(14) } if (s == Y2) { ST(15) } if (s == Y3) { ST(16) }
      if constexpr (s == Y3) { if (rank == 7 && w == 0) gstore8_sc1(tag, one, srdH, syncb); }
    }
    vmem2(IC<Q(s, 0)>{});
    asm volatile("s_waitcnt vmcnt(%2)" : "+v"(hf[s][0]), "+v"(hf[s][1]) : "i"(younger_than(K_H, s, 1, Q(s, 1))) : "memory");
    if (s == 0) { ST(17) }
    SB();
    sfor<0, 4>([&](auto A) INL {
      constexpr int a = A.v;
      if constexpr (s == 0) mfma_new(acc2[0][a], join(df[0][a]), hf[0][0]); else mfma_acc(acc2[0][a], join(df[s & 1][a]), hf[s][0]);
      SB();
      if constexpr (s + 1 < 14) df[(s + 1) & 1][a].lo = ldd(s + 1, a, 0);
      SB();
      if constexpr (s == 0) mfma_new(acc2[1][a], join(df[0][a]), hf[0][1]); else mfma_acc(acc2[1][a], join(df[s & 1][a]), hf[s][1]);
      SB();
      if constexpr (s + 1 < 14) df[(s + 1) & 1][a].hi = ldd(s + 1, a, 1);
      vmem2(IC<Q(s, a + 1)>{});
      if constexpr (s == 13 && a == 3) { asm volatile("s_nop 1" ::: "memory"); SB(); wpart(0); SB(); wpart(1); }
      SB();
    });
  });
  ST(18)
  wpart(2);
  SB();
  asm volatile("s_nop 1" ::: "memory");
  SB();
  wpart(3);
  wait_lgkm0();
  BAR();
  ST(19)
  {
    f32x4 p0[4], p1[4];
#pragma unroll
    for (int v = 0; v < 4; ++v) {
      const unsigned ad = v == 3 ? ra3 : ra + v * (64 * 272);
      p0[v] = *(LDS_AS f32x4*)(uintptr_t)(ad);
      p1[v] = *(LDS_AS f32x4*)(uintptr_t)(ad + 16);
    }
    SB();
    const f32x4 s0 = (p0[0] + p0[1]) + (p0[2] + p0[3]), s1 = (p1[0] + p1[1]) + (p1[2] + p1[3]);
    u32x4 o = {pack_bf16(s0[0], s0[1]), pack_bf16(s0[2], s0[3]), pack_bf16(s1[0], s1[1]), pack_bf16(s1[2], s1[3])};
    asm volatile("buffer_store_dwordx4 %0, %1, %2, %3 offen" ::"v"(o), "v"(vo), "s"(srdO), "s"(so) : "memory");
  }
  ST(20)
}

extern "C" void kernel_launch(void* const* d_in, const int* in_sizes, int n_in, void* d_out, int out_size,
                              void* d_ws, size_t ws_size, hipStream_t stream) {
  (void)in_sizes; (void)n_in; (void)out_size; (void)ws_size;
  (void)hipFuncSetAttribute((const void*)k_fused, hipFuncAttributeMaxDynamicSharedMemorySize, LDS_TOTAL);
  k_fused<<<256, 512, LDS_TOTAL, stream>>>(d_in[0], d_in[1], d_in[2], d_in[3], d_out, d_ws);
}
```

```cpp
#include <hip/hip_runtime.h>
#include <hip/hip_bf16.h>
#include <stdint.h>

typedef __attribute__((ext_vector_type(8))) __bf16 bf16x8;
typedef __attribute__((ext_vector_type(4))) short s16x4;
typedef __attribute__((ext_vector_type(8))) short s16x8;
typedef __attribute__((ext_vector_type(2))) __bf16 bf16x2;
typedef __attribute__((ext_vector_type(2))) float f32x2;
typedef __attribute__((ext_vector_type(4))) float f32x4;
typedef __attribute__((ext_vector_type(4))) unsigned u32x4;
typedef __attribute__((ext_vector_type(2))) unsigned u32x2;
#define LDS_AS __attribute__((address_space(3)))

constexpr int M_ = 2048, D_ = 512, F_ = 1792;
constexpr unsigned SYNC_OFF = 8u << 20;
constexpr unsigned SPIN_CAP = 1u << 17;
constexpr unsigned CODE_BYTES = 18000;

__device__ __forceinline__ unsigned rfl(unsigned v) { return __builtin_amdgcn_readfirstlane(v); }

__device__ __forceinline__ u32x4 make_srd(const void* p, unsigned bytes) {
  uint64_t a = (uint64_t)p;
  u32x4 r;
  r[0] = rfl((unsigned)a);
  r[1] = rfl((unsigned)(a >> 32)) & 0xffffu;
  r[2] = bytes;
  r[3] = 0x00020000u;
  return r;
}

template <int IMM>
__device__ __forceinline__ void dma16(unsigned m0v, unsigned voff, u32x4 srd, unsigned soff) {
  asm volatile("s_mov_b32 m0, %0\n\ts_nop 0\n\tbuffer_load_dwordx4 %1, %2, %3 offen offset:%4 lds"
               ::"s"(m0v), "v"(voff), "s"(srd), "s"(soff), "i"(IMM) : "memory");
}
template <int IMM>
__device__ __forceinline__ void dma16n(unsigned voff, u32x4 srd, unsigned soff) {
  asm volatile("buffer_load_dwordx4 %0, %1, %2 offen offset:%3 lds" ::"v"(voff), "s"(srd), "s"(soff), "i"(IMM) : "memory");
}
__device__ __forceinline__ void set_m0(unsigned v) { asm volatile("s_mov_b32 m0, %0" ::"s"(v) : "memory"); }
__device__ __forceinline__ unsigned opq(unsigned v) { asm volatile("" : "+v"(v)); return v; }
__device__ __forceinline__ unsigned opqs(unsigned v) { asm volatile("" : "+s"(v)); return v; }
__device__ __forceinline__ void gload16_sc1(u32x4& dst, unsigned voff, u32x4 srd, unsigned soff) {
  asm volatile("buffer_load_dwordx4 %0, %1, %2, %3 offen sc1" : "=v"(dst) : "v"(voff), "s"(srd), "s"(soff) : "memory");
}
__device__ __forceinline__ void gstore16_sc1(u32x4 v, unsigned voff, u32x4 srd, unsigned soff) {
  asm volatile("buffer_store_dwordx4 %0, %1, %2, %3 offen sc1\n\ts_nop 1" ::"v"(v), "v"(voff), "s"(srd), "s"(soff) : "memory");
}
__device__ __forceinline__ void gload8_sc1(u32x2& dst, unsigned voff, u32x4 srd, unsigned soff) {
  asm volatile("buffer_load_dwordx2 %0, %1, %2, %3 offen sc1" : "=v"(dst) : "v"(voff), "s"(srd), "s"(soff) : "memory");
}
__device__ __forceinline__ void gstore8_sc1(u32x2 v, unsigned voff, u32x4 srd, unsigned soff) {
  asm volatile("buffer_store_dwordx2 %0, %1, %2, %3 offen sc1" ::"v"(v), "v"(voff), "s"(srd), "s"(soff) : "memory");
}

__device__ __forceinline__ s16x4 tr_read(unsigned addr) {
  return __builtin_amdgcn_ds_read_tr16_b64_v4i16((LDS_AS s16x4*)(uintptr_t)addr);
}
struct Frag { s16x4 lo, hi; };
__device__ __forceinline__ bf16x8 join(const Frag& f) {
  s16x8 v = __builtin_shufflevector(f.lo, f.hi, 0, 1, 2, 3, 4, 5, 6, 7);
  return __builtin_bit_cast(bf16x8, v);
}
__device__ __forceinline__ bf16x8 lds_read16(unsigned addr) { return *(LDS_AS bf16x8*)(uintptr_t)addr; }
__device__ __forceinline__ unsigned pack_bf16(float a, float b) {
  f32x2 v = {a, b};
  bf16x2 r = __builtin_convertvector(v, bf16x2);
  return __builtin_bit_cast(unsigned, r);
}
template <class TB>
__device__ __forceinline__ void mfma_acc(f32x4& c, bf16x8 a, TB b) {
  asm volatile("v_mfma_f32_16x16x32_bf16 %0, %1, %2, %0" : "+v"(c) : "v"(a), "v"(b));
}
template <class TB>
__device__ __forceinline__ void mfma_new(f32x4& c, bf16x8 a, TB b) {
  asm volatile("v_mfma_f32_16x16x32_bf16 %0, %1, %2, 0" : "=&v"(c) : "v"(a), "v"(b));
}

template <int I> struct IC { static constexpr int v = I; };
template <int I, int N, class F> __device__ __forceinline__ void sfor(F&& f) {
  if constexpr (I < N) { f(IC<I>{}); sfor<I + 1, N>(f); }
}
template <int N> __device__ __forceinline__ void wait_vm() { asm volatile("s_waitcnt vmcnt(%0)" ::"i"(N) : "memory"); }
__device__ __forceinline__ void wait_lgkm0() { asm volatile("s_waitcnt lgkmcnt(0)" ::: "memory"); }
#define BAR() asm volatile("s_barrier" ::: "memory")
#define SB() __builtin_amdgcn_sched_barrier(0)
#define INL __attribute__((always_inline))

constexpr int UNIT = 16384, LDS_TOTAL = 10 * UNIT;
constexpr int uaddr(int n) { return (n % 10) * UNIT; }


enum { K_DMA = 1, K_STORE, K_FLAG, K_POLL, K_H };
struct Op { int pos, kind, n, i; };
struct Sched { int cnt; Op op[200]; };
constexpr void put(Sched& s, int pos, int kind, int n, int i) { s.op[s.cnt++] = Op{pos, kind, n, i}; }
constexpr void spread(Sched& s, int p0, int p1, int u0, int nu) {
  for (int k = 0; k < 2 * nu; ++k) put(s, p0 + (k * (p1 - p0)) / (2 * nu), K_DMA, u0 + (k >> 1), k & 1);
}
constexpr int Q(int step, int slot) { return 200 + 8 * step + slot; }
constexpr void pair(Sched& s, int step, int slot, int kind, int n) { put(s, Q(step, slot), kind, n, 0); put(s, Q(step, slot == 0 ? 0 : slot + 1), kind, n, 1); }
constexpr int HB = 3;
constexpr int Y1 = 2, Y2 = 5, FCHK = 7, Y3 = 10;
constexpr Sched make_sched() {
  Sched s{};
  for (int n = 0; n < 4; ++n) { put(s, -1, K_DMA, n, 0); put(s, -1, K_DMA, n, 1); }
  put(s, -1, K_POLL, 0, 0);
  spread(s, 0, 2, 4, 2);
  for (int t = 0; t < 7; ++t) spread(s, 8 * t + 2, 8 * t + 9, 2 * t + 6, 2);
  spread(s, 58, 66, 20, 2);
  put(s, 67, K_STORE, 0, 0);
  put(s, 69, K_STORE, 0, 1);
  spread(s, 72, 80, 22, 1);
  spread(s, 82, 89, 23, 2);
  put(s, 90, K_FLAG, 0, 0);
  spread(s, 91, 103, 25, 2);
  spread(s, 103, 111, 27, 2);
  put(s, 111, K_POLL, 1, 0);
  spread(s, 112, 121, 29, 2);
  for (int k = 0; k < 4; ++k) put(s, 122 + k, K_H, k >> 1, k & 1);
  put(s, 128, K_STORE, 1, 0);
  put(s, 128, K_STORE, 1, 1);
  put(s, 128, K_H, 2, 0);
  put(s, 128, K_H, 2, 1);
  put(s, Q(Y1, 0), K_FLAG, 1, 0);
  pair(s, 2, 0, K_H, 3); pair(s, 2, 0, K_H, 4);
  pair(s, 2, 3, K_DMA, 31);
  pair(s, 3, 1, K_H, 5); pair(s, 3, 3, K_H, 6);
  pair(s, 4, 1, K_DMA, 32); pair(s, 4, 3, K_DMA, 33);
  pair(s, 5, 1, K_DMA, 34); pair(s, 5, 3, K_DMA, 35);
  put(s, Q(6, 0), K_POLL, 2, 0);
  pair(s, 6, 1, K_DMA, 36); pair(s, 6, 3, K_DMA, 37);
  for (int k = 0; k < HB; ++k) pair(s, 7, 0, K_H, 7 + k);
  for (int k = 7 + HB; k < 14; ++k) pair(s, k - HB, 1, K_H, k);
  return s;
}
constexpr Sched SCH = make_sched();
constexpr int younger_than(int kind, int n, int i, int P) {
  int c = -1;
  for (int k = 0; k < SCH.cnt; ++k) {
    if (SCH.op[k].pos >= P) break;
    if (SCH.op[k].kind == kind && SCH.op[k].n == n && SCH.op[k].i == i) c = 0;
    else if (c >= 0 && SCH.op[k].kind != K_FLAG && !(SCH.op[k].kind == K_POLL && SCH.op[k].n > 0)) ++c;
  }
  return c;
}
constexpr int first_op(int P) { int k = 0; while (k < SCH.cnt && SCH.op[k].pos < P) ++k; return k; }
constexpr bool is_bar(int P) { return P < 64 ? (P & 7) == 2 : (P & 15) == 10; }
constexpr int bar_wait(int P) {
  if (P < 64) return younger_than(K_DMA, 2 * (P >> 3) + 3, 1, P);
  if (P == 74) return younger_than(K_DMA, 19, 1, P);
  if (P == 90) return younger_than(K_STORE, 0, 1, P);
  if (P == 106) return younger_than(K_DMA, 23, 1, P);
  if (P == 122) return younger_than(K_POLL, 1, 0, P);
  if (P == Q(Y1, 0)) return 0;
  if (P == Q(Y2, 0)) return younger_than(K_DMA, 31, 1, P);
  if (P == Q(FCHK, 0)) return younger_than(K_POLL, 2, 0, P);
  return younger_than(K_DMA, 37, 1, P);
}
constexpr bool sched_sorted() { for (int k = 1; k < SCH.cnt; ++k) if (SCH.op[k].pos < SCH.op[k - 1].pos) return false; return true; }
static_assert(sched_sorted(), "schedule order");

__global__ __launch_bounds__(512, 2) void k_fused(const void* __restrict__ x, const void* __restrict__ Wg,
                                                  const void* __restrict__ Wu, const void* __restrict__ Wd,
                                                  void* __restrict__ out, void* __restrict__ ws) {
  extern __shared__ __attribute__((aligned(16))) char smem[];
  const unsigned lds0 = (unsigned)(uintptr_t)(LDS_AS char*)smem;
  const unsigned bid = blockIdx.x;
  const unsigned w = rfl(threadIdx.x >> 6);
  const bool busy = bid < 224;
  const unsigned mi = (bid & 6) * 2 + ((bid >> 3) & 3), half = bid & 1, idx = bid >> 5;
  const unsigned rot = ((bid & 6) + 4 * half) & 7;
  unsigned vw0, vw1, vx0, vx1;
  {
    const unsigned lane = threadIdx.x & 63, g = lane >> 4;
    vw0 = (lane >> 3) * 3584 + (((lane & 7) ^ ((g & 1) << 1)) << 4);
    vw1 = (vw0 ^ 64) + 8 * 3584 - 1024;
    vx0 = (lane >> 3) * 1024 + (((lane & 7) ^ g) << 4);
    vx1 = (vx0 ^ 64) + 8192 - 1024;
  }
  unsigned sx0 = (mi * 128 + 16 * w) * 1024;
  unsigned sw0 = (16 * (w & 3)) * 3584 + (half * 14 + idx) * 128;
  unsigned mu0 = lds0 + w * 2048;
  unsigned sw00 = sw0 + rot * (64 * 3584), sx00 = sx0 + rot * 128;
  asm volatile("" : "+v"(vw0), "+v"(vw1), "+v"(vx0), "+v"(vx1));
  asm volatile("" : "+s"(sx0), "+s"(sw0), "+s"(mu0), "+s"(sw00), "+s"(sx00));
  SB();
  const u32x4 srdX = make_srd(x, M_ * D_ * 2);
  const u32x4 srdW = make_srd((w >> 2) ? Wu : Wg, D_ * F_ * 2);
  auto issue_xw = [&](int n, int i, bool pre = false) {
    const int s = n < 16 ? n >> 1 : n - 16;
    const unsigned sg = (s + rot) & 7;
    if (n < 16 && (n & 1) == 0) {
      const unsigned so = n == 0 ? sx00 : sx0 + sg * 128;
      if (i == 0) { if (pre) dma16n<0>(vx0, srdX, so); else dma16<0>(mu0 + uaddr(n), vx0, srdX, so); } else dma16n<1024>(vx1, srdX, so);
    } else {
      const unsigned so = n == 1 ? sw00 : sw0 + sg * (64 * 3584) + (n >= 16 ? 896 : 0);
      if (i == 0) { if (pre) dma16n<0>(vw0, srdW, so); else dma16<0>(mu0 + uaddr(n), vw0, srdW, so); } else dma16n<1024>(vw1, srdW, so);
    }
  };
  asm volatile("" ::"s"(Wd), "s"(out), "s"(ws));
  if (busy) {
    issue_xw(0, 0); issue_xw(0, 1); issue_xw(1, 0); issue_xw(1, 1);
    BAR();
    issue_xw(2, 0); issue_xw(2, 1); issue_xw(3, 0); issue_xw(3, 1);
  } else {
    uint64_t pc;
    asm volatile("s_getpc_b64 %0" : "=s"(pc));
    const unsigned o = min(threadIdx.x * 64u, CODE_BYTES);
    asm volatile("global_load_dword v249, %0, %1" ::"v"(o), "s"(pc) : "v249", "memory");
  }
  SB();
  const unsigned tid = opq(threadIdx.x), lane = tid & 63;
  const unsigned bid2 = opqs(bid), xcd = bid2 & 7, ml = (bid2 >> 3) & 3;
  unsigned rank = half * 8 + idx;
  const unsigned m64 = 2 * mi + half, n64 = idx;
  const unsigned g = lane >> 4, q = (lane & 15) >> 2, p = lane & 3;
  const u32x4 srdH = make_srd(ws, SYNC_OFF + 16 * 8192);
  const u32x4 srdD = make_srd(Wd, F_ * D_ * 2);
  unsigned syncb = SYNC_OFF + mi * 8192;
  const unsigned lane16 = lane * 16;

  const unsigned kq = w >> 1, mb = w & 1;
  unsigned rot2 = 2 * ml + idx; rot2 = rot2 >= 7 ? rot2 - 7 : rot2;
  unsigned kb[7];
  auto set_kb = [&]() {
    unsigned e = opqs(rot2);
#pragma unroll
    for (int j = 0; j < 7; ++j) {
      kb[j] = opqs((e + 7 * kq + (kq >> 1) * 14) * 1024);
      e = e == 6 ? 0 : e + 1;
    }
  };
  auto kap = [&](int s) { return kb[s % 7] + (s >= 7 ? 14 * 1024 : 0); };
  const unsigned vd0 = (lane >> 3) * 1024 + (((lane & 7) ^ (g << 1)) << 4);
  const unsigned vd1 = vd0 + 8192 - 1024;
  unsigned sd0 = (16 * mb) * 1024 + n64 * 128;
  auto issue_wd = [&](int s, int i, bool pre = false) {
    if (i == 0) { if (pre) dma16n<0>(vd0, srdD, sd0 + kap(s) * 32); else dma16<0>(mu0 + uaddr(24 + s), vd0, srdD, sd0 + kap(s) * 32); }
    else dma16n<1024>(vd1, srdD, sd0 + kap(s) * 32);
  };
  const unsigned dl = lds0 + kq * 4096 + (4 * g + q) * 128 + (((2 * g + (q >> 1)) & 3) << 5) + p * 8;
  unsigned db[3][4];
  auto set_db = [&](int k) {
#pragma unroll
    for (int c = 0; c < 4; ++c) db[k][c] = opq((dl ^ (c << 5)) + k * 4 * UNIT);
  };
  auto ldd = [&](int s, int a, int u) {
    const int ua = uaddr(24 + s);
    return tr_read(db[ua >> 16][a] + (ua & 65535) + u * 2048);
  };
  u32x2 ep, tag, fl;
  auto mktag = [&]() {
    const uint64_t t = (((uint64_t)ep[1] << 32) | ep[0]) * 0x9E3779B97F4A7C15ull + 1ull;
    tag = u32x2{(unsigned)t, (unsigned)(t >> 32)};
  };
  const unsigned one = tid == 0 ? 0u : 0x40000000u;
  const unsigned fo = tid < 16 ? 128 * (1 + tid) : 0x40000000u;
  unsigned fmask = 0x7F7Fu;
  auto flags_ok = [&]() { return (rfl((unsigned)__builtin_amdgcn_ballot_w64(fl[0] == tag[0] && fl[1] == tag[1])) & fmask) == fmask; };
  auto poll = [&](unsigned off) {
    unsigned cnt = SPIN_CAP, t;
#define PLD(r) "buffer_load_dwordx2 " r ", %2, %3, %4 offen sc1\n\t"
#define PCK(r) "s_waitcnt vmcnt(2)\n\tv_cmp_eq_u64 vcc, " r ", %5\n\ts_nop 1\n\ts_and_b32 %1, vcc_lo, %6\n\ts_cmp_eq_u32 %1, %6\n\ts_cbranch_scc1 2f\n\t"
    asm volatile(PLD("v[250:251]") "s_sleep 3\n\t" PLD("v[252:253]") "s_sleep 3\n\t" PLD("v[254:255]")
                 "1:\n\t"
                 PCK("v[250:251]") PLD("v[250:251]") PCK("v[252:253]") PLD("v[252:253]") PCK("v[254:255]") PLD("v[254:255]")
                 "s_sub_u32 %0, %0, 1\n\ts_cbranch_scc0 1b\n\t"
                 "2:"
                 : "+s"(cnt), "=&s"(t) : "v"(fo), "s"(srdH), "s"(syncb + off), "v"(tag), "s"(fmask)
                 : "v250", "v251", "v252", "v253", "v254", "v255", "vcc", "scc", "memory");
#undef PLD
#undef PCK
  };
  auto poll_slow = [&](unsigned off) {
#pragma nounroll
    for (unsigned spin = 0; spin < SPIN_CAP; ++spin) {
      gload8_sc1(fl, fo, srdH, syncb + off);
      asm volatile("s_waitcnt vmcnt(0)" : "+v"(fl)::"memory");
      if (flags_ok()) break;
      __builtin_amdgcn_s_sleep(8);
    }
  };
  Frag df[2][4];
  u32x4 hf[14][2];
  unsigned sh2 = ((m64 * 4 + mb * 2) * 56) * 1024;
  const unsigned lane16b = lane16 + 56 * 1024;
  auto issue_h = [&](int s, int b) { gload16_sc1(hf[s][b], b ? lane16b : lane16, srdH, sh2 + kap(s)); };

  set_kb();
  if (__builtin_expect(busy, 1)) {
    gload8_sc1(ep, 0u, srdH, syncb);
    const unsigned wm = w & 3, wn = w >> 2;
    const unsigned xrow = 32 * wm + (lane & 15);
    const unsigned xa0 = lds0 + xrow * 128 + ((g ^ ((xrow >> 1) & 7)) << 4);
    const unsigned wa0 = lds0 + (8 * g + q) * 128 + (((2 * wn) ^ ((q >> 1) | ((g & 1) << 1))) << 5) + p * 8;
    unsigned xa[3][2], wa[3][2];
#pragma unroll
    for (int k = 0; k < 3; ++k)
#pragma unroll
      for (int c = 0; c < 2; ++c) { xa[k][c] = opq((xa0 ^ (c << 6)) + k * 4 * UNIT); wa[k][c] = opq((wa0 ^ (c << 5)) + k * 4 * UNIT); }

    f32x4 acc[2][2][4];
    bf16x8 xf[8][2][2];
    Frag wf[4][2];
    auto ldx = [&](int s, int b, int kk) { const int ua = uaddr(2 * s); xf[s][b][kk] = lds_read16(xa[ua >> 16][kk] + (ua & 65535) + b * 2048); };
    auto ldw = [&](int G, int kk, int u = 2) {
      const int st = G >> 2, a = G & 3, ua = uaddr(st < 8 ? 2 * st + 1 : 8 + st);
      const unsigned ad = wa[ua >> 16][a & 1] + (ua & 65535) + (a >> 1) * 8192 + kk * 4096;
      if (u != 1) wf[G & 3][kk].lo = tr_read(ad);
      if (u != 0) wf[G & 3][kk].hi = tr_read(ad + 512);
    };
    const unsigned sh = ((mi * 8 + wm * 2) * 56 + half * 28 + idx * 2 + wn) * 1024;
    u32x4 ho[2];
    auto epi = [&](int ps, int b, int c) {
      const int t = c >> 1, i = c & 1;
      float g0 = __builtin_elementwise_maximum(acc[ps][b][t][2 * i], 0.f), g1 = __builtin_elementwise_maximum(acc[ps][b][t][2 * i + 1], 0.f);
      float p0 = g0 * acc[ps][b][2 + t][2 * i];
      asm volatile("" : "+v"(p0));
      ho[b][c] = pack_bf16(p0, g1 * acc[ps][b][2 + t][2 * i + 1]);
    };
    auto sth = [&](int ps, int b) { gstore16_sc1(ho[b], lane16, srdH, sh + b * (56 * 1024) + ps * (14 * 1024)); };

    auto vmem = [&](auto PP) INL {
      sfor<first_op(PP.v), first_op(PP.v + 1)>([&](auto K) INL {
        constexpr Op o = SCH.op[K.v];
        if constexpr (o.pos == PP.v) {
          constexpr bool pre = PP.v < 128 && K.v == first_op(PP.v);
          if constexpr (o.kind == K_DMA) { if constexpr (o.n < 24) issue_xw(o.n, o.i, pre); else issue_wd(o.n - 24, o.i, pre); }
          if constexpr (o.kind == K_STORE) sth(o.n, o.i);
          if constexpr (o.kind == K_FLAG) gstore8_sc1(tag, one, srdH, syncb + 128 * (1 + rank));
          if constexpr (o.kind == K_POLL) gload8_sc1(fl, fo, srdH, syncb);
          if constexpr (o.kind == K_H) issue_h(o.n, o.i);
        }
      });
    };
    wait_vm<5>();
    BAR();
    ldw(0, 0); ldx(0, 0, 0); ldw(0, 1); ldx(0, 0, 1); ldx(0, 1, 0); ldx(0, 1, 1); ldw(1, 0); ldw(1, 1); ldw(2, 0); ldw(2, 1);
    SB();
    sfor<0, 128>([&](auto Z) INL {
      constexpr int P = Z.v, ps = P >> 6, s = (P >> 3) & 7, G = P >> 1, a = G & 3, b = P & 1;
      if constexpr (is_bar(P)) {
        constexpr int n = bar_wait(P);
        static_assert(n >= 0 && n < 24, "wait count");
        if constexpr (P == 10) asm volatile("s_waitcnt vmcnt(%1)" : "+v"(ep) : "i"(n) : "memory");
        else if constexpr (P == 122) {
          asm volatile("s_waitcnt vmcnt(%1)" : "+v"(fl) : "i"(n) : "memory");
          if (w == 0) { if (__builtin_expect(!flags_ok(), 0)) poll(0); }
        } else wait_vm<n>();
        BAR();
      }
      if constexpr (P < 58 ? (P & 1) == 0 : (P & 3) == 2) __builtin_amdgcn_s_setprio(P < 58 ? 3 - (((P + 6) & 7) >> 1) : 3 - (((P + 6) & 15) >> 2));
      if constexpr (s == 0) mfma_new(acc[ps][b][a], join(wf[G & 3][0]), xf[s][b][0]); else mfma_acc(acc[ps][b][a], join(wf[G & 3][0]), xf[s][b][0]);
      SB();
      if constexpr (G + 3 < 64) ldw(G + 3, b, 0);
      if constexpr (P >= 124) df[0][P - 124].lo = ldd(0, P - 124, 0);
      {
        constexpr int k0 = first_op(P);
        if constexpr (k0 < first_op(P + 1) && SCH.op[k0].kind == K_DMA && SCH.op[k0].i == 0) set_m0(mu0 + uaddr(SCH.op[k0].n));
        else if constexpr (P < 56 && (P & 7) >= 2 && (P & 7) < 6) ldx(s + 1, ((P - 2) >> 1) & 1, P & 1);
      }
      SB();
      mfma_acc(acc[ps][b][a], join(wf[G & 3][1]), xf[s][b][1]);
      SB();
      if constexpr (G + 3 < 64) ldw(G + 3, b, 1);
      if constexpr (P >= 124) df[0][P - 124].hi = ldd(0, P - 124, 1);
      {
        constexpr int k0 = first_op(P);
        if constexpr (k0 < first_op(P + 1) && SCH.op[k0].kind == K_DMA && SCH.op[k0].i == 0 && P < 56 && (P & 7) >= 2 && (P & 7) < 6) ldx(s + 1, ((P - 2) >> 1) & 1, P & 1);
      }
      if constexpr (P == 120) set_db(1);
      if constexpr (P == 65) { epi(0, 0, 0); epi(0, 0, 1); }
      if constexpr (P == 66) { epi(0, 1, 0); epi(0, 1, 1); }
      if constexpr (P == 67) { epi(0, 0, 2); epi(0, 0, 3); }
      if constexpr (P == 68 || P == 69) epi(0, 1, P - 66);
      if constexpr (P == 84) mktag();
      vmem(Z);
      SB();
    });
    set_db(0); set_db(2);
    epi(1, 0, 0); epi(1, 0, 1); epi(1, 1, 0); epi(1, 1, 1);
    SB();
    epi(1, 0, 2); epi(1, 0, 3); epi(1, 1, 2); epi(1, 1, 3);
    vmem(IC<128>{});
    __builtin_amdgcn_s_setprio(0);
  } else {
    gload8_sc1(ep, 0u, srdH, syncb);
    set_db(0); set_db(1); set_db(2);
    {
      unsigned t = 0;
      {
        const unsigned st = (rot + 2 + (lane & 3)) & 7;
        const unsigned vxo = (lane & 3) < 2 ? (mi * 128 + w * 16 + (lane >> 2)) * 1024 + st * 128 : 0x40000000u;
        asm volatile("buffer_load_dword %0, %1, %2, 0 offen" : "+v"(t) : "v"(vxo), "s"(srdX) : "memory");
      }
      {
        const unsigned li = (ml * 4 + (w & 3)) * 56 + lane;
        const unsigned row = li / 7, c = li - row * 7;
        const unsigned vo = lane < 56 ? (rot * 64 + row) * 3584 + (half * 14 + 7 + c) * 128 : 0x40000000u;
        asm volatile("buffer_load_dword %0, %1, %2, 0 offen" : "+v"(t) : "v"(vo), "s"(srdW) : "memory");
      }
#pragma unroll
      for (int i = 0; i < 3; ++i) {
        unsigned c = i + xcd; c = c >= 7 ? c - 7 : c; c = c >= 7 ? c - 7 : c;
        asm volatile("buffer_load_dword %0, %1, %2, %3 offen" : "+v"(t) : "v"(lane * 128), "s"(srdD), "s"(((c * 4 + ml) * 8 + w) * 8192) : "memory");
      }
      asm volatile("s_waitcnt vmcnt(0)" : "+v"(t), "+v"(ep)::"memory");
      mktag();
    }
#pragma unroll
    for (int s = 0; s < 7; ++s) { issue_wd(s, 0); issue_wd(s, 1); }
    if (w == 0) poll_slow(0); else wait_vm<0>();
    BAR();
#pragma unroll
    for (int s = 0; s < 3; ++s) { issue_h(s, 0); issue_h(s, 1); }
#pragma unroll
    for (int a = 0; a < 4; ++a) { df[0][a].lo = ldd(0, a, 0); df[0][a].hi = ldd(0, a, 1); }
    wait_vm<0>();
  }
  asm volatile("" : "+s"(mu0), "+s"(sd0), "+s"(sh2), "+s"(syncb), "+s"(rank));
  asm volatile("" : "+s"(kb[0]), "+s"(kb[1]), "+s"(kb[2]), "+s"(kb[3]), "+s"(kb[4]), "+s"(kb[5]), "+s"(kb[6]));
  fmask = rank == 7 ? 0xFFFFu : 0x7F7Fu;
  f32x4 acc2[2][4];
  const unsigned pa = opq(lds0 + (kq == 3 ? 8 * UNIT : kq * (64 * 272)) + (mb * 32 + (lane & 15)) * 272 + g * 16);
  const unsigned ra = opq(lds0 + (w * 8 + (lane >> 3)) * 272 + (lane & 7) * 32), ra3 = opq(ra + 8 * UNIT);
  const unsigned vo = opq((w * 8 + (lane >> 3)) * 1024 + (lane & 7) * 16), so = opqs(m64 * 65536 + n64 * 128);
  const u32x4 srdO = make_srd(out, M_ * D_ * 2);
  auto wpart = [&](int a) {
    *(LDS_AS f32x4*)(uintptr_t)(pa + a * 64) = acc2[0][a];
    *(LDS_AS f32x4*)(uintptr_t)(pa + 16 * 272 + a * 64) = acc2[1][a];
  };
  auto vmem2 = [&](auto PP) INL {
    sfor<first_op(PP.v), first_op(PP.v + 1)>([&](auto K) INL {
      constexpr Op o = SCH.op[K.v];
      if constexpr (o.pos == PP.v) {
        if constexpr (o.kind == K_DMA) issue_wd(o.n - 24, o.i);
        if constexpr (o.kind == K_FLAG) gstore8_sc1(tag, one, srdH, syncb + 128 * (17 + rank));
        if constexpr (o.kind == K_POLL) gload8_sc1(fl, fo, srdH, syncb + 2048);
        if constexpr (o.kind == K_H) issue_h(o.n, o.i);
      }
    });
  };
  sfor<0, 14>([&](auto S) INL {
    constexpr int s = S.v;
    if constexpr (s == Y1 || s == FCHK || s == Y2 || s == Y3) {
      constexpr int n = bar_wait(Q(s, 0));
      static_assert(n >= 0 && n < 24, "wait count");
      if constexpr (s == FCHK) {
        asm volatile("s_waitcnt vmcnt(%1)" : "+v"(fl) : "i"(n) : "memory");
        if (w == 0) { if (__builtin_expect(!flags_ok(), 0)) poll(2048); }
      } else wait_vm<n>();
      BAR();
      if constexpr (s == Y3) { if (rank == 7 && w == 0) gstore8_sc1(tag, one, srdH, syncb); }
    }
    vmem2(IC<Q(s, 0)>{});
    if constexpr (s == Y1) asm volatile("" : "+v"(hf[s][0]), "+v"(hf[s][1])::"memory");
    else asm volatile("s_waitcnt vmcnt(%2)" : "+v"(hf[s][0]), "+v"(hf[s][1]) : "i"(younger_than(K_H, s, 1, Q(s, 1))) : "memory");
    SB();
    sfor<0, 4>([&](auto A) INL {
      constexpr int a = A.v;
      if constexpr (s == 0) mfma_new(acc2[0][a], join(df[0][a]), hf[0][0]); else mfma_acc(acc2[0][a], join(df[s & 1][a]), hf[s][0]);
      SB();
      if constexpr (s + 1 < 14) df[(s + 1) & 1][a].lo = ldd(s + 1, a, 0);
      SB();
      if constexpr (s == 0) mfma_new(acc2[1][a], join(df[0][a]), hf[0][1]); else mfma_acc(acc2[1][a], join(df[s & 1][a]), hf[s][1]);
      SB();
      if constexpr (s + 1 < 14) df[(s + 1) & 1][a].hi = ldd(s + 1, a, 1);
      vmem2(IC<Q(s, a + 1)>{});
      if constexpr (s == 13 && a == 3) { asm volatile("s_nop 1" ::: "memory"); SB(); wpart(0); SB(); wpart(1); }
      SB();
    });
  });
  wpart(2);
  SB();
  asm volatile("s_nop 1" ::: "memory");
  SB();
  wpart(3);
  wait_lgkm0();
  BAR();
  {
    f32x4 p0[4], p1[4];
#pragma unroll
    for (int v = 0; v < 4; ++v) {
      const unsigned ad = v == 3 ? ra3 : ra + v * (64 * 272);
      p0[v] = *(LDS_AS f32x4*)(uintptr_t)(ad);
      p1[v] = *(LDS_AS f32x4*)(uintptr_t)(ad + 16);
    }
    SB();
    const f32x4 s0 = (p0[0] + p0[1]) + (p0[2] + p0[3]), s1 = (p1[0] + p1[1]) + (p1[2] + p1[3]);
    u32x4 o = {pack_bf16(s0[0], s0[1]), pack_bf16(s0[2], s0[3]), pack_bf16(s1[0], s1[1]), pack_bf16(s1[2], s1[3])};
    asm volatile("buffer_store_dwordx4 %0, %1, %2, %3 offen" ::"v"(o), "v"(vo), "s"(srdO), "s"(so) : "memory");
  }
}

extern "C" void kernel_launch(void* const* d_in, const int* in_sizes, int n_in, void* d_out, int out_size,
                              void* d_ws, size_t ws_size, hipStream_t stream) {
  (void)in_sizes; (void)n_in; (void)out_size; (void)ws_size;
  (void)hipFuncSetAttribute((const void*)k_fused, hipFuncAttributeMaxDynamicSharedMemorySize, LDS_TOTAL);
  k_fused<<<256, 512, LDS_TOTAL, stream>>>(d_in[0], d_in[1], d_in[2], d_in[3], d_out, d_ws);
}
```

```cpp
#include <hip/hip_runtime.h>
#include <hip/hip_bf16.h>
#include <stdint.h>

typedef __attribute__((ext_vector_type(8))) __bf16 bf16x8;
typedef __attribute__((ext_vector_type(4))) short s16x4;
typedef __attribute__((ext_vector_type(8))) short s16x8;
typedef __attribute__((ext_vector_type(2))) __bf16 bf16x2;
typedef __attribute__((ext_vector_type(2))) float f32x2;
typedef __attribute__((ext_vector_type(4))) float f32x4;
typedef __attribute__((ext_vector_type(4))) unsigned u32x4;
typedef __attribute__((ext_vector_type(2))) unsigned u32x2;
#define LDS_AS __attribute__((address_space(3)))

constexpr int M_ = 2048, D_ = 512, F_ = 1792;
constexpr unsigned SYNC_OFF = 8u << 20;
constexpr unsigned SPIN_CAP = 1u << 17;
constexpr unsigned CODE_BYTES = 18000;

__device__ __forceinline__ unsigned rfl(unsigned v) { return __builtin_amdgcn_readfirstlane(v); }

__device__ __forceinline__ u32x4 make_srd(const void* p, unsigned bytes) {
  uint64_t a = (uint64_t)p;
  u32x4 r;
  r[0] = rfl((unsigned)a);
  r[1] = rfl((unsigned)(a >> 32)) & 0xffffu;
  r[2] = bytes;
  r[3] = 0x00020000u;
  return r;
}

template <int IMM>
__device__ __forceinline__ void dma16(unsigned m0v, unsigned voff, u32x4 srd, unsigned soff) {
  asm volatile("s_mov_b32 m0, %0\n\ts_nop 0\n\tbuffer_load_dwordx4 %1, %2, %3 offen offset:%4 lds"
               ::"s"(m0v), "v"(voff), "s"(srd), "s"(soff), "i"(IMM) : "memory");
}
template <int IMM>
__device__ __forceinline__ void dma16n(unsigned voff, u32x4 srd, unsigned soff) {
  asm volatile("buffer_load_dwordx4 %0, %1, %2 offen offset:%3 lds" ::"v"(voff), "s"(srd), "s"(soff), "i"(IMM) : "memory");
}
__device__ __forceinline__ void set_m0(unsigned v) { asm volatile("s_mov_b32 m0, %0" ::"s"(v) : "memory"); }
__device__ __forceinline__ unsigned opq(unsigned v) { asm volatile("" : "+v"(v)); return v; }
__device__ __forceinline__ unsigned opqs(unsigned v) { asm volatile("" : "+s"(v)); return v; }
__device__ __forceinline__ void gload16_sc1(u32x4& dst, unsigned voff, u32x4 srd, unsigned soff) {
  asm volatile("buffer_load_dwordx4 %0, %1, %2, %3 offen sc1" : "=v"(dst) : "v"(voff), "s"(srd), "s"(soff) : "memory");
}
__device__ __forceinline__ void gstore16_sc1(u32x4 v, unsigned voff, u32x4 srd, unsigned soff) {
  asm volatile("buffer_store_dwordx4 %0, %1, %2, %3 offen sc1\n\ts_nop 1" ::"v"(v), "v"(voff), "s"(srd), "s"(soff) : "memory");
}
__device__ __forceinline__ void gload8_sc1(u32x2& dst, unsigned voff, u32x4 srd, unsigned soff) {
  asm volatile("buffer_load_dwordx2 %0, %1, %2, %3 offen sc1" : "=v"(dst) : "v"(voff), "s"(srd), "s"(soff) : "memory");
}
__device__ __forceinline__ void gstore8_sc1(u32x2 v, unsigned voff, u32x4 srd, unsigned soff) {
  asm volatile("buffer_store_dwordx2 %0, %1, %2, %3 offen sc1" ::"v"(v), "v"(voff), "s"(srd), "s"(soff) : "memory");
}

__device__ __forceinline__ s16x4 tr_read(unsigned addr) {
  return __builtin_amdgcn_ds_read_tr16_b64_v4i16((LDS_AS s16x4*)(uintptr_t)addr);
}
struct Frag { s16x4 lo, hi; };
__device__ __forceinline__ bf16x8 join(const Frag& f) {
  s16x8 v = __builtin_shufflevector(f.lo, f.hi, 0, 1, 2, 3, 4, 5, 6, 7);
  return __builtin_bit_cast(bf16x8, v);
}
__device__ __forceinline__ bf16x8 lds_read16(unsigned addr) { return *(LDS_AS bf16x8*)(uintptr_t)addr; }
__device__ __forceinline__ unsigned pack_bf16(float a, float b) {
  f32x2 v = {a, b};
  bf16x2 r = __builtin_convertvector(v, bf16x2);
  return __builtin_bit_cast(unsigned, r);
}
template <class TB>
__device__ __forceinline__ void mfma_acc(f32x4& c, bf16x8 a, TB b) {
  asm volatile("v_mfma_f32_16x16x32_bf16 %0, %1, %2, %0" : "+v"(c) : "v"(a), "v"(b));
}
template <class TB>
__device__ __forceinline__ void mfma_new(f32x4& c, bf16x8 a, TB b) {
  asm volatile("v_mfma_f32_16x16x32_bf16 %0, %1, %2, 0" : "=&v"(c) : "v"(a), "v"(b));
}

template <int I> struct IC { static constexpr int v = I; };
template <int I, int N, class F> __device__ __forceinline__ void sfor(F&& f) {
  if constexpr (I < N) { f(IC<I>{}); sfor<I + 1, N>(f); }
}
template <int N> __device__ __forceinline__ void wait_vm() { asm volatile("s_waitcnt vmcnt(%0)" ::"i"(N) : "memory"); }
__device__ __forceinline__ void wait_lgkm0() { asm volatile("s_waitcnt lgkmcnt(0)" ::: "memory"); }
#define BAR() asm volatile("s_barrier" ::: "memory")
#define SB() __builtin_amdgcn_sched_barrier(0)
#define INL __attribute__((always_inline))

constexpr int UNIT = 16384, LDS_TOTAL = 10 * UNIT;
constexpr int uaddr(int n) { return (n % 10) * UNIT; }

enum { K_DMA = 1, K_STORE, K_FLAG, K_POLL, K_H };
struct Op { int pos, kind, n, i; };
struct Sched { int cnt; Op op[200]; };
constexpr void put(Sched& s, int pos, int kind, int n, int i) { s.op[s.cnt++] = Op{pos, kind, n, i}; }
constexpr void spread(Sched& s, int p0, int p1, int u0, int nu) {
  for (int k = 0; k < 2 * nu; ++k) put(s, p0 + (k * (p1 - p0)) / (2 * nu), K_DMA, u0 + (k >> 1), k & 1);
}
constexpr int Q(int step, int slot) { return 200 + 8 * step + slot; }
constexpr void pair(Sched& s, int step, int slot, int kind, int n) { put(s, Q(step, slot), kind, n, 0); put(s, Q(step, slot == 0 ? 0 : slot + 1), kind, n, 1); }
#define Y3V 10
constexpr int HB = 3;
constexpr int Y1 = 2, Y2 = 5, FCHK = 7, Y3 = Y3V;
constexpr Sched make_sched() {
  Sched s{};
  for (int n = 0; n < 4; ++n) { put(s, -1, K_DMA, n, 0); put(s, -1, K_DMA, n, 1); }
  put(s, -1, K_POLL, 0, 0);
  spread(s, 0, 2, 4, 2);
  for (int t = 0; t < 7; ++t) spread(s, 8 * t + 2, 8 * t + 9, 2 * t + 6, 2);
  spread(s, 58, 64, 20, 2);
  put(s, 65, K_STORE, 0, 0);
  put(s, 66, K_STORE, 0, 1);
  spread(s, 72, 80, 22, 1);
  spread(s, 82, 89, 23, 2);
  put(s, 90, K_FLAG, 0, 0);
  spread(s, 91, 103, 25, 2);
  spread(s, 103, 111, 27, 2);
  put(s, 111, K_POLL, 1, 0);
  spread(s, 112, 121, 29, 2);
  for (int k = 0; k < 4; ++k) put(s, 122 + k, K_H, k >> 1, k & 1);
  put(s, 128, K_STORE, 1, 0);
  put(s, 128, K_STORE, 1, 1);
  put(s, 128, K_H, 2, 0);
  put(s, 128, K_H, 2, 1);
  put(s, Q(Y1, 0), K_FLAG, 1, 0);
  pair(s, 2, 0, K_H, 3); pair(s, 2, 0, K_H, 4);
  pair(s, 2, 3, K_DMA, 31);
  pair(s, 3, 1, K_H, 5); pair(s, 3, 3, K_H, 6);
  pair(s, 4, 1, K_DMA, 32); pair(s, 4, 3, K_DMA, 33);
  pair(s, 5, 1, K_DMA, 34); pair(s, 5, 3, K_DMA, 35);
  put(s, Q(6, 0), K_POLL, 2, 0);
  pair(s, 6, 1, K_DMA, 36); pair(s, 6, 3, K_DMA, 37);
  for (int k = 0; k < HB; ++k) pair(s, 7, 0, K_H, 7 + k);
  for (int k = 7 + HB; k < 14; ++k) pair(s, k - HB, 1, K_H, k);
  return s;
}
constexpr Sched SCH = make_sched();
constexpr int younger_than(int kind, int n, int i, int P) {
  int c = -1;
  for (int k = 0; k < SCH.cnt; ++k) {
    if (SCH.op[k].pos >= P) break;
    if (SCH.op[k].kind == kind && SCH.op[k].n == n && SCH.op[k].i == i) c = 0;
    else if (c >= 0 && SCH.op[k].kind != K_FLAG && !(SCH.op[k].kind == K_POLL && SCH.op[k].n > 0)) ++c;
  }
  return c;
}
constexpr int first_op(int P) { int k = 0; while (k < SCH.cnt && SCH.op[k].pos < P) ++k; return k; }
constexpr bool is_bar(int P) { return P < 64 ? (P & 7) == 2 : (P & 15) == 10; }
constexpr int bar_wait(int P) {
  if (P < 64) return younger_than(K_DMA, 2 * (P >> 3) + 3, 1, P);
  if (P == 74) return younger_than(K_DMA, 19, 1, P);
  if (P == 90) return younger_than(K_STORE, 0, 1, P);
  if (P == 106) return younger_than(K_DMA, 23, 1, P);
  if (P == 122) return younger_than(K_POLL, 1, 0, P);
  if (P == Q(Y1, 0)) return 0;
  if (P == Q(Y2, 0)) return younger_than(K_DMA, 31, 1, P);
  if (P == Q(FCHK, 0)) return younger_than(K_POLL, 2, 0, P);
  return younger_than(K_DMA, 37, 1, P);
}
constexpr bool sched_sorted() { for (int k = 1; k < SCH.cnt; ++k) if (SCH.op[k].pos < SCH.op[k - 1].pos) return false; return true; }
static_assert(sched_sorted(), "schedule order");

__global__ __launch_bounds__(512, 2) void k_fused(const void* __restrict__ x, const void* __restrict__ Wg,
                                                  const void* __restrict__ Wu, const void* __restrict__ Wd,
                                                  void* __restrict__ out, void* __restrict__ ws) {
  extern __shared__ __attribute__((aligned(16))) char smem[];
  const unsigned lds0 = (unsigned)(uintptr_t)(LDS_AS char*)smem;
  const unsigned bid = blockIdx.x;
  const unsigned w = rfl(threadIdx.x >> 6);
  const bool busy = bid < 224;
  uint64_t pc;
  asm volatile("s_getpc_b64 %0" : "=s"(pc));
  u32x2 px, pw;
  asm volatile("s_load_dwordx2 %0, %2, 0x0\n\ts_load_dwordx2 %1, %2, %3"
               : "=&s"(px), "=&s"(pw) : "s"(__builtin_amdgcn_kernarg_segment_ptr()), "s"(8 + 8 * (w >> 2)));
  SB();
  const unsigned mi = (bid & 6) * 2 + ((bid >> 3) & 3), half = bid & 1, idx = bid >> 5;
  const unsigned rot = ((bid & 6) + 4 * half) & 7;
  unsigned vw0, vw1, vx0, vx1;
  {
    const unsigned lane = threadIdx.x & 63, g = lane >> 4;
    vw0 = (lane >> 3) * 3584 + (((lane & 7) ^ ((g & 1) << 1)) << 4);
    vw1 = (vw0 ^ 64) + 8 * 3584 - 1024;
    vx0 = (lane >> 3) * 1024 + (((lane & 7) ^ g) << 4);
    vx1 = (vx0 ^ 64) + 8192 - 1024;
  }
  unsigned sx0 = (mi * 128 + 16 * w) * 1024;
  unsigned sw0 = (16 * (w & 3)) * 3584 + (half * 14 + idx) * 128;
  unsigned mu0 = lds0 + w * 2048;
  unsigned sw00 = sw0 + rot * (64 * 3584), sx00 = sx0 + rot * 128;
  asm volatile("" : "+v"(vw0), "+v"(vw1), "+v"(vx0), "+v"(vx1));
  asm volatile("" : "+s"(sx0), "+s"(sw0), "+s"(mu0), "+s"(sw00), "+s"(sx00));
  SB();
  asm volatile("s_waitcnt lgkmcnt(0)" : "+s"(px), "+s"(pw));
  const u32x4 srdX = {px[0], px[1] & 0xffffu, M_ * D_ * 2, 0x00020000u};
  const u32x4 srdW = {pw[0], pw[1] & 0xffffu, D_ * F_ * 2, 0x00020000u};
  auto issue_xw = [&](int n, int i, bool pre = false) {
    const int s = n < 16 ? n >> 1 : n - 16;
    const unsigned sg = (s + rot) & 7;
    if (n < 16 && (n & 1) == 0) {
      const unsigned so = n == 0 ? sx00 : sx0 + sg * 128;
      if (i == 0) { if (pre) dma16n<0>(vx0, srdX, so); else dma16<0>(mu0 + uaddr(n), vx0, srdX, so); } else dma16n<1024>(vx1, srdX, so);
    } else {
      const unsigned so = n == 1 ? sw00 : sw0 + sg * (64 * 3584) + (n >= 16 ? 896 : 0);
      if (i == 0) { if (pre) dma16n<0>(vw0, srdW, so); else dma16<0>(mu0 + uaddr(n), vw0, srdW, so); } else dma16n<1024>(vw1, srdW, so);
    }
  };
  if (busy) {
    issue_xw(0, 0); issue_xw(0, 1); issue_xw(1, 0); issue_xw(1, 1);
    BAR();
    issue_xw(2, 0); issue_xw(2, 1); issue_xw(3, 0); issue_xw(3, 1);
  } else {
    const unsigned o = min(threadIdx.x * 64u, CODE_BYTES);
    asm volatile("global_load_dword v249, %0, %1" ::"v"(o), "s"(pc) : "v249", "memory");
  }
  SB();
  const unsigned tid = opq(threadIdx.x), lane = tid & 63;
  const unsigned bid2 = opqs(bid), xcd = bid2 & 7, ml = (bid2 >> 3) & 3;
  unsigned rank = half * 8 + idx;
  const unsigned m64 = 2 * mi + half, n64 = idx;
  const unsigned g = lane >> 4, q = (lane & 15) >> 2, p = lane & 3;
  const u32x4 srdH = make_srd(ws, SYNC_OFF + 16 * 8192);
  const u32x4 srdD = make_srd(Wd, F_ * D_ * 2);
  unsigned syncb = SYNC_OFF + mi * 8192;
  const unsigned lane16 = lane * 16;

  const unsigned kq = w >> 1, mb = w & 1;
  unsigned rot2 = 2 * ml + idx; rot2 = rot2 >= 7 ? rot2 - 7 : rot2;
  unsigned kb[7];
  auto set_kb = [&]() {
    unsigned e = opqs(rot2);
#pragma unroll
    for (int j = 0; j < 7; ++j) {
      kb[j] = opqs((e + 7 * kq + (kq >> 1) * 14) * 1024);
      e = e == 6 ? 0 : e + 1;
    }
  };
  auto kap = [&](int s) { return kb[s % 7] + (s >= 7 ? 14 * 1024 : 0); };
  const unsigned vd0 = (lane >> 3) * 1024 + (((lane & 7) ^ (g << 1)) << 4);
  const unsigned vd1 = vd0 + 8192 - 1024;
  unsigned sd0 = (16 * mb) * 1024 + n64 * 128;
  auto issue_wd = [&](int s, int i, bool pre = false) {
    if (i == 0) { if (pre) dma16n<0>(vd0, srdD, sd0 + kap(s) * 32); else dma16<0>(mu0 + uaddr(24 + s), vd0, srdD, sd0 + kap(s) * 32); }
    else dma16n<1024>(vd1, srdD, sd0 + kap(s) * 32);
  };
  const unsigned dl = lds0 + kq * 4096 + (4 * g + q) * 128 + (((2 * g + (q >> 1)) & 3) << 5) + p * 8;
  unsigned db[3][4];
  auto set_db = [&](int k) {
#pragma unroll
    for (int c = 0; c < 4; ++c) db[k][c] = opq((dl ^ (c << 5)) + k * 4 * UNIT);
  };
  auto ldd = [&](int s, int a, int u) {
    const int ua = uaddr(24 + s);
    return tr_read(db[ua >> 16][a] + (ua & 65535) + u * 2048);
  };
  u32x2 ep, tag, fl;
  auto mktag = [&]() {
    const uint64_t t = (((uint64_t)ep[1] << 32) | ep[0]) * 0x9E3779B97F4A7C15ull + 1ull;
    tag = u32x2{(unsigned)t, (unsigned)(t >> 32)};
  };
  const unsigned one = tid == 0 ? 0u : 0x40000000u;
  const unsigned fo = tid < 16 ? 128 * (1 + tid) : 0x40000000u;
  unsigned fmask = 0x7F7Fu;
  auto flags_ok = [&]() { return (rfl((unsigned)__builtin_amdgcn_ballot_w64(fl[0] == tag[0] && fl[1] == tag[1])) & fmask) == fmask; };
  auto poll = [&](unsigned off) {
    unsigned cnt = SPIN_CAP, t;
#define PLD(r) "buffer_load_dwordx2 " r ", %2, %3, %4 offen sc1\n\t"
#define PCK(r) "s_waitcnt vmcnt(2)\n\tv_cmp_eq_u64 vcc, " r ", %5\n\ts_nop 1\n\ts_and_b32 %1, vcc_lo, %6\n\ts_cmp_eq_u32 %1, %6\n\ts_cbranch_scc1 2f\n\t"
    asm volatile(PLD("v[250:251]") "s_sleep 3\n\t" PLD("v[252:253]") "s_sleep 3\n\t" PLD("v[254:255]")
                 "1:\n\t"
                 PCK("v[250:251]") PLD("v[250:251]") PCK("v[252:253]") PLD("v[252:253]") PCK("v[254:255]") PLD("v[254:255]")
                 "s_sub_u32 %0, %0, 1\n\ts_cbranch_scc0 1b\n\t"
                 "2:"
                 : "+s"(cnt), "=&s"(t) : "v"(fo), "s"(srdH), "s"(syncb + off), "v"(tag), "s"(fmask)
                 : "v250", "v251", "v252", "v253", "v254", "v255", "vcc", "scc", "memory");
#undef PLD
#undef PCK
  };
  auto poll_slow = [&](unsigned off) {
#pragma nounroll
    for (unsigned spin = 0; spin < SPIN_CAP; ++spin) {
      gload8_sc1(fl, fo, srdH, syncb + off);
      asm volatile("s_waitcnt vmcnt(0)" : "+v"(fl)::"memory");
      if (flags_ok()) break;
      __builtin_amdgcn_s_sleep(8);
    }
  };
  Frag df[2][4];
  u32x4 hf[14][2];
  unsigned sh2 = ((m64 * 4 + mb * 2) * 56) * 1024;
  const unsigned lane16b = lane16 + 56 * 1024;
  auto issue_h = [&](int s, int b) { gload16_sc1(hf[s][b], b ? lane16b : lane16, srdH, sh2 + kap(s)); };

  set_kb();
  if (__builtin_expect(busy, 1)) {
    gload8_sc1(ep, 0u, srdH, syncb);
    const unsigned wm = w & 3, wn = w >> 2;
    const unsigned xrow = 32 * wm + (lane & 15);
    const unsigned xa0 = lds0 + xrow * 128 + ((g ^ ((xrow >> 1) & 7)) << 4);
    const unsigned wa0 = lds0 + (8 * g + q) * 128 + (((2 * wn) ^ ((q >> 1) | ((g & 1) << 1))) << 5) + p * 8;
    unsigned xa[3][2], wa[3][2];
#pragma unroll
    for (int k = 0; k < 3; ++k)
#pragma unroll
      for (int c = 0; c < 2; ++c) { xa[k][c] = opq((xa0 ^ (c << 6)) + k * 4 * UNIT); wa[k][c] = opq((wa0 ^ (c << 5)) + k * 4 * UNIT); }

    f32x4 acc[2][2][4];
    bf16x8 xf[8][2][2];
    Frag wf[4][2];
    auto ldx = [&](int s, int b, int kk) { const int ua = uaddr(2 * s); xf[s][b][kk] = lds_read16(xa[ua >> 16][kk] + (ua & 65535) + b * 2048); };
    auto ldw = [&](int G, int kk, int u = 2) {
      const int st = G >> 2, a = ((G & 1) << 1) | ((G >> 1) & 1), ua = uaddr(st < 8 ? 2 * st + 1 : 8 + st);
      const unsigned ad = wa[ua >> 16][a & 1] + (ua & 65535) + (a >> 1) * 8192 + kk * 4096;
      if (u != 1) wf[G & 3][kk].lo = tr_read(ad);
      if (u != 0) wf[G & 3][kk].hi = tr_read(ad + 512);
    };
    const unsigned sh = ((mi * 8 + wm * 2) * 56 + half * 28 + idx * 2 + wn) * 1024;
    u32x4 ho[2];
    auto epi = [&](int ps, int b, int c) {
      const int t = c >> 1, i = c & 1;
      float g0 = __builtin_elementwise_maximum(acc[ps][b][t][2 * i], 0.f), g1 = __builtin_elementwise_maximum(acc[ps][b][t][2 * i + 1], 0.f);
      float p0 = g0 * acc[ps][b][2 + t][2 * i];
      asm volatile("" : "+v"(p0));
      ho[b][c] = pack_bf16(p0, g1 * acc[ps][b][2 + t][2 * i + 1]);
    };
    auto sth = [&](int ps, int b) { gstore16_sc1(ho[b], lane16, srdH, sh + b * (56 * 1024) + ps * (14 * 1024)); };

    auto vmem = [&](auto PP) INL {
      sfor<first_op(PP.v), first_op(PP.v + 1)>([&](auto K) INL {
        constexpr Op o = SCH.op[K.v];
        if constexpr (o.pos == PP.v) {
          constexpr bool pre = PP.v < 128 && K.v == first_op(PP.v);
          if constexpr (o.kind == K_DMA) { if constexpr (o.n < 24) issue_xw(o.n, o.i, pre); else issue_wd(o.n - 24, o.i, pre); }
          if constexpr (o.kind == K_STORE) sth(o.n, o.i);
          if constexpr (o.kind == K_FLAG) gstore8_sc1(tag, one, srdH, syncb + 128 * (1 + rank));
          if constexpr (o.kind == K_POLL) gload8_sc1(fl, fo, srdH, syncb);
          if constexpr (o.kind == K_H) issue_h(o.n, o.i);
        }
      });
    };
    wait_vm<5>();
    BAR();
    ldw(0, 0); ldx(0, 0, 0); ldw(0, 1); ldx(0, 0, 1); ldx(0, 1, 0); ldx(0, 1, 1); ldw(1, 0); ldw(1, 1); ldw(2, 0); ldw(2, 1);
    SB();
    sfor<0, 128>([&](auto Z) INL {
      constexpr int P = Z.v, ps = P >> 6, s = (P >> 3) & 7, G = P >> 1, a = ((G & 1) << 1) | ((G >> 1) & 1), b = P & 1;
      if constexpr (is_bar(P)) {
        constexpr int n = bar_wait(P);
        static_assert(n >= 0 && n < 24, "wait count");
        if constexpr (P == 10) asm volatile("s_waitcnt vmcnt(%1)" : "+v"(ep) : "i"(n) : "memory");
        else if constexpr (P == 122) {
          asm volatile("s_waitcnt vmcnt(%1)" : "+v"(fl) : "i"(n) : "memory");
          if (w == 0) { if (__builtin_expect(!flags_ok(), 0)) poll(0); }
        } else wait_vm<n>();
        BAR();
      }
      if constexpr (P < 58 ? (P & 1) == 0 : (P & 3) == 2) __builtin_amdgcn_s_setprio(P < 58 ? 3 - (((P + 6) & 7) >> 1) : 3 - (((P + 6) & 15) >> 2));
      if constexpr (s == 0) mfma_new(acc[ps][b][a], join(wf[G & 3][0]), xf[s][b][0]); else mfma_acc(acc[ps][b][a], join(wf[G & 3][0]), xf[s][b][0]);
      SB();
      if constexpr (G + 3 < 64) ldw(G + 3, b, 0);
      if constexpr (P >= 124) df[0][P - 124].lo = ldd(0, P - 124, 0);
      {
        constexpr int k0 = first_op(P);
        if constexpr (k0 < first_op(P + 1) && SCH.op[k0].kind == K_DMA && SCH.op[k0].i == 0) set_m0(mu0 + uaddr(SCH.op[k0].n));
        else if constexpr (P < 56 && (P & 7) >= 2 && (P & 7) < 6) ldx(s + 1, ((P - 2) >> 1) & 1, P & 1);
      }
      SB();
      mfma_acc(acc[ps][b][a], join(wf[G & 3][1]), xf[s][b][1]);
      SB();
      if constexpr (G + 3 < 64) ldw(G + 3, b, 1);
      if constexpr (P >= 124) df[0][P - 124].hi = ldd(0, P - 124, 1);
      {
        constexpr int k0 = first_op(P);
        if constexpr (k0 < first_op(P + 1) && SCH.op[k0].kind == K_DMA && SCH.op[k0].i == 0 && P < 56 && (P & 7) >= 2 && (P & 7) < 6) ldx(s + 1, ((P - 2) >> 1) & 1, P & 1);
      }
      if constexpr (P == 120) set_db(1);
      if constexpr ((P & 63) == 62 || (P & 63) == 63) { epi(ps, P & 1, 0); epi(ps, P & 1, 1); }
      if constexpr (P == 65 || P == 66) { epi(0, P - 65, 2); epi(0, P - 65, 3); }
      if constexpr (P == 116) set_db(0);
      if constexpr (P == 118) set_db(2);
      if constexpr (P == 84) mktag();
      vmem(Z);
      SB();
    });
    epi(1, 0, 2); epi(1, 0, 3);
    sth(1, 0);
    SB();
    epi(1, 1, 2); epi(1, 1, 3);
    sth(1, 1);
    issue_h(2, 0); issue_h(2, 1);
    __builtin_amdgcn_s_setprio(0);
  } else {
    gload8_sc1(ep, 0u, srdH, syncb);
    set_db(0); set_db(1); set_db(2);
    {
      unsigned t = 0;
      {
        const unsigned st = (rot + 2 + (lane & 3)) & 7;
        const unsigned vxo = (lane & 3) < 2 ? (mi * 128 + w * 16 + (lane >> 2)) * 1024 + st * 128 : 0x40000000u;
        asm volatile("buffer_load_dword %0, %1, %2, 0 offen" : "+v"(t) : "v"(vxo), "s"(srdX) : "memory");
      }
      {
        const unsigned li = (ml * 4 + (w & 3)) * 56 + lane;
        const unsigned row = li / 7, c = li - row * 7;
        const unsigned vo = lane < 56 ? (rot * 64 + row) * 3584 + (half * 14 + 7 + c) * 128 : 0x40000000u;
        asm volatile("buffer_load_dword %0, %1, %2, 0 offen" : "+v"(t) : "v"(vo), "s"(srdW) : "memory");
      }
#pragma unroll
      for (int i = 0; i < 3; ++i) {
        unsigned c = i + xcd; c = c >= 7 ? c - 7 : c; c = c >= 7 ? c - 7 : c;
        asm volatile("buffer_load_dword %0, %1, %2, %3 offen" : "+v"(t) : "v"(lane * 128), "s"(srdD), "s"(((c * 4 + ml) * 8 + w) * 8192) : "memory");
      }
      asm volatile("s_waitcnt vmcnt(0)" : "+v"(t), "+v"(ep)::"memory");
      mktag();
    }
#pragma unroll
    for (int s = 0; s < 7; ++s) { issue_wd(s, 0); issue_wd(s, 1); }
    if (w == 0) poll_slow(0); else wait_vm<0>();
    BAR();
#pragma unroll
    for (int s = 0; s < 3; ++s) { issue_h(s, 0); issue_h(s, 1); }
#pragma unroll
    for (int a = 0; a < 4; ++a) { df[0][a].lo = ldd(0, a, 0); df[0][a].hi = ldd(0, a, 1); }
    wait_vm<0>();
  }
  asm volatile("" : "+s"(mu0), "+s"(sd0), "+s"(sh2), "+s"(syncb), "+s"(rank));
  asm volatile("" : "+s"(kb[0]), "+s"(kb[1]), "+s"(kb[2]), "+s"(kb[3]), "+s"(kb[4]), "+s"(kb[5]), "+s"(kb[6]));
  fmask = rank == 7 ? 0xFFFFu : 0x7F7Fu;
  f32x4 acc2[2][4];
  const unsigned pa = opq(lds0 + ((kq + 8) % 10) * UNIT + (mb * 32 + (lane & 15)) * 256 + ((g ^ (lane & 7)) << 4));
  const unsigned pa1 = opq(pa ^ 64);
  const unsigned ra = opq(lds0 + (w * 8 + (lane >> 3)) * 256 + (((2 * (lane & 7)) ^ (lane >> 3)) << 4)), rb = opq(ra ^ 16);
  const unsigned ra8 = opq(ra + 8 * UNIT), rb8 = opq(rb + 8 * UNIT);
  const unsigned vo = opq((w * 8 + (lane >> 3)) * 1024 + (lane & 7) * 16), so = opqs(m64 * 65536 + n64 * 128);
  const u32x4 srdO = make_srd(out, M_ * D_ * 2);
  auto wpart = [&](int a) {
    const unsigned ad = ((a & 1) ? pa1 : pa) + (a >> 1) * 128;
    *(LDS_AS f32x4*)(uintptr_t)(ad) = acc2[0][a];
    *(LDS_AS f32x4*)(uintptr_t)(ad + 16 * 256) = acc2[1][a];
  };
  auto vmem2 = [&](auto PP) INL {
    sfor<first_op(PP.v), first_op(PP.v + 1)>([&](auto K) INL {
      constexpr Op o = SCH.op[K.v];
      if constexpr (o.pos == PP.v) {
        if constexpr (o.kind == K_DMA) issue_wd(o.n - 24, o.i);
        if constexpr (o.kind == K_FLAG) gstore8_sc1(tag, one, srdH, syncb + 128 * (17 + rank));
        if constexpr (o.kind == K_POLL) gload8_sc1(fl, fo, srdH, syncb + 2048);
        if constexpr (o.kind == K_H) issue_h(o.n, o.i);
      }
    });
  };
  sfor<0, 14>([&](auto S) INL {
    constexpr int s = S.v;
    if constexpr (s == Y1 || s == FCHK || s == Y2 || s == Y3) {
      constexpr int n = bar_wait(Q(s, 0));
      static_assert(n >= 0 && n < 24, "wait count");
      if constexpr (s == FCHK) {
        asm volatile("s_waitcnt vmcnt(%1)" : "+v"(fl) : "i"(n) : "memory");
        if (w == 0) { if (__builtin_expect(!flags_ok(), 0)) poll(2048); }
      } else wait_vm<n>();
      BAR();
      if constexpr (s == Y3) { if (rank == 7 && w == 0) gstore8_sc1(tag, one, srdH, syncb); }
    }
    vmem2(IC<Q(s, 0)>{});
    if constexpr (s == Y1) asm volatile("" : "+v"(hf[s][0]), "+v"(hf[s][1])::"memory");
    else asm volatile("s_waitcnt vmcnt(%2)" : "+v"(hf[s][0]), "+v"(hf[s][1]) : "i"(younger_than(K_H, s, 1, Q(s, 1))) : "memory");
    SB();
    sfor<0, 4>([&](auto A) INL {
      constexpr int a = A.v;
      if constexpr (s == 0) mfma_new(acc2[0][a], join(df[0][a]), hf[0][0]); else mfma_acc(acc2[0][a], join(df[s & 1][a]), hf[s][0]);
      SB();
      if constexpr (s + 1 < 14) df[(s + 1) & 1][a].lo = ldd(s + 1, a, 0);
      SB();
      if constexpr (s == 0) mfma_new(acc2[1][a], join(df[0][a]), hf[0][1]); else mfma_acc(acc2[1][a], join(df[s & 1][a]), hf[s][1]);
      SB();
      if constexpr (s + 1 < 14) df[(s + 1) & 1][a].hi = ldd(s + 1, a, 1);
      vmem2(IC<Q(s, a + 1)>{});
      if constexpr (s == 13 && a == 3) { asm volatile("s_nop 1" ::: "memory"); SB(); wpart(0); SB(); wpart(1); }
      SB();
    });
  });
  wpart(2);
  SB();
  asm volatile("s_nop 1" ::: "memory");
  SB();
  wpart(3);
  wait_lgkm0();
  BAR();
  {
    f32x4 p0[4], p1[4];
#pragma unroll
    for (int v = 0; v < 4; ++v) {
      p0[v] = *(LDS_AS f32x4*)(uintptr_t)((v < 2 ? ra8 : ra) + (v & 1) * UNIT);
      p1[v] = *(LDS_AS f32x4*)(uintptr_t)((v < 2 ? rb8 : rb) + (v & 1) * UNIT);
    }
    SB();
    const f32x4 s0 = (p0[0] + p0[1]) + (p0[2] + p0[3]), s1 = (p1[0] + p1[1]) + (p1[2] + p1[3]);
    u32x4 o = {pack_bf16(s0[0], s0[1]), pack_bf16(s0[2], s0[3]), pack_bf16(s1[0], s1[1]), pack_bf16(s1[2], s1[3])};
    asm volatile("buffer_store_dwordx4 %0, %1, %2, %3 offen" ::"v"(o), "v"(vo), "s"(srdO), "s"(so) : "memory");
  }
}

extern "C" void kernel_launch(void* const* d_in, const int* in_sizes, int n_in, void* d_out, int out_size,
                              void* d_ws, size_t ws_size, hipStream_t stream) {
  (void)in_sizes; (void)n_in; (void)out_size; (void)ws_size;
  (void)hipFuncSetAttribute((const void*)k_fused, hipFuncAttributeMaxDynamicSharedMemorySize, LDS_TOTAL);
  k_fused<<<256, 512, LDS_TOTAL, stream>>>(d_in[0], d_in[1], d_in[2], d_in[3], d_out, d_ws);
}
```

```cpp
#include <hip/hip_runtime.h>
#include <hip/hip_bf16.h>
#include <stdint.h>

typedef __attribute__((ext_vector_type(8))) __bf16 bf16x8;
typedef __attribute__((ext_vector_type(4))) short s16x4;
typedef __attribute__((ext_vector_type(8))) short s16x8;
typedef __attribute__((ext_vector_type(2))) __bf16 bf16x2;
typedef __attribute__((ext_vector_type(2))) float f32x2;
typedef __attribute__((ext_vector_type(4))) float f32x4;
typedef __attribute__((ext_vector_type(4))) unsigned u32x4;
typedef __attribute__((ext_vector_type(2))) unsigned u32x2;
#define LDS_AS __attribute__((address_space(3)))
#define ST_DECL
#define ST(i)
#define ST_FLUSH(a,b)

constexpr int M_ = 2048, D_ = 512, F_ = 1792;
constexpr unsigned SYNC_OFF = 8u << 20;
constexpr unsigned SPIN_CAP = 1u << 17;
constexpr unsigned CODE_BYTES = 12544;

__device__ __forceinline__ unsigned rfl(unsigned v) { return __builtin_amdgcn_readfirstlane(v); }

__device__ __forceinline__ u32x4 make_srd(const void* p, unsigned bytes) {
  uint64_t a = (uint64_t)p;
  u32x4 r;
  r[0] = rfl((unsigned)a);
  r[1] = rfl((unsigned)(a >> 32)) & 0xffffu;
  r[2] = bytes;
  r[3] = 0x00020000u;
  return r;
}

template <int IMM>
__device__ __forceinline__ void dma16(unsigned m0v, unsigned voff, u32x4 srd, unsigned soff) {
  asm volatile("s_mov_b32 m0, %0\n\ts_nop 0\n\tbuffer_load_dwordx4 %1, %2, %3 offen offset:%4 lds"
               ::"s"(m0v), "v"(voff), "s"(srd), "s"(soff), "i"(IMM) : "memory");
}
template <int IMM>
__device__ __forceinline__ void dma16n(unsigned voff, u32x4 srd, unsigned soff) {
  asm volatile("buffer_load_dwordx4 %0, %1, %2 offen offset:%3 lds" ::"v"(voff), "s"(srd), "s"(soff), "i"(IMM) : "memory");
}
__device__ __forceinline__ void set_m0(unsigned v) { asm volatile("s_mov_b32 m0, %0" ::"s"(v) : "memory"); }
__device__ __forceinline__ unsigned opq(unsigned v) { asm volatile("" : "+v"(v)); return v; }
__device__ __forceinline__ unsigned opqs(unsigned v) { asm volatile("" : "+s"(v)); return v; }
__device__ __forceinline__ void gload16_sc1(u32x4& dst, unsigned voff, u32x4 srd, unsigned soff) {
  asm volatile("buffer_load_dwordx4 %0, %1, %2, %3 offen sc1" : "=v"(dst) : "v"(voff), "s"(srd), "s"(soff) : "memory");
}
__device__ __forceinline__ void gstore16_sc1(u32x4 v, unsigned voff, u32x4 srd, unsigned soff) {
  asm volatile("buffer_store_dwordx4 %0, %1, %2, %3 offen sc1\n\ts_nop 1" ::"v"(v), "v"(voff), "s"(srd), "s"(soff) : "memory");
}
__device__ __forceinline__ void gload8_sc1(u32x2& dst, unsigned voff, u32x4 srd, unsigned soff) {
  asm volatile("buffer_load_dwordx2 %0, %1, %2, %3 offen sc1" : "=v"(dst) : "v"(voff), "s"(srd), "s"(soff) : "memory");
}
__device__ __forceinline__ void gstore8_sc1(u32x2 v, unsigned voff, u32x4 srd, unsigned soff) {
  asm volatile("buffer_store_dwordx2 %0, %1, %2, %3 offen sc1" ::"v"(v), "v"(voff), "s"(srd), "s"(soff) : "memory");
}

__device__ __forceinline__ s16x4 tr_read(unsigned addr) {
  return __builtin_amdgcn_ds_read_tr16_b64_v4i16((LDS_AS s16x4*)(uintptr_t)addr);
}
struct Frag { s16x4 lo, hi; };
__device__ __forceinline__ bf16x8 join(const Frag& f) {
  s16x8 v = __builtin_shufflevector(f.lo, f.hi, 0, 1, 2, 3, 4, 5, 6, 7);
  return __builtin_bit_cast(bf16x8, v);
}
__device__ __forceinline__ bf16x8 lds_read16(unsigned addr) { return *(LDS_AS bf16x8*)(uintptr_t)addr; }
__device__ __forceinline__ unsigned pack_bf16(float a, float b) {
  f32x2 v = {a, b};
  bf16x2 r = __builtin_convertvector(v, bf16x2);
  return __builtin_bit_cast(unsigned, r);
}
template <class TB>
__device__ __forceinline__ void mfma_acc(f32x4& c, bf16x8 a, TB b) {
  asm volatile("v_mfma_f32_16x16x32_bf16 %0, %1, %2, %0" : "+v"(c) : "v"(a), "v"(b));
}
template <class TB>
__device__ __forceinline__ void mfma_new(f32x4& c, bf16x8 a, TB b) {
  asm volatile("v_mfma_f32_16x16x32_bf16 %0, %1, %2, 0" : "=&v"(c) : "v"(a), "v"(b));
}

template <int I> struct IC { static constexpr int v = I; };
template <int I, int N, class F> __device__ __forceinline__ void sfor(F&& f) {
  if constexpr (I < N) { f(IC<I>{}); sfor<I + 1, N>(f); }
}
template <int N> __device__ __forceinline__ void wait_vm() { asm volatile("s_waitcnt vmcnt(%0)" ::"i"(N) : "memory"); }
__device__ __forceinline__ void wait_lgkm0() { asm volatile("s_waitcnt lgkmcnt(0)" ::: "memory"); }
#define BAR() asm volatile("s_barrier" ::: "memory")
#define SB() __builtin_amdgcn_sched_barrier(0)
#define INL __attribute__((always_inline))

constexpr int UNIT = 16384, LDS_TOTAL = 10 * UNIT;
constexpr int uaddr(int n) { return (n % 10) * UNIT; }

enum { K_DMA = 1, K_STORE, K_FLAG, K_POLL, K_H };
struct Op { int pos, kind, n, i; };
struct Sched { int cnt; Op op[200]; };
constexpr void put(Sched& s, int pos, int kind, int n, int i) { s.op[s.cnt++] = Op{pos, kind, n, i}; }
constexpr void spread(Sched& s, int p0, int p1, int u0, int nu) {
  for (int k = 0; k < 2 * nu; ++k) put(s, p0 + (k * (p1 - p0)) / (2 * nu), K_DMA, u0 + (k >> 1), k & 1);
}
constexpr int Q(int step, int slot) { return 200 + 8 * step + slot; }
constexpr void pair(Sched& s, int step, int slot, int kind, int n) { put(s, Q(step, slot), kind, n, 0); put(s, Q(step, slot == 0 ? 0 : slot + 1), kind, n, 1); }
constexpr int HB = 3;
constexpr int Y1 = 2, Y2 = 5, FCHK = 7, Y3 = 10;
constexpr Sched make_sched() {
  Sched s{};
  for (int n = 0; n < 4; ++n) { put(s, -1, K_DMA, n, 0); put(s, -1, K_DMA, n, 1); }
  put(s, -1, K_POLL, 0, 0);
  spread(s, 0, 2, 4, 2);
  for (int t = 0; t < 7; ++t) spread(s, 8 * t + 2, 8 * t + 9, 2 * t + 6, 2);
  spread(s, 58, 64, 20, 2);
  put(s, 65, K_STORE, 0, 0);
  put(s, 66, K_STORE, 0, 1);
  spread(s, 72, 80, 22, 1);
  spread(s, 82, 89, 23, 2);
  put(s, 90, K_FLAG, 0, 0);
  spread(s, 91, 103, 25, 2);
  spread(s, 103, 111, 27, 2);
  put(s, 111, K_POLL, 1, 0);
  spread(s, 112, 121, 29, 2);
  for (int k = 0; k < 4; ++k) put(s, 122 + k, K_H, k >> 1, k & 1);
  put(s, 128, K_STORE, 1, 0);
  put(s, 128, K_STORE, 1, 1);
  put(s, 128, K_H, 2, 0);
  put(s, 128, K_H, 2, 1);
  put(s, Q(Y1, 0), K_FLAG, 1, 0);
  pair(s, 2, 0, K_H, 3); pair(s, 2, 0, K_H, 4);
  pair(s, 2, 3, K_DMA, 31);
  pair(s, 3, 1, K_H, 5); pair(s, 3, 3, K_H, 6);
  pair(s, 4, 1, K_DMA, 32); pair(s, 4, 3, K_DMA, 33);
  pair(s, 5, 1, K_DMA, 34); pair(s, 5, 3, K_DMA, 35);
  put(s, Q(6, 0), K_POLL, 2, 0);
  pair(s, 6, 1, K_DMA, 36); pair(s, 6, 3, K_DMA, 37);
  for (int k = 0; k < HB; ++k) pair(s, 7, 0, K_H, 7 + k);
  for (int k = 7 + HB; k < 14; ++k) pair(s, k - HB, 1, K_H, k);
  return s;
}
constexpr Sched SCH = make_sched();
constexpr int younger_than(int kind, int n, int i, int P) {
  int c = -1;
  for (int k = 0; k < SCH.cnt; ++k) {
    if (SCH.op[k].pos >= P) break;
    if (SCH.op[k].kind == kind && SCH.op[k].n == n && SCH.op[k].i == i) c = 0;
    else if (c >= 0 && SCH.op[k].kind != K_FLAG && !(SCH.op[k].kind == K_POLL && SCH.op[k].n > 0)) ++c;
  }
  return c;
}
constexpr int first_op(int P) { int k = 0; while (k < SCH.cnt && SCH.op[k].pos < P) ++k; return k; }
constexpr bool is_bar(int P) { return P < 64 ? (P & 7) == 2 : (P & 15) == 10; }
constexpr int bar_wait(int P) {
  if (P < 64) return younger_than(K_DMA, 2 * (P >> 3) + 3, 1, P);
  if (P == 74) return younger_than(K_DMA, 19, 1, P);
  if (P == 90) return younger_than(K_STORE, 0, 1, P);
  if (P == 106) return younger_than(K_DMA, 23, 1, P);
  if (P == 122) return younger_than(K_POLL, 1, 0, P);
  if (P == Q(Y1, 0)) return 0;
  if (P == Q(Y2, 0)) return younger_than(K_DMA, 31, 1, P);
  if (P == Q(FCHK, 0)) return younger_than(K_POLL, 2, 0, P);
  return younger_than(K_DMA, 37, 1, P);
}
constexpr bool sched_sorted() { for (int k = 1; k < SCH.cnt; ++k) if (SCH.op[k].pos < SCH.op[k - 1].pos) return false; return true; }
static_assert(sched_sorted(), "schedule order");

__global__ __launch_bounds__(512, 2) void k_fused(const void* __restrict__ x, const void* __restrict__ Wg,
                                                  const void* __restrict__ Wu, const void* __restrict__ Wd,
                                                  void* __restrict__ out, void* __restrict__ ws) {
  extern __shared__ __attribute__((aligned(16))) char smem[];
  const unsigned lds0 = (unsigned)(uintptr_t)(LDS_AS char*)smem;
  ST_DECL ST(0)
  const unsigned bid = blockIdx.x;
  const unsigned w = rfl(threadIdx.x >> 6);
  const bool busy = bid < 224;
  uint64_t pc;
  asm volatile("s_getpc_b64 %0" : "=s"(pc));
  u32x2 px, pw;
  asm volatile("s_load_dwordx2 %0, %2, 0x0\n\ts_load_dwordx2 %1, %2, %3"
               : "=&s"(px), "=&s"(pw) : "s"(__builtin_amdgcn_kernarg_segment_ptr()), "s"(8 + 8 * (w >> 2)));
  SB();
  const unsigned mi = (bid & 6) * 2 + ((bid >> 3) & 3), half = bid & 1, idx = bid >> 5;
  const unsigned rot = ((bid & 6) + 4 * half + ((bid >> 3) & RA) + (idx & RB)) & 7;
  unsigned vw0, vw1, vx0, vx1;
  {
    const unsigned lane = threadIdx.x & 63, g = lane >> 4;
    vw0 = (lane >> 3) * 3584 + (((lane & 7) ^ ((g & 1) << 1)) << 4);
    vw1 = (vw0 ^ 64) + 8 * 3584 - 1024;
    vx0 = (lane >> 3) * 1024 + (((lane & 7) ^ g) << 4);
    vx1 = (vx0 ^ 64) + 8192 - 1024;
  }
  unsigned sx0 = (mi * 128 + 16 * w) * 1024;
  unsigned sw0 = (16 * (w & 3)) * 3584 + (half * 14 + idx) * 128;
  unsigned mu0 = lds0 + w * 2048;
  unsigned sw00 = sw0 + rot * (64 * 3584), sx00 = sx0 + rot * 128;
  asm volatile("" : "+v"(vw0), "+v"(vw1), "+v"(vx0), "+v"(vx1));
  asm volatile("" : "+s"(sx0), "+s"(sw0), "+s"(mu0), "+s"(sw00), "+s"(sx00));
  SB();
  asm volatile("s_waitcnt lgkmcnt(0)" : "+s"(px), "+s"(pw));
  const u32x4 srdX = {px[0], px[1] & 0xffffu, M_ * D_ * 2, 0x00020000u};
  const u32x4 srdW = {pw[0], pw[1] & 0xffffu, D_ * F_ * 2, 0x00020000u};
  auto issue_xw = [&](int n, int i, bool pre = false) {
    const int s = n < 16 ? n >> 1 : n - 16;
    const unsigned sg = (s + rot) & 7;
    if (n < 16 && (n & 1) == 0) {
      const unsigned so = n == 0 ? sx00 : sx0 + sg * 128;
      if (i == 0) { if (pre) dma16n<0>(vx0, srdX, so); else dma16<0>(mu0 + uaddr(n), vx0, srdX, so); } else dma16n<1024>(vx1, srdX, so);
    } else {
      const unsigned so = n == 1 ? sw00 : sw0 + sg * (64 * 3584) + (n >= 16 ? 896 : 0);
      if (i == 0) { if (pre) dma16n<0>(vw0, srdW, so); else dma16<0>(mu0 + uaddr(n), vw0, srdW, so); } else dma16n<1024>(vw1, srdW, so);
    }
  };
  if (busy) {
    issue_xw(0, 0); issue_xw(0, 1); issue_xw(1, 0); issue_xw(1, 1);
    BAR();
    issue_xw(2, 0); issue_xw(2, 1); issue_xw(3, 0); issue_xw(3, 1);
  } else {
    const unsigned o = min(threadIdx.x * 64u, CODE_BYTES);
    asm volatile("global_load_dword v249, %0, %1" ::"v"(o), "s"(pc) : "v249", "memory");
  }
  SB();
  const unsigned tid = opq(threadIdx.x), lane = tid & 63;
  const unsigned bid2 = opqs(bid), xcd = bid2 & 7, ml = (bid2 >> 3) & 3;
  unsigned rank = half * 8 + idx;
  const unsigned m64 = 2 * mi + half, n64 = idx;
  const unsigned g = lane >> 4, q = (lane & 15) >> 2, p = lane & 3;
  const u32x4 srdH = make_srd(ws, SYNC_OFF + 16 * 8192);
  const u32x4 srdD = make_srd(Wd, F_ * D_ * 2);
  unsigned syncb = SYNC_OFF + mi * 8192;
  const unsigned lane16 = lane * 16;

  const unsigned kq = w >> 1, mb = w & 1;
  unsigned rot2 = 2 * ml + idx; rot2 = rot2 >= 7 ? rot2 - 7 : rot2;
  unsigned kb[7];
  auto set_kb = [&]() {
    unsigned e = opqs(rot2);
#pragma unroll
    for (int j = 0; j < 7; ++j) {
      kb[j] = opqs((e + 7 * kq + (kq >> 1) * 14) * 1024);
      e = e == 6 ? 0 : e + 1;
    }
  };
  auto kap = [&](int s) { return kb[s % 7] + (s >= 7 ? 14 * 1024 : 0); };
  const unsigned vd0 = (lane >> 3) * 1024 + (((lane & 7) ^ (g << 1)) << 4);
  const unsigned vd1 = vd0 + 8192 - 1024;
  unsigned sd0 = (16 * mb) * 1024 + n64 * 128;
  auto issue_wd = [&](int s, int i, bool pre = false) {
    if (i == 0) { if (pre) dma16n<0>(vd0, srdD, sd0 + kap(s) * 32); else dma16<0>(mu0 + uaddr(24 + s), vd0, srdD, sd0 + kap(s) * 32); }
    else dma16n<1024>(vd1, srdD, sd0 + kap(s) * 32);
  };
  const unsigned dl = lds0 + kq * 4096 + (4 * g + q) * 128 + (((2 * g + (q >> 1)) & 3) << 5) + p * 8;
  unsigned db[3][4];
  auto set_db = [&](int k) {
#pragma unroll
    for (int c = 0; c < 4; ++c) db[k][c] = opq((dl ^ (c << 5)) + k * 4 * UNIT);
  };
  auto ldd = [&](int s, int a, int u) {
    const int ua = uaddr(24 + s);
    return tr_read(db[ua >> 16][a] + (ua & 65535) + u * 2048);
  };
  u32x2 ep, tag, fl;
  auto mktag = [&]() {
    const uint64_t t = (((uint64_t)ep[1] << 32) | ep[0]) * 0x9E3779B97F4A7C15ull + 1ull;
    tag = u32x2{(unsigned)t, (unsigned)(t >> 32)};
  };
  const unsigned one = tid == 0 ? 0u : 0x40000000u;
  const unsigned fo = tid < 16 ? 128 * (1 + tid) : 0x40000000u;
  unsigned fmask = 0x7F7Fu;
  auto flags_ok = [&]() { return (rfl((unsigned)__builtin_amdgcn_ballot_w64(fl[0] == tag[0] && fl[1] == tag[1])) & fmask) == fmask; };
  auto poll = [&](unsigned off) {
    unsigned cnt = SPIN_CAP, t;
#define PLD(r) "buffer_load_dwordx2 " r ", %2, %3, %4 offen sc1\n\t"
#define PCK(r) "s_waitcnt vmcnt(2)\n\tv_cmp_eq_u64 vcc, " r ", %5\n\ts_nop 1\n\ts_and_b32 %1, vcc_lo, %6\n\ts_cmp_eq_u32 %1, %6\n\ts_cbranch_scc1 2f\n\t"
    asm volatile(PLD("v[250:251]") "s_sleep 3\n\t" PLD("v[252:253]") "s_sleep 3\n\t" PLD("v[254:255]")
                 "1:\n\t"
                 PCK("v[250:251]") PLD("v[250:251]") PCK("v[252:253]") PLD("v[252:253]") PCK("v[254:255]") PLD("v[254:255]")
                 "s_sub_u32 %0, %0, 1\n\ts_cbranch_scc0 1b\n\t"
                 "2:"
                 : "+s"(cnt), "=&s"(t) : "v"(fo), "s"(srdH), "s"(syncb + off), "v"(tag), "s"(fmask)
                 : "v250", "v251", "v252", "v253", "v254", "v255", "vcc", "scc", "memory");
#undef PLD
#undef PCK
  };
  auto poll_slow = [&](unsigned off) {
#pragma nounroll
    for (unsigned spin = 0; spin < SPIN_CAP; ++spin) {
      gload8_sc1(fl, fo, srdH, syncb + off);
      asm volatile("s_waitcnt vmcnt(0)" : "+v"(fl)::"memory");
      if (flags_ok()) break;
      __builtin_amdgcn_s_sleep(8);
    }
  };
  Frag df[2][4];
  u32x4 hf[14][2];
  unsigned sh2 = ((m64 * 4 + mb * 2) * 56) * 1024;
  const unsigned lane16b = lane16 + 56 * 1024;
  auto issue_h = [&](int s, int b) { gload16_sc1(hf[s][b], b ? lane16b : lane16, srdH, sh2 + kap(s)); };

  set_kb();
  if (__builtin_expect(busy, 1)) {
    gload8_sc1(ep, 0u, srdH, syncb);
    const unsigned wm = w & 3, wn = w >> 2;
    const unsigned xrow = 32 * wm + (lane & 15);
    const unsigned xa0 = lds0 + xrow * 128 + ((g ^ ((xrow >> 1) & 7)) << 4);
    const unsigned wa0 = lds0 + (8 * g + q) * 128 + (((2 * wn) ^ ((q >> 1) | ((g & 1) << 1))) << 5) + p * 8;
    unsigned xa[3][2], wa[3][2];
#pragma unroll
    for (int k = 0; k < 3; ++k)
#pragma unroll
      for (int c = 0; c < 2; ++c) { xa[k][c] = opq((xa0 ^ (c << 6)) + k * 4 * UNIT); wa[k][c] = opq((wa0 ^ (c << 5)) + k * 4 * UNIT); }
    ST(1)

    f32x4 acc[2][2][4];
    bf16x8 xf[8][2][2];
    Frag wf[4][2];
    auto ldx = [&](int s, int b, int kk) { const int ua = uaddr(2 * s); xf[s][b][kk] = lds_read16(xa[ua >> 16][kk] + (ua & 65535) + b * 2048); };
    auto ldw = [&](int G, int kk, int u = 2) {
      const int st = G >> 2, a = ((G & 1) << 1) | ((G >> 1) & 1), ua = uaddr(st < 8 ? 2 * st + 1 : 8 + st);
      const unsigned ad = wa[ua >> 16][a & 1] + (ua & 65535) + (a >> 1) * 8192 + kk * 4096;
      if (u != 1) wf[G & 3][kk].lo = tr_read(ad);
      if (u != 0) wf[G & 3][kk].hi = tr_read(ad + 512);
    };
    const unsigned sh = ((mi * 8 + wm * 2) * 56 + half * 28 + idx * 2 + wn) * 1024;
    u32x4 ho[2];
    auto epi = [&](int ps, int b, int c) {
      const int t = c >> 1, i = c & 1;
      float g0 = __builtin_elementwise_maximum(acc[ps][b][t][2 * i], 0.f), g1 = __builtin_elementwise_maximum(acc[ps][b][t][2 * i + 1], 0.f);
      float p0 = g0 * acc[ps][b][2 + t][2 * i];
      asm volatile("" : "+v"(p0));
      ho[b][c] = pack_bf16(p0, g1 * acc[ps][b][2 + t][2 * i + 1]);
    };
    auto sth = [&](int ps, int b) { gstore16_sc1(ho[b], lane16, srdH, sh + b * (56 * 1024) + ps * (14 * 1024)); };

    auto vmem = [&](auto PP) INL {
      sfor<first_op(PP.v), first_op(PP.v + 1)>([&](auto K) INL {
        constexpr Op o = SCH.op[K.v];
        if constexpr (o.pos == PP.v) {
          constexpr bool pre = PP.v < 128 && K.v == first_op(PP.v);
          if constexpr (o.kind == K_DMA) { if constexpr (o.n < 24) issue_xw(o.n, o.i, pre); else issue_wd(o.n - 24, o.i, pre); }
          if constexpr (o.kind == K_STORE) sth(o.n, o.i);
          if constexpr (o.kind == K_FLAG) gstore8_sc1(tag, one, srdH, syncb + 128 * (1 + rank));
          if constexpr (o.kind == K_POLL) gload8_sc1(fl, fo, srdH, syncb);
          if constexpr (o.kind == K_H) issue_h(o.n, o.i);
        }
      });
    };
    wait_vm<5>();
    ST(2)
    BAR();
    ldw(0, 0); ldx(0, 0, 0); ldw(0, 1); ldx(0, 0, 1); ldx(0, 1, 0); ldx(0, 1, 1); ldw(1, 0); ldw(1, 1); ldw(2, 0); ldw(2, 1);
    SB();
    sfor<0, 128>([&](auto Z) INL {
      constexpr int P = Z.v, ps = P >> 6, s = (P >> 3) & 7, G = P >> 1, a = ((G & 1) << 1) | ((G >> 1) & 1), b = P & 1;
      if constexpr (is_bar(P)) {
        constexpr int n = bar_wait(P);
        static_assert(n >= 0 && n < 24, "wait count");
        if constexpr (P == 10) asm volatile("s_waitcnt vmcnt(%1)" : "+v"(ep) : "i"(n) : "memory");
        else if constexpr (P == 122) {
          asm volatile("s_waitcnt vmcnt(%1)" : "+v"(fl) : "i"(n) : "memory");
          if (w == 0) { if (__builtin_expect(!flags_ok(), 0)) poll(0); }
        } else wait_vm<n>();
        BAR();
        if (P == 2) { ST(3) } if (P == 26) { ST(4) } if (P == 58) { ST(5) } if (P == 74) { ST(6) } if (P == 90) { ST(7) } if (P == 106) { ST(8) } if (P == 122) { ST(9) }
      }
      if constexpr (P < 58 ? (P & 1) == 0 : (P & 3) == 2) __builtin_amdgcn_s_setprio(P < 58 ? 3 - (((P + 6) & 7) >> 1) : 3 - (((P + 6) & 15) >> 2));
      if constexpr (s == 0) mfma_new(acc[ps][b][a], join(wf[G & 3][0]), xf[s][b][0]); else mfma_acc(acc[ps][b][a], join(wf[G & 3][0]), xf[s][b][0]);
      SB();
      if constexpr (G + 3 < 64) ldw(G + 3, b, 0);
      if constexpr (P >= 124) df[0][P - 124].lo = ldd(0, P - 124, 0);
      {
        constexpr int k0 = first_op(P);
        if constexpr (k0 < first_op(P + 1) && SCH.op[k0].kind == K_DMA && SCH.op[k0].i == 0) set_m0(mu0 + uaddr(SCH.op[k0].n));
        else if constexpr (P < 56 && (P & 7) >= 2 && (P & 7) < 6) ldx(s + 1, ((P - 2) >> 1) & 1, P & 1);
      }
      SB();
      mfma_acc(acc[ps][b][a], join(wf[G & 3][1]), xf[s][b][1]);
      SB();
      if constexpr (G + 3 < 64) ldw(G + 3, b, 1);
      if constexpr (P >= 124) df[0][P - 124].hi = ldd(0, P - 124, 1);
      {
        constexpr int k0 = first_op(P);
        if constexpr (k0 < first_op(P + 1) && SCH.op[k0].kind == K_DMA && SCH.op[k0].i == 0 && P < 56 && (P & 7) >= 2 && (P & 7) < 6) ldx(s + 1, ((P - 2) >> 1) & 1, P & 1);
      }
      if constexpr (P == 120) set_db(1);
      if constexpr ((P & 63) == 62 || (P & 63) == 63) { epi(ps, P & 1, 0); epi(ps, P & 1, 1); }
      if constexpr (P == 65 || P == 66) { epi(0, P - 65, 2); epi(0, P - 65, 3); }
      if constexpr (P == 116) set_db(0);
      if constexpr (P == 118) set_db(2);
      if constexpr (P == 84) mktag();
      vmem(Z);
      SB();
    });
    ST(11)
    epi(1, 0, 2); epi(1, 0, 3);
    sth(1, 0);
    SB();
    epi(1, 1, 2); epi(1, 1, 3);
    sth(1, 1);
    issue_h(2, 0); issue_h(2, 1);
    __builtin_amdgcn_s_setprio(0);
    ST(12)
  } else {
    gload8_sc1(ep, 0u, srdH, syncb);
    set_db(0); set_db(1); set_db(2);
    {
      unsigned t = 0;
      const unsigned rotx = ((xcd & 6) + 4 * half) & 7;
      {
        const unsigned li = (ml * 4 + (w & 3)) * 56 + lane;
        const unsigned row = li / 7, c = li - row * 7;
        const unsigned vo = lane < 56 ? (rotx * 64 + row) * 3584 + (half * 14 + 7 + c) * 128 : 0x40000000u;
        asm volatile("buffer_load_dword %0, %1, %2, 0 offen" : "+v"(t) : "v"(vo), "s"(srdW) : "memory");
      }
#pragma unroll
      for (int i = 0; i < 3; ++i) {
        unsigned c = i + xcd; c = c >= 7 ? c - 7 : c; c = c >= 7 ? c - 7 : c;
        asm volatile("buffer_load_dword %0, %1, %2, %3 offen" : "+v"(t) : "v"(lane * 128), "s"(srdD), "s"(((c * 4 + ml) * 8 + w) * 8192) : "memory");
      }
      asm volatile("s_waitcnt vmcnt(0)" : "+v"(t), "+v"(ep)::"memory");
      mktag();
    }
#pragma unroll
    for (int s = 0; s < 7; ++s) { issue_wd(s, 0); issue_wd(s, 1); }
    if (w == 0) poll_slow(0); else wait_vm<0>();
    BAR();
#pragma unroll
    for (int s = 0; s < 3; ++s) { issue_h(s, 0); issue_h(s, 1); }
#pragma unroll
    for (int a = 0; a < 4; ++a) { df[0][a].lo = ldd(0, a, 0); df[0][a].hi = ldd(0, a, 1); }
    wait_vm<0>();
  }
  asm volatile("" : "+s"(mu0), "+s"(sd0), "+s"(sh2), "+s"(syncb), "+s"(rank));
  asm volatile("" : "+s"(kb[0]), "+s"(kb[1]), "+s"(kb[2]), "+s"(kb[3]), "+s"(kb[4]), "+s"(kb[5]), "+s"(kb[6]));
  fmask = rank == 7 ? 0xFFFFu : 0x7F7Fu;
  f32x4 acc2[2][4];
  const unsigned pa = opq(lds0 + ((kq + 8) % 10) * UNIT + (mb * 32 + (lane & 15)) * 256 + ((g ^ (lane & 7)) << 4));
  const unsigned pa1 = opq(pa ^ 64);
  const unsigned ra = opq(lds0 + (w * 8 + (lane >> 3)) * 256 + (((2 * (lane & 7)) ^ (lane >> 3)) << 4)), rb = opq(ra ^ 16);
  const unsigned ra8 = opq(ra + 8 * UNIT), rb8 = opq(rb + 8 * UNIT);
  const unsigned vo = opq((w * 8 + (lane >> 3)) * 1024 + (lane & 7) * 16), so = opqs(m64 * 65536 + n64 * 128);
  const u32x4 srdO = make_srd(out, M_ * D_ * 2);
  auto wpart = [&](int a) {
    const unsigned ad = ((a & 1) ? pa1 : pa) + (a >> 1) * 128;
    *(LDS_AS f32x4*)(uintptr_t)(ad) = acc2[0][a];
    *(LDS_AS f32x4*)(uintptr_t)(ad + 16 * 256) = acc2[1][a];
  };
  auto vmem2 = [&](auto PP) INL {
    sfor<first_op(PP.v), first_op(PP.v + 1)>([&](auto K) INL {
      constexpr Op o = SCH.op[K.v];
      if constexpr (o.pos == PP.v) {
        if constexpr (o.kind == K_DMA) issue_wd(o.n - 24, o.i);
        if constexpr (o.kind == K_FLAG) gstore8_sc1(tag, one, srdH, syncb + 128 * (17 + rank));
        if constexpr (o.kind == K_POLL) gload8_sc1(fl, fo, srdH, syncb + 2048);
        if constexpr (o.kind == K_H) issue_h(o.n, o.i);
      }
    });
  };
  sfor<0, 14>([&](auto S) INL {
    constexpr int s = S.v;
    if constexpr (s == Y1 || s == FCHK || s == Y2 || s == Y3) {
      constexpr int n = bar_wait(Q(s, 0));
      static_assert(n >= 0 && n < 24, "wait count");
      if constexpr (s == FCHK) {
        asm volatile("s_waitcnt vmcnt(%1)" : "+v"(fl) : "i"(n) : "memory");
        if (w == 0) { if (__builtin_expect(!flags_ok(), 0)) poll(2048); }
      } else wait_vm<n>();
      BAR();
      if (s == Y1) { ST(13) } if (s == FCHK) { ST(14) } if (s == Y2) { ST(15) } if (s == Y3) { ST(16) }
      if constexpr (s == Y3) { if (rank == 7 && w == 0) gstore8_sc1(tag, one, srdH, syncb); }
    }
    vmem2(IC<Q(s, 0)>{});
    if constexpr (s == Y1) asm volatile("" : "+v"(hf[s][0]), "+v"(hf[s][1])::"memory");
    else asm volatile("s_waitcnt vmcnt(%2)" : "+v"(hf[s][0]), "+v"(hf[s][1]) : "i"(younger_than(K_H, s, 1, Q(s, 1))) : "memory");
    if (s == 0) { ST(17) }
    SB();
    sfor<0, 4>([&](auto A) INL {
      constexpr int a = A.v;
      if constexpr (s == 0) mfma_new(acc2[0][a], join(df[0][a]), hf[0][0]); else mfma_acc(acc2[0][a], join(df[s & 1][a]), hf[s][0]);
      SB();
      if constexpr (s + 1 < 14) df[(s + 1) & 1][a].lo = ldd(s + 1, a, 0);
      SB();
      if constexpr (s == 0) mfma_new(acc2[1][a], join(df[0][a]), hf[0][1]); else mfma_acc(acc2[1][a], join(df[s & 1][a]), hf[s][1]);
      SB();
      if constexpr (s + 1 < 14) df[(s + 1) & 1][a].hi = ldd(s + 1, a, 1);
      vmem2(IC<Q(s, a + 1)>{});
      if constexpr (s == 13 && a == 3) { asm volatile("s_nop 1" ::: "memory"); SB(); wpart(0); SB(); wpart(1); }
      SB();
    });
  });
  ST(18)
  wpart(2);
  SB();
  asm volatile("s_nop 1" ::: "memory");
  SB();
  wpart(3);
  wait_lgkm0();
  BAR();
  ST(19)
  {
    f32x4 p0[4], p1[4];
#pragma unroll
    for (int v = 0; v < 4; ++v) {
      p0[v] = *(LDS_AS f32x4*)(uintptr_t)((v < 2 ? ra8 : ra) + (v & 1) * UNIT);
      p1[v] = *(LDS_AS f32x4*)(uintptr_t)((v < 2 ? rb8 : rb) + (v & 1) * UNIT);
    }
    SB();
    const f32x4 s0 = (p0[0] + p0[1]) + (p0[2] + p0[3]), s1 = (p1[0] + p1[1]) + (p1[2] + p1[3]);
    u32x4 o = {pack_bf16(s0[0], s0[1]), pack_bf16(s0[2], s0[3]), pack_bf16(s1[0], s1[1]), pack_bf16(s1[2], s1[3])};
    asm volatile("buffer_store_dwordx4 %0, %1, %2, %3 offen" ::"v"(o), "v"(vo), "s"(srdO), "s"(so) : "memory");
  }
  ST(20)
}

extern "C" void kernel_launch(void* const* d_in, const int* in_sizes, int n_in, void* d_out, int out_size,
                              void* d_ws, size_t ws_size, hipStream_t stream) {
  (void)in_sizes; (void)n_in; (void)out_size; (void)ws_size;
  (void)hipFuncSetAttribute((const void*)k_fused, hipFuncAttributeMaxDynamicSharedMemorySize, LDS_TOTAL);
  k_fused<<<256, 512, LDS_TOTAL, stream>>>(d_in[0], d_in[1], d_in[2], d_in[3], d_out, d_ws);
}
```

```cpp
#include <hip/hip_runtime.h>
#include <hip/hip_bf16.h>
#include <stdint.h>

typedef __attribute__((ext_vector_type(8))) __bf16 bf16x8;
typedef __attribute__((ext_vector_type(4))) short s16x4;
typedef __attribute__((ext_vector_type(8))) short s16x8;
typedef __attribute__((ext_vector_type(2))) __bf16 bf16x2;
typedef __attribute__((ext_vector_type(2))) float f32x2;
typedef __attribute__((ext_vector_type(4))) float f32x4;
typedef __attribute__((ext_vector_type(4))) unsigned u32x4;
typedef __attribute__((ext_vector_type(2))) unsigned u32x2;
#define LDS_AS __attribute__((address_space(3)))
#define ST_DECL
#define ST(i)
#define ST_FLUSH(a,b)

#define NPF 3
#define PFB 1
#define CSPLIT 0
constexpr int M_ = 2048, D_ = 512, F_ = 1792;
constexpr unsigned SYNC_OFF = 8u << 20;
constexpr unsigned SPIN_CAP = 1u << 17;
constexpr unsigned CODE_BYTES = 12544;

__device__ __forceinline__ unsigned rfl(unsigned v) { return __builtin_amdgcn_readfirstlane(v); }

__device__ __forceinline__ u32x4 make_srd(const void* p, unsigned bytes) {
  uint64_t a = (uint64_t)p;
  u32x4 r;
  r[0] = rfl((unsigned)a);
  r[1] = rfl((unsigned)(a >> 32)) & 0xffffu;
  r[2] = bytes;
  r[3] = 0x00020000u;
  return r;
}

template <int IMM>
__device__ __forceinline__ void dma16(unsigned m0v, unsigned voff, u32x4 srd, unsigned soff) {
  asm volatile("s_mov_b32 m0, %0\n\ts_nop 0\n\tbuffer_load_dwordx4 %1, %2, %3 offen offset:%4 lds"
               ::"s"(m0v), "v"(voff), "s"(srd), "s"(soff), "i"(IMM) : "memory");
}
template <int IMM>
__device__ __forceinline__ void dma16n(unsigned voff, u32x4 srd, unsigned soff) {
  asm volatile("buffer_load_dwordx4 %0, %1, %2 offen offset:%3 lds" ::"v"(voff), "s"(srd), "s"(soff), "i"(IMM) : "memory");
}
__device__ __forceinline__ void set_m0(unsigned v) { asm volatile("s_mov_b32 m0, %0" ::"s"(v) : "memory"); }
__device__ __forceinline__ unsigned opq(unsigned v) { asm volatile("" : "+v"(v)); return v; }
__device__ __forceinline__ unsigned opqs(unsigned v) { asm volatile("" : "+s"(v)); return v; }
__device__ __forceinline__ void gload16_sc1(u32x4& dst, unsigned voff, u32x4 srd, unsigned soff) {
  asm volatile("buffer_load_dwordx4 %0, %1, %2, %3 offen sc1" : "=v"(dst) : "v"(voff), "s"(srd), "s"(soff) : "memory");
}
__device__ __forceinline__ void gstore16_sc1(u32x4 v, unsigned voff, u32x4 srd, unsigned soff) {
  asm volatile("buffer_store_dwordx4 %0, %1, %2, %3 offen sc1\n\ts_nop 1" ::"v"(v), "v"(voff), "s"(srd), "s"(soff) : "memory");
}
__device__ __forceinline__ void gload8_sc1(u32x2& dst, unsigned voff, u32x4 srd, unsigned soff) {
  asm volatile("buffer_load_dwordx2 %0, %1, %2, %3 offen sc1" : "=v"(dst) : "v"(voff), "s"(srd), "s"(soff) : "memory");
}
__device__ __forceinline__ void gstore8_sc1(u32x2 v, unsigned voff, u32x4 srd, unsigned soff) {
  asm volatile("buffer_store_dwordx2 %0, %1, %2, %3 offen sc1" ::"v"(v), "v"(voff), "s"(srd), "s"(soff) : "memory");
}

__device__ __forceinline__ s16x4 tr_read(unsigned addr) {
  return __builtin_amdgcn_ds_read_tr16_b64_v4i16((LDS_AS s16x4*)(uintptr_t)addr);
}
struct Frag { s16x4 lo, hi; };
__device__ __forceinline__ bf16x8 join(const Frag& f) {
  s16x8 v = __builtin_shufflevector(f.lo, f.hi, 0, 1, 2, 3, 4, 5, 6, 7);
  return __builtin_bit_cast(bf16x8, v);
}
__device__ __forceinline__ bf16x8 lds_read16(unsigned addr) { return *(LDS_AS bf16x8*)(uintptr_t)addr; }
__device__ __forceinline__ unsigned pack_bf16(float a, float b) {
  f32x2 v = {a, b};
  bf16x2 r = __builtin_convertvector(v, bf16x2);
  return __builtin_bit_cast(unsigned, r);
}
template <class TB>
__device__ __forceinline__ void mfma_acc(f32x4& c, bf16x8 a, TB b) {
  asm volatile("v_mfma_f32_16x16x32_bf16 %0, %1, %2, %0" : "+v"(c) : "v"(a), "v"(b));
}
template <class TB>
__device__ __forceinline__ void mfma_new(f32x4& c, bf16x8 a, TB b) {
  asm volatile("v_mfma_f32_16x16x32_bf16 %0, %1, %2, 0" : "=&v"(c) : "v"(a), "v"(b));
}

template <int I> struct IC { static constexpr int v = I; };
template <int I, int N, class F> __device__ __forceinline__ void sfor(F&& f) {
  if constexpr (I < N) { f(IC<I>{}); sfor<I + 1, N>(f); }
}
template <int N> __device__ __forceinline__ void wait_vm() { asm volatile("s_waitcnt vmcnt(%0)" ::"i"(N) : "memory"); }
__device__ __forceinline__ void wait_lgkm0() { asm volatile("s_waitcnt lgkmcnt(0)" ::: "memory"); }
#define BAR() asm volatile("s_barrier" ::: "memory")
#define SB() __builtin_amdgcn_sched_barrier(0)
#define INL __attribute__((always_inline))

constexpr int UNIT = 16384, LDS_TOTAL = 10 * UNIT;
constexpr int uaddr(int n) { return (n % 10) * UNIT; }

enum { K_DMA = 1, K_STORE, K_FLAG, K_POLL, K_H };
struct Op { int pos, kind, n, i; };
struct Sched { int cnt; Op op[200]; };
constexpr void put(Sched& s, int pos, int kind, int n, int i) { s.op[s.cnt++] = Op{pos, kind, n, i}; }
constexpr void spread(Sched& s, int p0, int p1, int u0, int nu) {
  for (int k = 0; k < 2 * nu; ++k) put(s, p0 + (k * (p1 - p0)) / (2 * nu), K_DMA, u0 + (k >> 1), k & 1);
}
constexpr int Q(int step, int slot) { return 200 + 8 * step + slot; }
constexpr void pair(Sched& s, int step, int slot, int kind, int n) { put(s, Q(step, slot), kind, n, 0); put(s, Q(step, slot == 0 ? 0 : slot + 1), kind, n, 1); }
constexpr int HB = 3;
constexpr int Y1 = 2, Y2 = 5, FCHK = 7, Y3 = 10;
constexpr Sched make_sched() {
  Sched s{};
  for (int n = 0; n < 4; ++n) { put(s, -1, K_DMA, n, 0); put(s, -1, K_DMA, n, 1); }
  put(s, -1, K_POLL, 0, 0);
  spread(s, 0, 2, 4, 2);
  for (int t = 0; t < 7; ++t) spread(s, 8 * t + 2, 8 * t + 9, 2 * t + 6, 2);
  spread(s, 58, 64, 20, 2);
  put(s, 65, K_STORE, 0, 0);
  put(s, 66, K_STORE, 0, 1);
  spread(s, 72, 80, 22, 1);
  spread(s, 82, 89, 23, 2);
  put(s, 90, K_FLAG, 0, 0);
  spread(s, 91, 103, 25, 2);
  spread(s, 103, 111, 27, 2);
  put(s, 111, K_POLL, 1, 0);
  spread(s, 112, 121, 29, 2);
  for (int k = 0; k < 4; ++k) put(s, 122 + k, K_H, k >> 1, k & 1);
  put(s, 128, K_STORE, 1, 0);
  put(s, 128, K_STORE, 1, 1);
  put(s, 128, K_H, 2, 0);
  put(s, 128, K_H, 2, 1);
  put(s, Q(Y1, 0), K_FLAG, 1, 0);
  pair(s, 2, 0, K_H, 3); pair(s, 2, 0, K_H, 4);
  pair(s, 2, 3, K_DMA, 31);
  pair(s, 3, 1, K_H, 5); pair(s, 3, 3, K_H, 6);
  pair(s, 4, 1, K_DMA, 32); pair(s, 4, 3, K_DMA, 33);
  pair(s, 5, 1, K_DMA, 34); pair(s, 5, 3, K_DMA, 35);
  put(s, Q(6, 0), K_POLL, 2, 0);
  pair(s, 6, 1, K_DMA, 36); pair(s, 6, 3, K_DMA, 37);
  for (int k = 0; k < HB; ++k) pair(s, 7, 0, K_H, 7 + k);
  for (int k = 7 + HB; k < 14; ++k) pair(s, k - HB, 1, K_H, k);
  return s;
}
constexpr Sched SCH = make_sched();
constexpr int younger_than(int kind, int n, int i, int P) {
  int c = -1;
  for (int k = 0; k < SCH.cnt; ++k) {
    if (SCH.op[k].pos >= P) break;
    if (SCH.op[k].kind == kind && SCH.op[k].n == n && SCH.op[k].i == i) c = 0;
    else if (c >= 0 && SCH.op[k].kind != K_FLAG && !(SCH.op[k].kind == K_POLL && SCH.op[k].n > 0)) ++c;
  }
  return c;
}
constexpr int first_op(int P) { int k = 0; while (k < SCH.cnt && SCH.op[k].pos < P) ++k; return k; }
constexpr bool is_bar(int P) { return P < 64 ? (P & 7) == 2 : (P & 15) == 10; }
constexpr int bar_wait(int P) {
  if (P < 64) return younger_than(K_DMA, 2 * (P >> 3) + 3, 1, P);
  if (P == 74) return younger_than(K_DMA, 19, 1, P);
  if (P == 90) return younger_than(K_STORE, 0, 1, P);
  if (P == 106) return younger_than(K_DMA, 23, 1, P);
  if (P == 122) return younger_than(K_POLL, 1, 0, P);
  if (P == Q(Y1, 0)) return 0;
  if (P == Q(Y2, 0)) return younger_than(K_DMA, 31, 1, P);
  if (P == Q(FCHK, 0)) return younger_than(K_POLL, 2, 0, P);
  return younger_than(K_DMA, 37, 1, P);
}
constexpr bool sched_sorted() { for (int k = 1; k < SCH.cnt; ++k) if (SCH.op[k].pos < SCH.op[k - 1].pos) return false; return true; }
static_assert(sched_sorted(), "schedule order");

__global__ __launch_bounds__(512, 2) void k_fused(const void* __restrict__ x, const void* __restrict__ Wg,
                                                  const void* __restrict__ Wu, const void* __restrict__ Wd,
                                                  void* __restrict__ out, void* __restrict__ ws) {
  extern __shared__ __attribute__((aligned(16))) char smem[];
  const unsigned lds0 = (unsigned)(uintptr_t)(LDS_AS char*)smem;
  ST_DECL ST(0)
  const unsigned bid = blockIdx.x;
  const unsigned w = rfl(threadIdx.x >> 6);
  const bool busy = bid < 224;
  uint64_t pc;
  asm volatile("s_getpc_b64 %0" : "=s"(pc));
  u32x2 px, pw;
  asm volatile("s_load_dwordx2 %0, %2, 0x0\n\ts_load_dwordx2 %1, %2, %3"
               : "=&s"(px), "=&s"(pw) : "s"(__builtin_amdgcn_kernarg_segment_ptr()), "s"(8 + 8 * (w >> 2)));
  SB();
  const unsigned mi = (bid & 6) * 2 + ((bid >> 3) & 3), half = bid & 1, idx = bid >> 5;
  const unsigned rot = ((bid & 6) + 4 * half + ((bid >> 3) & 1) + (idx & 3)) & 7;
  unsigned vw0, vw1, vx0, vx1;
  {
    const unsigned lane = threadIdx.x & 63, g = lane >> 4;
    vw0 = (lane >> 3) * 3584 + (((lane & 7) ^ ((g & 1) << 1)) << 4);
    vw1 = (vw0 ^ 64) + 8 * 3584 - 1024;
    vx0 = (lane >> 3) * 1024 + (((lane & 7) ^ g) << 4);
    vx1 = (vx0 ^ 64) + 8192 - 1024;
  }
  unsigned sx0 = (mi * 128 + 16 * w) * 1024;
  unsigned sw0 = (16 * (w & 3)) * 3584 + (half * 14 + idx) * 128;
  unsigned mu0 = lds0 + w * 2048;
  unsigned sw00 = sw0 + rot * (64 * 3584), sx00 = sx0 + rot * 128;
  asm volatile("" : "+v"(vw0), "+v"(vw1), "+v"(vx0), "+v"(vx1));
  asm volatile("" : "+s"(sx0), "+s"(sw0), "+s"(mu0), "+s"(sw00), "+s"(sx00));
  SB();
  asm volatile("s_waitcnt lgkmcnt(0)" : "+s"(px), "+s"(pw));
  const u32x4 srdX = {px[0], px[1] & 0xffffu, M_ * D_ * 2, 0x00020000u};
  const u32x4 srdW = {pw[0], pw[1] & 0xffffu, D_ * F_ * 2, 0x00020000u};
  auto issue_xw = [&](int n, int i, bool pre = false) {
    const int s = n < 16 ? n >> 1 : n - 16;
    const unsigned sg = (s + rot) & 7;
    if (n < 16 && (n & 1) == 0) {
      const unsigned so = n == 0 ? sx00 : sx0 + sg * 128;
      if (i == 0) { if (pre) dma16n<0>(vx0, srdX, so); else dma16<0>(mu0 + uaddr(n), vx0, srdX, so); } else dma16n<1024>(vx1, srdX, so);
    } else {
      const unsigned so = n == 1 ? sw00 : sw0 + sg * (64 * 3584) + (n >= 16 ? 896 : 0);
      if (i == 0) { if (pre) dma16n<0>(vw0, srdW, so); else dma16<0>(mu0 + uaddr(n), vw0, srdW, so); } else dma16n<1024>(vw1, srdW, so);
    }
  };
  if (busy) {
    issue_xw(0, 0); issue_xw(0, 1); issue_xw(1, 0); issue_xw(1, 1);
    BAR();
    issue_xw(2, 0); issue_xw(2, 1); issue_xw(3, 0); issue_xw(3, 1);
  } else {
    const unsigned o = min(threadIdx.x * 64u, CODE_BYTES);
    asm volatile("global_load_dword v249, %0, %1" ::"v"(o), "s"(pc) : "v249", "memory");
  }
  SB();
  const unsigned tid = opq(threadIdx.x), lane = tid & 63;
  const unsigned bid2 = opqs(bid), xcd = bid2 & 7, ml = (bid2 >> 3) & 3;
  unsigned rank = half * 8 + idx;
  const unsigned m64 = 2 * mi + half, n64 = idx;
  const unsigned g = lane >> 4, q = (lane & 15) >> 2, p = lane & 3;
  const u32x4 srdH = make_srd(ws, SYNC_OFF + 16 * 8192);
  const u32x4 srdD = make_srd(Wd, F_ * D_ * 2);
  unsigned syncb = SYNC_OFF + mi * 8192;
  const unsigned lane16 = lane * 16;

  const unsigned kq = w >> 1, mb = w & 1;
  unsigned rot2 = 2 * ml + idx; rot2 = rot2 >= 7 ? rot2 - 7 : rot2;
  unsigned kb[7];
  auto set_kb = [&]() {
    unsigned e = opqs(rot2);
#pragma unroll
    for (int j = 0; j < 7; ++j) {
      kb[j] = opqs((e + 7 * kq + (kq >> 1) * 14) * 1024);
      e = e == 6 ? 0 : e + 1;
    }
  };
  auto kap = [&](int s) { return kb[s % 7] + (s >= 7 ? 14 * 1024 : 0); };
  const unsigned vd0 = (lane >> 3) * 1024 + (((lane & 7) ^ (g << 1)) << 4);
  const unsigned vd1 = vd0 + 8192 - 1024;
  unsigned sd0 = (16 * mb) * 1024 + n64 * 128;
  auto issue_wd = [&](int s, int i, bool pre = false) {
    if (i == 0) { if (pre) dma16n<0>(vd0, srdD, sd0 + kap(s) * 32); else dma16<0>(mu0 + uaddr(24 + s), vd0, srdD, sd0 + kap(s) * 32); }
    else dma16n<1024>(vd1, srdD, sd0 + kap(s) * 32);
  };
  const unsigned dl = lds0 + kq * 4096 + (4 * g + q) * 128 + (((2 * g + (q >> 1)) & 3) << 5) + p * 8;
  unsigned db[3][4];
  auto set_db = [&](int k) {
#pragma unroll
    for (int c = 0; c < 4; ++c) db[k][c] = opq((dl ^ (c << 5)) + k * 4 * UNIT);
  };
  auto ldd = [&](int s, int a, int u) {
    const int ua = uaddr(24 + s);
    return tr_read(db[ua >> 16][a] + (ua & 65535) + u * 2048);
  };
  u32x2 ep, tag, fl;
  auto mktag = [&]() {
    const uint64_t t = (((uint64_t)ep[1] << 32) | ep[0]) * 0x9E3779B97F4A7C15ull + 1ull;
    tag = u32x2{(unsigned)t, (unsigned)(t >> 32)};
  };
  const unsigned one = tid == 0 ? 0u : 0x40000000u;
  const unsigned fo = tid < 16 ? 128 * (1 + tid) : 0x40000000u;
  unsigned fmask = 0x7F7Fu;
  auto flags_ok = [&]() { return (rfl((unsigned)__builtin_amdgcn_ballot_w64(fl[0] == tag[0] && fl[1] == tag[1])) & fmask) == fmask; };
  auto poll = [&](unsigned off) {
    unsigned cnt = SPIN_CAP, t;
#define PLD(r) "buffer_load_dwordx2 " r ", %2, %3, %4 offen sc1\n\t"
#define PCK(r) "s_waitcnt vmcnt(2)\n\tv_cmp_eq_u64 vcc, " r ", %5\n\ts_nop 1\n\ts_and_b32 %1, vcc_lo, %6\n\ts_cmp_eq_u32 %1, %6\n\ts_cbranch_scc1 2f\n\t"
    asm volatile(PLD("v[250:251]") "s_sleep 3\n\t" PLD("v[252:253]") "s_sleep 3\n\t" PLD("v[254:255]")
                 "1:\n\t"
                 PCK("v[250:251]") PLD("v[250:251]") PCK("v[252:253]") PLD("v[252:253]") PCK("v[254:255]") PLD("v[254:255]")
                 "s_sub_u32 %0, %0, 1\n\ts_cbranch_scc0 1b\n\t"
                 "2:"
                 : "+s"(cnt), "=&s"(t) : "v"(fo), "s"(srdH), "s"(syncb + off), "v"(tag), "s"(fmask)
                 : "v250", "v251", "v252", "v253", "v254", "v255", "vcc", "scc", "memory");
#undef PLD
#undef PCK
  };
  auto poll_slow = [&](unsigned off) {
#pragma nounroll
    for (unsigned spin = 0; spin < SPIN_CAP; ++spin) {
      gload8_sc1(fl, fo, srdH, syncb + off);
      asm volatile("s_waitcnt vmcnt(0)" : "+v"(fl)::"memory");
      if (flags_ok()) break;
      __builtin_amdgcn_s_sleep(8);
    }
  };
  Frag df[2][4];
  u32x4 hf[14][2];
  unsigned sh2 = ((m64 * 4 + mb * 2) * 56) * 1024;
  const unsigned lane16b = lane16 + 56 * 1024;
  auto issue_h = [&](int s, int b) { gload16_sc1(hf[s][b], b ? lane16b : lane16, srdH, sh2 + kap(s)); };

  set_kb();
  if (__builtin_expect(busy, 1)) {
    gload8_sc1(ep, 0u, srdH, syncb);
    const unsigned wm = w & 3, wn = w >> 2;
    const unsigned xrow = 32 * wm + (lane & 15);
    const unsigned xa0 = lds0 + xrow * 128 + ((g ^ ((xrow >> 1) & 7)) << 4);
    const unsigned wa0 = lds0 + (8 * g + q) * 128 + (((2 * wn) ^ ((q >> 1) | ((g & 1) << 1))) << 5) + p * 8;
    unsigned xa[3][2], wa[3][2];
#pragma unroll
    for (int k = 0; k < 3; ++k)
#pragma unroll
      for (int c = 0; c < 2; ++c) { xa[k][c] = opq((xa0 ^ (c << 6)) + k * 4 * UNIT); wa[k][c] = opq((wa0 ^ (c << 5)) + k * 4 * UNIT); }
    ST(1)

    f32x4 acc[2][2][4];
    bf16x8 xf[8][2][2];
    Frag wf[4][2];
    auto ldx = [&](int s, int b, int kk) { const int ua = uaddr(2 * s); xf[s][b][kk] = lds_read16(xa[ua >> 16][kk] + (ua & 65535) + b * 2048); };
    auto ldw = [&](int G, int kk, int u = 2) {
      const int st = G >> 2, a = ((G & 1) << 1) | ((G >> 1) & 1), ua = uaddr(st < 8 ? 2 * st + 1 : 8 + st);
      const unsigned ad = wa[ua >> 16][a & 1] + (ua & 65535) + (a >> 1) * 8192 + kk * 4096;
      if (u != 1) wf[G & 3][kk].lo = tr_read(ad);
      if (u != 0) wf[G & 3][kk].hi = tr_read(ad + 512);
    };
    const unsigned sh = ((mi * 8 + wm * 2) * 56 + half * 28 + idx * 2 + wn) * 1024;
    u32x4 ho[2];
    auto epi = [&](int ps, int b, int c) {
      const int t = c >> 1, i = c & 1;
      float g0 = __builtin_elementwise_maximum(acc[ps][b][t][2 * i], 0.f), g1 = __builtin_elementwise_maximum(acc[ps][b][t][2 * i + 1], 0.f);
      float p0 = g0 * acc[ps][b][2 + t][2 * i];
      asm volatile("" : "+v"(p0));
      ho[b][c] = pack_bf16(p0, g1 * acc[ps][b][2 + t][2 * i + 1]);
    };
    auto sth = [&](int ps, int b) { gstore16_sc1(ho[b], lane16, srdH, sh + b * (56 * 1024) + ps * (14 * 1024)); };

    auto vmem = [&](auto PP) INL {
      sfor<first_op(PP.v), first_op(PP.v + 1)>([&](auto K) INL {
        constexpr Op o = SCH.op[K.v];
        if constexpr (o.pos == PP.v) {
          constexpr bool pre = PP.v < 128 && K.v == first_op(PP.v);
          if constexpr (o.kind == K_DMA) { if constexpr (o.n < 24) issue_xw(o.n, o.i, pre); else issue_wd(o.n - 24, o.i, pre); }
          if constexpr (o.kind == K_STORE) sth(o.n, o.i);
          if constexpr (o.kind == K_FLAG) gstore8_sc1(tag, one, srdH, syncb + 128 * (1 + rank));
          if constexpr (o.kind == K_POLL) gload8_sc1(fl, fo, srdH, syncb);
          if constexpr (o.kind == K_H) issue_h(o.n, o.i);
        }
      });
    };
    wait_vm<5>();
    ST(2)
    BAR();
    ldw(0, 0); ldx(0, 0, 0); ldw(0, 1); ldx(0, 0, 1); ldx(0, 1, 0); ldx(0, 1, 1); ldw(1, 0); ldw(1, 1); ldw(2, 0); ldw(2, 1);
    SB();
    sfor<0, 128>([&](auto Z) INL {
      constexpr int P = Z.v, ps = P >> 6, s = (P >> 3) & 7, G = P >> 1, a = ((G & 1) << 1) | ((G >> 1) & 1), b = P & 1;
      if constexpr (is_bar(P)) {
        constexpr int n = bar_wait(P);
        static_assert(n >= 0 && n < 24, "wait count");
        if constexpr (P == 10) asm volatile("s_waitcnt vmcnt(%1)" : "+v"(ep) : "i"(n) : "memory");
        else if constexpr (P == 122) {
          asm volatile("s_waitcnt vmcnt(%1)" : "+v"(fl) : "i"(n) : "memory");
          if (w == 0) { if (__builtin_expect(!flags_ok(), 0)) poll(0); }
        } else wait_vm<n>();
        BAR();
        if (P == 2) { ST(3) } if (P == 26) { ST(4) } if (P == 58) { ST(5) } if (P == 74) { ST(6) } if (P == 90) { ST(7) } if (P == 106) { ST(8) } if (P == 122) { ST(9) }
      }
      if constexpr (P < 58 ? (P & 1) == 0 : (P & 3) == 2) __builtin_amdgcn_s_setprio(P < 58 ? 3 - (((P + 6) & 7) >> 1) : 3 - (((P + 6) & 15) >> 2));
      if constexpr (s == 0) mfma_new(acc[ps][b][a], join(wf[G & 3][0]), xf[s][b][0]); else mfma_acc(acc[ps][b][a], join(wf[G & 3][0]), xf[s][b][0]);
      SB();
      if constexpr (G + 3 < 64) ldw(G + 3, b, 0);
      if constexpr (P >= 124) df[0][P - 124].lo = ldd(0, P - 124, 0);
      {
        constexpr int k0 = first_op(P);
        if constexpr (k0 < first_op(P + 1) && SCH.op[k0].kind == K_DMA && SCH.op[k0].i == 0) set_m0(mu0 + uaddr(SCH.op[k0].n));
        else if constexpr (P < 56 && (P & 7) >= 2 && (P & 7) < 6) ldx(s + 1, ((P - 2) >> 1) & 1, P & 1);
      }
      SB();
      mfma_acc(acc[ps][b][a], join(wf[G & 3][1]), xf[s][b][1]);
      SB();
      if constexpr (G + 3 < 64) ldw(G + 3, b, 1);
      if constexpr (P >= 124) df[0][P - 124].hi = ldd(0, P - 124, 1);
      {
        constexpr int k0 = first_op(P);
        if constexpr (k0 < first_op(P + 1) && SCH.op[k0].kind == K_DMA && SCH.op[k0].i == 0 && P < 56 && (P & 7) >= 2 && (P & 7) < 6) ldx(s + 1, ((P - 2) >> 1) & 1, P & 1);
      }
      if constexpr (P == 120) set_db(1);
      if constexpr ((P & 63) == 62 || (P & 63) == 63) { epi(ps, P & 1, 0); epi(ps, P & 1, 1); }
      if constexpr (P == 65 || P == 66) { epi(0, P - 65, 2); epi(0, P - 65, 3); }
      if constexpr (P == 116) set_db(0);
      if constexpr (P == 118) set_db(2);
      if constexpr (P == 84) mktag();
      vmem(Z);
      SB();
    });
    ST(11)
    epi(1, 0, 2); epi(1, 0, 3);
    sth(1, 0);
    SB();
    epi(1, 1, 2); epi(1, 1, 3);
    sth(1, 1);
    issue_h(2, 0); issue_h(2, 1);
    __builtin_amdgcn_s_setprio(0);
    ST(12)
  } else {
    gload8_sc1(ep, 0u, srdH, syncb);
    set_db(0); set_db(1); set_db(2);
    {
      unsigned t = 0;
      const unsigned rotx = ((xcd & 6) + 4 * half) & 7;
      if (PFB) {
        const unsigned li = (ml * 4 + (w & 3)) * 56 + lane;
        const unsigned row = li / 7, c = li - row * 7;
        const unsigned vo = lane < 56 ? (rotx * 64 + row) * 3584 + (half * 14 + 7 + c) * 128 : 0x40000000u;
        asm volatile("buffer_load_dword %0, %1, %2, 0 offen" : "+v"(t) : "v"(vo), "s"(srdW) : "memory");
      }
#pragma unroll
      for (int i = 0; i < NPF; ++i) {
        unsigned c = i + xcd; c = c >= 7 ? c - 7 : c; c = c >= 7 ? c - 7 : c;
        asm volatile("buffer_load_dword %0, %1, %2, %3 offen" : "+v"(t) : "v"(lane * 128), "s"(srdD), "s"(((c * 4 + ml) * 8 + w) * 8192) : "memory");
      }
      asm volatile("s_waitcnt vmcnt(0)" : "+v"(t), "+v"(ep)::"memory");
      mktag();
    }
#pragma unroll
    for (int s = 0; s < 7; ++s) { issue_wd(s, 0); issue_wd(s, 1); }
    if (w == 0) poll_slow(0); else wait_vm<0>();
    BAR();
#pragma unroll
    for (int s = 0; s < 3; ++s) { issue_h(s, 0); issue_h(s, 1); }
#pragma unroll
    for (int a = 0; a < 4; ++a) { df[0][a].lo = ldd(0, a, 0); df[0][a].hi = ldd(0, a, 1); }
    wait_vm<0>();
  }
  asm volatile("" : "+s"(mu0), "+s"(sd0), "+s"(sh2), "+s"(syncb), "+s"(rank));
  asm volatile("" : "+s"(kb[0]), "+s"(kb[1]), "+s"(kb[2]), "+s"(kb[3]), "+s"(kb[4]), "+s"(kb[5]), "+s"(kb[6]));
  fmask = rank == 7 ? 0xFFFFu : 0x7F7Fu;
  f32x4 acc2[2][4];
  const unsigned pa = opq(lds0 + ((kq + 8) % 10) * UNIT + (mb * 32 + (lane & 15)) * 256 + ((g ^ (lane & 7)) << 4));
  const unsigned pa1 = opq(pa ^ 64);
  const unsigned ra = opq(lds0 + (w * 8 + (lane >> 3)) * 256 + (((2 * (lane & 7)) ^ (lane >> 3)) << 4)), rb = opq(ra ^ 16);
  const unsigned ra8 = opq(ra + 8 * UNIT), rb8 = opq(rb + 8 * UNIT);
  const unsigned vo = opq((w * 8 + (lane >> 3)) * 1024 + (lane & 7) * 16), so = opqs(m64 * 65536 + n64 * 128);
  const u32x4 srdO = make_srd(out, M_ * D_ * 2);
  auto wpart = [&](int a) {
    const unsigned ad = ((a & 1) ? pa1 : pa) + (a >> 1) * 128;
    *(LDS_AS f32x4*)(uintptr_t)(ad) = acc2[0][a];
    *(LDS_AS f32x4*)(uintptr_t)(ad + 16 * 256) = acc2[1][a];
  };
  auto vmem2 = [&](auto PP) INL {
    sfor<first_op(PP.v), first_op(PP.v + 1)>([&](auto K) INL {
      constexpr Op o = SCH.op[K.v];
      if constexpr (o.pos == PP.v) {
        if constexpr (o.kind == K_DMA) issue_wd(o.n - 24, o.i);
        if constexpr (o.kind == K_FLAG) gstore8_sc1(tag, one, srdH, syncb + 128 * (17 + rank));
        if constexpr (o.kind == K_POLL) gload8_sc1(fl, fo, srdH, syncb + 2048);
        if constexpr (o.kind == K_H) issue_h(o.n, o.i);
      }
    });
  };
  sfor<0, 14>([&](auto S) INL {
    constexpr int s = S.v;
    if constexpr (s == Y1 || s == FCHK || s == Y2 || s == Y3) {
      constexpr int n = bar_wait(Q(s, 0));
      static_assert(n >= 0 && n < 24, "wait count");
      if constexpr (s == FCHK) {
        asm volatile("s_waitcnt vmcnt(%1)" : "+v"(fl) : "i"(n) : "memory");
        if (w == 0) { if (__builtin_expect(!flags_ok(), 0)) poll(2048); }
      } else wait_vm<n>();
      BAR();
      if (s == Y1) { ST(13) } if (s == FCHK) { ST(14) } if (s == Y2) { ST(15) } if (s == Y3) { ST(16) }
      if constexpr (s == Y3) { if (rank == 7 && w == 0) gstore8_sc1(tag, one, srdH, syncb); }
    }
    vmem2(IC<Q(s, 0)>{});
    if constexpr (s == Y1) asm volatile("" : "+v"(hf[s][0]), "+v"(hf[s][1])::"memory");
    else asm volatile("s_waitcnt vmcnt(%2)" : "+v"(hf[s][0]), "+v"(hf[s][1]) : "i"(younger_than(K_H, s, 1, Q(s, 1))) : "memory");
    if (s == 0) { ST(17) }
    SB();
    sfor<0, 4>([&](auto A) INL {
      constexpr int a = A.v;
      if constexpr (s == 0) mfma_new(acc2[0][a], join(df[0][a]), hf[0][0]); else mfma_acc(acc2[0][a], join(df[s & 1][a]), hf[s][0]);
      SB();
      if constexpr (s + 1 < 14) df[(s + 1) & 1][a].lo = ldd(s + 1, a, 0);
      SB();
      if constexpr (s == 0) mfma_new(acc2[1][a], join(df[0][a]), hf[0][1]); else mfma_acc(acc2[1][a], join(df[s & 1][a]), hf[s][1]);
      SB();
      if constexpr (s + 1 < 14) df[(s + 1) & 1][a].hi = ldd(s + 1, a, 1);
      vmem2(IC<Q(s, a + 1)>{});
      if constexpr (s == 13 && a == 3) { asm volatile("s_nop 1" ::: "memory"); SB(); wpart(0); SB(); wpart(1); }
      SB();
    });
  });
  ST(18)
  wpart(2);
  SB();
  asm volatile("s_nop 1" ::: "memory");
  SB();
  wpart(3);
  wait_lgkm0();
  BAR();
  ST(19)
  {
    f32x4 p0[4], p1[4];
#pragma unroll
    for (int v = 0; v < 4; ++v) {
      p0[v] = *(LDS_AS f32x4*)(uintptr_t)((v < 2 ? ra8 : ra) + (v & 1) * UNIT);
      p1[v] = *(LDS_AS f32x4*)(uintptr_t)((v < 2 ? rb8 : rb) + (v & 1) * UNIT);
    }
    SB();
    const f32x4 s0 = (p0[0] + p0[1]) + (p0[2] + p0[3]), s1 = (p1[0] + p1[1]) + (p1[2] + p1[3]);
    u32x4 o = {pack_bf16(s0[0], s0[1]), pack_bf16(s0[2], s0[3]), pack_bf16(s1[0], s1[1]), pack_bf16(s1[2], s1[3])};
    asm volatile("buffer_store_dwordx4 %0, %1, %2, %3 offen" ::"v"(o), "v"(vo), "s"(srdO), "s"(so) : "memory");
  }
  ST(20)
}

extern "C" void kernel_launch(void* const* d_in, const int* in_sizes, int n_in, void* d_out, int out_size,
                              void* d_ws, size_t ws_size, hipStream_t stream) {
  (void)in_sizes; (void)n_in; (void)out_size; (void)ws_size;
  (void)hipFuncSetAttribute((const void*)k_fused, hipFuncAttributeMaxDynamicSharedMemorySize, LDS_TOTAL);
  k_fused<<<256, 512, LDS_TOTAL, stream>>>(d_in[0], d_in[1], d_in[2], d_in[3], d_out, d_ws);
}
```

```cpp
#include <hip/hip_runtime.h>
#include <hip/hip_bf16.h>
#include <stdint.h>

typedef __attribute__((ext_vector_type(8))) __bf16 bf16x8;
typedef __attribute__((ext_vector_type(4))) short s16x4;
typedef __attribute__((ext_vector_type(8))) short s16x8;
typedef __attribute__((ext_vector_type(2))) __bf16 bf16x2;
typedef __attribute__((ext_vector_type(2))) float f32x2;
typedef __attribute__((ext_vector_type(4))) float f32x4;
typedef __attribute__((ext_vector_type(4))) unsigned u32x4;
typedef __attribute__((ext_vector_type(2))) unsigned u32x2;
#define LDS_AS __attribute__((address_space(3)))
#define ST_DECL
#define ST(i)
#define ST_FLUSH(a,b)

#define SLP 8
constexpr int M_ = 2048, D_ = 512, F_ = 1792;
constexpr unsigned SYNC_OFF = 8u << 20;
constexpr unsigned SPIN_CAP = 1u << 17;
constexpr unsigned CODE_BYTES = 12544;

__device__ __forceinline__ unsigned rfl(unsigned v) { return __builtin_amdgcn_readfirstlane(v); }

__device__ __forceinline__ u32x4 make_srd(const void* p, unsigned bytes) {
  uint64_t a = (uint64_t)p;
  u32x4 r;
  r[0] = rfl((unsigned)a);
  r[1] = rfl((unsigned)(a >> 32)) & 0xffffu;
  r[2] = bytes;
  r[3] = 0x00020000u;
  return r;
}

template <int IMM>
__device__ __forceinline__ void dma16(unsigned m0v, unsigned voff, u32x4 srd, unsigned soff) {
  asm volatile("s_mov_b32 m0, %0\n\ts_nop 0\n\tbuffer_load_dwordx4 %1, %2, %3 offen offset:%4 lds"
               ::"s"(m0v), "v"(voff), "s"(srd), "s"(soff), "i"(IMM) : "memory");
}
template <int IMM>
__device__ __forceinline__ void dma16n(unsigned voff, u32x4 srd, unsigned soff) {
  asm volatile("buffer_load_dwordx4 %0, %1, %2 offen offset:%3 lds" ::"v"(voff), "s"(srd), "s"(soff), "i"(IMM) : "memory");
}
__device__ __forceinline__ void set_m0(unsigned v) { asm volatile("s_mov_b32 m0, %0" ::"s"(v) : "memory"); }
__device__ __forceinline__ unsigned opq(unsigned v) { asm volatile("" : "+v"(v)); return v; }
__device__ __forceinline__ unsigned opqs(unsigned v) { asm volatile("" : "+s"(v)); return v; }
__device__ __forceinline__ void gload16_sc1(u32x4& dst, unsigned voff, u32x4 srd, unsigned soff) {
  asm volatile("buffer_load_dwordx4 %0, %1, %2, %3 offen sc1" : "=v"(dst) : "v"(voff), "s"(srd), "s"(soff) : "memory");
}
__device__ __forceinline__ void gstore16_sc1(u32x4 v, unsigned voff, u32x4 srd, unsigned soff) {
  asm volatile("buffer_store_dwordx4 %0, %1, %2, %3 offen sc1\n\ts_nop 1" ::"v"(v), "v"(voff), "s"(srd), "s"(soff) : "memory");
}
__device__ __forceinline__ void gload8_sc1(u32x2& dst, unsigned voff, u32x4 srd, unsigned soff) {
  asm volatile("buffer_load_dwordx2 %0, %1, %2, %3 offen sc1" : "=v"(dst) : "v"(voff), "s"(srd), "s"(soff) : "memory");
}
__device__ __forceinline__ void gstore8_sc1(u32x2 v, unsigned voff, u32x4 srd, unsigned soff) {
  asm volatile("buffer_store_dwordx2 %0, %1, %2, %3 offen sc1" ::"v"(v), "v"(voff), "s"(srd), "s"(soff) : "memory");
}

__device__ __forceinline__ s16x4 tr_read(unsigned addr) {
  return __builtin_amdgcn_ds_read_tr16_b64_v4i16((LDS_AS s16x4*)(uintptr_t)addr);
}
struct Frag { s16x4 lo, hi; };
__device__ __forceinline__ bf16x8 join(const Frag& f) {
  s16x8 v = __builtin_shufflevector(f.lo, f.hi, 0, 1, 2, 3, 4, 5, 6, 7);
  return __builtin_bit_cast(bf16x8, v);
}
__device__ __forceinline__ bf16x8 lds_read16(unsigned addr) { return *(LDS_AS bf16x8*)(uintptr_t)addr; }
__device__ __forceinline__ unsigned pack_bf16(float a, float b) {
  f32x2 v = {a, b};
  bf16x2 r = __builtin_convertvector(v, bf16x2);
  return __builtin_bit_cast(unsigned, r);
}
template <class TB>
__device__ __forceinline__ void mfma_acc(f32x4& c, bf16x8 a, TB b) {
  asm volatile("v_mfma_f32_16x16x32_bf16 %0, %1, %2, %0" : "+v"(c) : "v"(a), "v"(b));
}
template <class TB>
__device__ __forceinline__ void mfma_new(f32x4& c, bf16x8 a, TB b) {
  asm volatile("v_mfma_f32_16x16x32_bf16 %0, %1, %2, 0" : "=&v"(c) : "v"(a), "v"(b));
}

template <int I> struct IC { static constexpr int v = I; };
template <int I, int N, class F> __device__ __forceinline__ void sfor(F&& f) {
  if constexpr (I < N) { f(IC<I>{}); sfor<I + 1, N>(f); }
}
template <int N> __device__ __forceinline__ void wait_vm() { asm volatile("s_waitcnt vmcnt(%0)" ::"i"(N) : "memory"); }
__device__ __forceinline__ void wait_lgkm0() { asm volatile("s_waitcnt lgkmcnt(0)" ::: "memory"); }
#define BAR() asm volatile("s_barrier" ::: "memory")
#define SB() __builtin_amdgcn_sched_barrier(0)
#define INL __attribute__((always_inline))

constexpr int UNIT = 16384, LDS_TOTAL = 10 * UNIT;
constexpr int uaddr(int n) { return (n % 10) * UNIT; }

enum { K_DMA = 1, K_STORE, K_FLAG, K_POLL, K_H };
struct Op { int pos, kind, n, i; };
struct Sched { int cnt; Op op[200]; };
constexpr void put(Sched& s, int pos, int kind, int n, int i) { s.op[s.cnt++] = Op{pos, kind, n, i}; }
constexpr void spread(Sched& s, int p0, int p1, int u0, int nu) {
  for (int k = 0; k < 2 * nu; ++k) put(s, p0 + (k * (p1 - p0)) / (2 * nu), K_DMA, u0 + (k >> 1), k & 1);
}
constexpr int Q(int step, int slot) { return 200 + 8 * step + slot; }
constexpr void pair(Sched& s, int step, int slot, int kind, int n) { put(s, Q(step, slot), kind, n, 0); put(s, Q(step, slot == 0 ? 0 : slot + 1), kind, n, 1); }
constexpr int HB = 3;
constexpr int Y1 = 2, Y2 = 5, FCHK = 7, Y3 = 10;
constexpr Sched make_sched() {
  Sched s{};
  for (int n = 0; n < 4; ++n) { put(s, -1, K_DMA, n, 0); put(s, -1, K_DMA, n, 1); }
  put(s, -1, K_POLL, 0, 0);
  spread(s, 0, 2, 4, 2);
  for (int t = 0; t < 7; ++t) spread(s, 8 * t + 2, 8 * t + 9, 2 * t + 6, 2);
  spread(s, 58, 64, 20, 2);
  put(s, 65, K_STORE, 0, 0);
  put(s, 66, K_STORE, 0, 1);
  spread(s, 72, 80, 22, 1);
  spread(s, 82, 89, 23, 2);
  put(s, 90, K_FLAG, 0, 0);
  spread(s, 91, 103, 25, 2);
  spread(s, 103, 111, 27, 2);
  put(s, 111, K_POLL, 1, 0);
  spread(s, 112, 121, 29, 2);
  for (int k = 0; k < 4; ++k) put(s, 122 + k, K_H, k >> 1, k & 1);
  put(s, 128, K_STORE, 1, 0);
  put(s, 128, K_STORE, 1, 1);
  put(s, 128, K_H, 2, 0);
  put(s, 128, K_H, 2, 1);
  put(s, Q(Y1, 0), K_FLAG, 1, 0);
  pair(s, 2, 0, K_H, 3); pair(s, 2, 0, K_H, 4);
  pair(s, 2, 3, K_DMA, 31);
  pair(s, 3, 1, K_H, 5); pair(s, 3, 3, K_H, 6);
  pair(s, 4, 1, K_DMA, 32); pair(s, 4, 3, K_DMA, 33);
  pair(s, 5, 1, K_DMA, 34); pair(s, 5, 3, K_DMA, 35);
  put(s, Q(6, 0), K_POLL, 2, 0);
  pair(s, 6, 1, K_DMA, 36); pair(s, 6, 3, K_DMA, 37);
  for (int k = 0; k < HB; ++k) pair(s, 7, 0, K_H, 7 + k);
  for (int k = 7 + HB; k < 14; ++k) pair(s, k - HB, 1, K_H, k);
  return s;
}
constexpr Sched SCH = make_sched();
constexpr int younger_than(int kind, int n, int i, int P) {
  int c = -1;
  for (int k = 0; k < SCH.cnt; ++k) {
    if (SCH.op[k].pos >= P) break;
    if (SCH.op[k].kind == kind && SCH.op[k].n == n && SCH.op[k].i == i) c = 0;
    else if (c >= 0 && SCH.op[k].kind != K_FLAG && !(SCH.op[k].kind == K_POLL && SCH.op[k].n > 0)) ++c;
  }
  return c;
}
constexpr int first_op(int P) { int k = 0; while (k < SCH.cnt && SCH.op[k].pos < P) ++k; return k; }
constexpr bool is_bar(int P) { return P < 64 ? (P & 7) == 2 : (P & 15) == 10; }
constexpr int bar_wait(int P) {
  if (P < 64) return younger_than(K_DMA, 2 * (P >> 3) + 3, 1, P);
  if (P == 74) return younger_than(K_DMA, 19, 1, P);
  if (P == 90) return younger_than(K_STORE, 0, 1, P);
  if (P == 106) return younger_than(K_DMA, 23, 1, P);
  if (P == 122) return younger_than(K_POLL, 1, 0, P);
  if (P == Q(Y1, 0)) return 0;
  if (P == Q(Y2, 0)) return younger_than(K_DMA, 31, 1, P);
  if (P == Q(FCHK, 0)) return younger_than(K_POLL, 2, 0, P);
  return younger_than(K_DMA, 37, 1, P);
}
constexpr bool sched_sorted() { for (int k = 1; k < SCH.cnt; ++k) if (SCH.op[k].pos < SCH.op[k - 1].pos) return false; return true; }
static_assert(sched_sorted(), "schedule order");

__global__ __launch_bounds__(512, 2) void k_fused(const void* __restrict__ x, const void* __restrict__ Wg,
                                                  const void* __restrict__ Wu, const void* __restrict__ Wd,
                                                  void* __restrict__ out, void* __restrict__ ws) {
  extern __shared__ __attribute__((aligned(16))) char smem[];
  const unsigned lds0 = (unsigned)(uintptr_t)(LDS_AS char*)smem;
  ST_DECL ST(0)
  const unsigned bid = blockIdx.x;
  const unsigned w = rfl(threadIdx.x >> 6);
  const bool busy = bid < 224;
  uint64_t pc;
  asm volatile("s_getpc_b64 %0" : "=s"(pc));
  u32x2 px, pw;
  asm volatile("s_load_dwordx2 %0, %2, 0x0\n\ts_load_dwordx2 %1, %2, %3"
               : "=&s"(px), "=&s"(pw) : "s"(__builtin_amdgcn_kernarg_segment_ptr()), "s"(8 + 8 * (w >> 2)));
  SB();
  const unsigned mi = (bid & 6) * 2 + ((bid >> 3) & 3), half = bid & 1, idx = bid >> 5;
  const unsigned rot = ((bid & 6) + 4 * half + ((bid >> 3) & 1) + (idx & 3)) & 7;
  unsigned vw0, vw1, vx0, vx1;
  {
    const unsigned lane = threadIdx.x & 63, g = lane >> 4;
    vw0 = (lane >> 3) * 3584 + (((lane & 7) ^ ((g & 1) << 1)) << 4);
    vw1 = (vw0 ^ 64) + 8 * 3584 - 1024;
    vx0 = (lane >> 3) * 1024 + (((lane & 7) ^ g) << 4);
    vx1 = (vx0 ^ 64) + 8192 - 1024;
  }
  unsigned sx0 = (mi * 128 + 16 * w) * 1024;
  unsigned sw0 = (16 * (w & 3)) * 3584 + (half * 14 + idx) * 128;
  unsigned mu0 = lds0 + w * 2048;
  unsigned sw00 = sw0 + rot * (64 * 3584), sx00 = sx0 + rot * 128;
  asm volatile("" : "+v"(vw0), "+v"(vw1), "+v"(vx0), "+v"(vx1));
  asm volatile("" : "+s"(sx0), "+s"(sw0), "+s"(mu0), "+s"(sw00), "+s"(sx00));
  SB();
  asm volatile("s_waitcnt lgkmcnt(0)" : "+s"(px), "+s"(pw));
  const u32x4 srdX = {px[0], px[1] & 0xffffu, M_ * D_ * 2, 0x00020000u};
  const u32x4 srdW = {pw[0], pw[1] & 0xffffu, D_ * F_ * 2, 0x00020000u};
  auto issue_xw = [&](int n, int i, bool pre = false) {
    const int s = n < 16 ? n >> 1 : n - 16;
    const unsigned sg = (s + rot) & 7;
    if (n < 16 && (n & 1) == 0) {
      const unsigned so = n == 0 ? sx00 : sx0 + sg * 128;
      if (i == 0) { if (pre) dma16n<0>(vx0, srdX, so); else dma16<0>(mu0 + uaddr(n), vx0, srdX, so); } else dma16n<1024>(vx1, srdX, so);
    } else {
      const unsigned so = n == 1 ? sw00 : sw0 + sg * (64 * 3584) + (n >= 16 ? 896 : 0);
      if (i == 0) { if (pre) dma16n<0>(vw0, srdW, so); else dma16<0>(mu0 + uaddr(n), vw0, srdW, so); } else dma16n<1024>(vw1, srdW, so);
    }
  };
  if (busy) {
    issue_xw(0, 0); issue_xw(0, 1); issue_xw(1, 0); issue_xw(1, 1);
    BAR();
    issue_xw(2, 0); issue_xw(2, 1); issue_xw(3, 0); issue_xw(3, 1);
  } else {
    const unsigned o = min(threadIdx.x * 64u, CODE_BYTES);
    asm volatile("global_load_dword v249, %0, %1" ::"v"(o), "s"(pc) : "v249", "memory");
  }
  SB();
  const unsigned tid = opq(threadIdx.x), lane = tid & 63;
  const unsigned bid2 = opqs(bid), xcd = bid2 & 7, ml = (bid2 >> 3) & 3;
  unsigned rank = half * 8 + idx;
  const unsigned m64 = 2 * mi + half, n64 = idx;
  const unsigned g = lane >> 4, q = (lane & 15) >> 2, p = lane & 3;
  const u32x4 srdH = make_srd(ws, SYNC_OFF + 16 * 8192);
  const u32x4 srdD = make_srd(Wd, F_ * D_ * 2);
  unsigned syncb = SYNC_OFF + mi * 8192;
  const unsigned lane16 = lane * 16;

  const unsigned kq = w >> 1, mb = w & 1;
  unsigned rot2 = 2 * ml + idx; rot2 = rot2 >= 7 ? rot2 - 7 : rot2;
  unsigned kb[7];
  auto set_kb = [&]() {
    unsigned e = opqs(rot2);
#pragma unroll
    for (int j = 0; j < 7; ++j) {
      kb[j] = opqs((e + 7 * kq + (kq >> 1) * 14) * 1024);
      e = e == 6 ? 0 : e + 1;
    }
  };
  auto kap = [&](int s) { return kb[s % 7] + (s >= 7 ? 14 * 1024 : 0); };
  const unsigned vd0 = (lane >> 3) * 1024 + (((lane & 7) ^ (g << 1)) << 4);
  const unsigned vd1 = vd0 + 8192 - 1024;
  unsigned sd0 = (16 * mb) * 1024 + n64 * 128;
  auto issue_wd = [&](int s, int i, bool pre = false) {
    if (i == 0) { if (pre) dma16n<0>(vd0, srdD, sd0 + kap(s) * 32); else dma16<0>(mu0 + uaddr(24 + s), vd0, srdD, sd0 + kap(s) * 32); }
    else dma16n<1024>(vd1, srdD, sd0 + kap(s) * 32);
  };
  const unsigned dl = lds0 + kq * 4096 + (4 * g + q) * 128 + (((2 * g + (q >> 1)) & 3) << 5) + p * 8;
  unsigned db[3][4];
  auto set_db = [&](int k) {
#pragma unroll
    for (int c = 0; c < 4; ++c) db[k][c] = opq((dl ^ (c << 5)) + k * 4 * UNIT);
  };
  auto ldd = [&](int s, int a, int u) {
    const int ua = uaddr(24 + s);
    return tr_read(db[ua >> 16][a] + (ua & 65535) + u * 2048);
  };
  u32x2 ep, tag, fl;
  auto mktag = [&]() {
    const uint64_t t = (((uint64_t)ep[1] << 32) | ep[0]) * 0x9E3779B97F4A7C15ull + 1ull;
    tag = u32x2{(unsigned)t, (unsigned)(t >> 32)};
  };
  const unsigned one = tid == 0 ? 0u : 0x40000000u;
  const unsigned fo = tid < 16 ? 128 * (1 + tid) : 0x40000000u;
  unsigned fmask = 0x7F7Fu;
  auto flags_ok = [&]() { return (rfl((unsigned)__builtin_amdgcn_ballot_w64(fl[0] == tag[0] && fl[1] == tag[1])) & fmask) == fmask; };
  auto poll = [&](unsigned off) {
    unsigned cnt = SPIN_CAP, t;
#define PLD(r) "buffer_load_dwordx2 " r ", %2, %3, %4 offen sc1\n\t"
#define PCK(r) "s_waitcnt vmcnt(2)\n\tv_cmp_eq_u64 vcc, " r ", %5\n\ts_nop 1\n\ts_and_b32 %1, vcc_lo, %6\n\ts_cmp_eq_u32 %1, %6\n\ts_cbranch_scc1 2f\n\t"
    asm volatile(PLD("v[250:251]") "s_sleep 3\n\t" PLD("v[252:253]") "s_sleep 3\n\t" PLD("v[254:255]")
                 "1:\n\t"
                 PCK("v[250:251]") PLD("v[250:251]") PCK("v[252:253]") PLD("v[252:253]") PCK("v[254:255]") PLD("v[254:255]")
                 "s_sub_u32 %0, %0, 1\n\ts_cbranch_scc0 1b\n\t"
                 "2:"
                 : "+s"(cnt), "=&s"(t) : "v"(fo), "s"(srdH), "s"(syncb + off), "v"(tag), "s"(fmask)
                 : "v250", "v251", "v252", "v253", "v254", "v255", "vcc", "scc", "memory");
#undef PLD
#undef PCK
  };
  auto poll_slow = [&](unsigned off) {
#pragma nounroll
    for (unsigned spin = 0; spin < SPIN_CAP; ++spin) {
      gload8_sc1(fl, fo, srdH, syncb + off);
      asm volatile("s_waitcnt vmcnt(0)" : "+v"(fl)::"memory");
      if (flags_ok()) break;
      __builtin_amdgcn_s_sleep(SLP);
    }
  };
  Frag df[2][4];
  u32x4 hf[14][2];
  unsigned sh2 = ((m64 * 4 + mb * 2) * 56) * 1024;
  const unsigned lane16b = lane16 + 56 * 1024;
  auto issue_h = [&](int s, int b) { gload16_sc1(hf[s][b], b ? lane16b : lane16, srdH, sh2 + kap(s)); };

  set_kb();
  if (__builtin_expect(busy, 1)) {
    gload8_sc1(ep, 0u, srdH, syncb);
    const unsigned wm = w & 3, wn = w >> 2;
    const unsigned xrow = 32 * wm + (lane & 15);
    const unsigned xa0 = lds0 + xrow * 128 + ((g ^ ((xrow >> 1) & 7)) << 4);
    const unsigned wa0 = lds0 + (8 * g + q) * 128 + (((2 * wn) ^ ((q >> 1) | ((g & 1) << 1))) << 5) + p * 8;
    unsigned xa[3][2], wa[3][2];
#pragma unroll
    for (int k = 0; k < 3; ++k)
#pragma unroll
      for (int c = 0; c < 2; ++c) { xa[k][c] = opq((xa0 ^ (c << 6)) + k * 4 * UNIT); wa[k][c] = opq((wa0 ^ (c << 5)) + k * 4 * UNIT); }
    ST(1)

    f32x4 acc[2][2][4];
    bf16x8 xf[8][2][2];
    Frag wf[4][2];
    auto ldx = [&](int s, int b, int kk) { const int ua = uaddr(2 * s); xf[s][b][kk] = lds_read16(xa[ua >> 16][kk] + (ua & 65535) + b * 2048); };
    auto ldw = [&](int G, int kk, int u = 2) {
      const int st = G >> 2, a = ((G & 1) << 1) | ((G >> 1) & 1), ua = uaddr(st < 8 ? 2 * st + 1 : 8 + st);
      const unsigned ad = wa[ua >> 16][a & 1] + (ua & 65535) + (a >> 1) * 8192 + kk * 4096;
      if (u != 1) wf[G & 3][kk].lo = tr_read(ad);
      if (u != 0) wf[G & 3][kk].hi = tr_read(ad + 512);
    };
    const unsigned sh = ((mi * 8 + wm * 2) * 56 + half * 28 + idx * 2 + wn) * 1024;
    u32x4 ho[2];
    auto epi = [&](int ps, int b, int c) {
      const int t = c >> 1, i = c & 1;
      float g0 = __builtin_elementwise_maximum(acc[ps][b][t][2 * i], 0.f), g1 = __builtin_elementwise_maximum(acc[ps][b][t][2 * i + 1], 0.f);
      float p0 = g0 * acc[ps][b][2 + t][2 * i];
      asm volatile("" : "+v"(p0));
      ho[b][c] = pack_bf16(p0, g1 * acc[ps][b][2 + t][2 * i + 1]);
    };
    auto sth = [&](int ps, int b) { gstore16_sc1(ho[b], lane16, srdH, sh + b * (56 * 1024) + ps * (14 * 1024)); };

    auto vmem = [&](auto PP) INL {
      sfor<first_op(PP.v), first_op(PP.v + 1)>([&](auto K) INL {
        constexpr Op o = SCH.op[K.v];
        if constexpr (o.pos == PP.v) {
          constexpr bool pre = PP.v < 128 && K.v == first_op(PP.v);
          if constexpr (o.kind == K_DMA) { if constexpr (o.n < 24) issue_xw(o.n, o.i, pre); else issue_wd(o.n - 24, o.i, pre); }
          if constexpr (o.kind == K_STORE) sth(o.n, o.i);
          if constexpr (o.kind == K_FLAG) gstore8_sc1(tag, one, srdH, syncb + 128 * (1 + rank));
          if constexpr (o.kind == K_POLL) gload8_sc1(fl, fo, srdH, syncb);
          if constexpr (o.kind == K_H) issue_h(o.n, o.i);
        }
      });
    };
    wait_vm<5>();
    ST(2)
    BAR();
    ldw(0, 0); ldx(0, 0, 0); ldw(0, 1); ldx(0, 0, 1); ldx(0, 1, 0); ldx(0, 1, 1); ldw(1, 0); ldw(1, 1); ldw(2, 0); ldw(2, 1);
    SB();
    sfor<0, 128>([&](auto Z) INL {
      constexpr int P = Z.v, ps = P >> 6, s = (P >> 3) & 7, G = P >> 1, a = ((G & 1) << 1) | ((G >> 1) & 1), b = P & 1;
      if constexpr (is_bar(P)) {
        constexpr int n = bar_wait(P);
        static_assert(n >= 0 && n < 24, "wait count");
        if constexpr (P == 10) asm volatile("s_waitcnt vmcnt(%1)" : "+v"(ep) : "i"(n) : "memory");
        else if constexpr (P == 122) {
          asm volatile("s_waitcnt vmcnt(%1)" : "+v"(fl) : "i"(n) : "memory");
          if (w == 0) { if (__builtin_expect(!flags_ok(), 0)) poll(0); }
        } else wait_vm<n>();
        BAR();
        if (P == 2) { ST(3) } if (P == 26) { ST(4) } if (P == 58) { ST(5) } if (P == 74) { ST(6) } if (P == 90) { ST(7) } if (P == 106) { ST(8) } if (P == 122) { ST(9) }
      }
      if constexpr (P < 58 ? (P & 1) == 0 : (P & 3) == 2) __builtin_amdgcn_s_setprio(P < 58 ? 3 - (((P + 6) & 7) >> 1) : 3 - (((P + 6) & 15) >> 2));
      if constexpr (s == 0) mfma_new(acc[ps][b][a], join(wf[G & 3][0]), xf[s][b][0]); else mfma_acc(acc[ps][b][a], join(wf[G & 3][0]), xf[s][b][0]);
      SB();
      if constexpr (G + 3 < 64) ldw(G + 3, b, 0);
      if constexpr (P >= 124) df[0][P - 124].lo = ldd(0, P - 124, 0);
      {
        constexpr int k0 = first_op(P);
        if constexpr (k0 < first_op(P + 1) && SCH.op[k0].kind == K_DMA && SCH.op[k0].i == 0) set_m0(mu0 + uaddr(SCH.op[k0].n));
        else if constexpr (P < 56 && (P & 7) >= 2 && (P & 7) < 6) ldx(s + 1, ((P - 2) >> 1) & 1, P & 1);
      }
      SB();
      mfma_acc(acc[ps][b][a], join(wf[G & 3][1]), xf[s][b][1]);
      SB();
      if constexpr (G + 3 < 64) ldw(G + 3, b, 1);
      if constexpr (P >= 124) df[0][P - 124].hi = ldd(0, P - 124, 1);
      {
        constexpr int k0 = first_op(P);
        if constexpr (k0 < first_op(P + 1) && SCH.op[k0].kind == K_DMA && SCH.op[k0].i == 0 && P < 56 && (P & 7) >= 2 && (P & 7) < 6) ldx(s + 1, ((P - 2) >> 1) & 1, P & 1);
      }
      if constexpr (P == 120) set_db(1);
      if constexpr ((P & 63) == 62 || (P & 63) == 63) { epi(ps, P & 1, 0); epi(ps, P & 1, 1); }
      if constexpr (P == 65 || P == 66) { epi(0, P - 65, 2); epi(0, P - 65, 3); }
      if constexpr (P == 116) set_db(0);
      if constexpr (P == 118) set_db(2);
      if constexpr (P == 84) mktag();
      vmem(Z);
      SB();
    });
    ST(11)
    epi(1, 0, 2); epi(1, 0, 3);
    sth(1, 0);
    SB();
    epi(1, 1, 2); epi(1, 1, 3);
    sth(1, 1);
    issue_h(2, 0); issue_h(2, 1);
    __builtin_amdgcn_s_setprio(0);
    ST(12)
  } else {
    gload8_sc1(ep, 0u, srdH, syncb);
    set_db(0); set_db(1); set_db(2);
    {
      unsigned t = 0;
      const unsigned rotx = ((xcd & 6) + 4 * half) & 7;
      {
        const unsigned li = (ml * 4 + (w & 3)) * 56 + lane;
        const unsigned row = li / 7, c = li - row * 7;
        const unsigned vo = lane < 56 ? (rotx * 64 + row) * 3584 + (half * 14 + 7 + c) * 128 : 0x40000000u;
        asm volatile("buffer_load_dword %0, %1, %2, 0 offen" : "+v"(t) : "v"(vo), "s"(srdW) : "memory");
      }
#pragma unroll
      for (int i = 0; i < 3; ++i) {
        unsigned c = i + xcd; c = c >= 7 ? c - 7 : c; c = c >= 7 ? c - 7 : c;
        asm volatile("buffer_load_dword %0, %1, %2, %3 offen" : "+v"(t) : "v"(lane * 128), "s"(srdD), "s"(((c * 4 + ml) * 8 + w) * 8192) : "memory");
      }
      asm volatile("s_waitcnt vmcnt(0)" : "+v"(t), "+v"(ep)::"memory");
      mktag();
    }
#pragma unroll
    for (int s = 0; s < 7; ++s) { issue_wd(s, 0); issue_wd(s, 1); }
    if (w == 0) poll_slow(0); else wait_vm<0>();
    BAR();
#pragma unroll
    for (int s = 0; s < 3; ++s) { issue_h(s, 0); issue_h(s, 1); }
#pragma unroll
    for (int a = 0; a < 4; ++a) { df[0][a].lo = ldd(0, a, 0); df[0][a].hi = ldd(0, a, 1); }
    wait_vm<0>();
  }
  asm volatile("" : "+s"(mu0), "+s"(sd0), "+s"(sh2), "+s"(syncb), "+s"(rank));
  asm volatile("" : "+s"(kb[0]), "+s"(kb[1]), "+s"(kb[2]), "+s"(kb[3]), "+s"(kb[4]), "+s"(kb[5]), "+s"(kb[6]));
  fmask = rank == 7 ? 0xFFFFu : 0x7F7Fu;
  f32x4 acc2[2][4];
  const unsigned pa = opq(lds0 + ((kq + 8) % 10) * UNIT + (mb * 32 + (lane & 15)) * 256 + ((g ^ (lane & 7)) << 4));
  const unsigned pa1 = opq(pa ^ 64);
  const unsigned ra = opq(lds0 + (w * 8 + (lane >> 3)) * 256 + (((2 * (lane & 7)) ^ (lane >> 3)) << 4)), rb = opq(ra ^ 16);
  const unsigned ra8 = opq(ra + 8 * UNIT), rb8 = opq(rb + 8 * UNIT);
  const unsigned vo = opq((w * 8 + (lane >> 3)) * 1024 + (lane & 7) * 16), so = opqs(m64 * 65536 + n64 * 128);
  const u32x4 srdO = make_srd(out, M_ * D_ * 2);
  auto wpart = [&](int a) {
    const unsigned ad = ((a & 1) ? pa1 : pa) + (a >> 1) * 128;
    *(LDS_AS f32x4*)(uintptr_t)(ad) = acc2[0][a];
    *(LDS_AS f32x4*)(uintptr_t)(ad + 16 * 256) = acc2[1][a];
  };
  auto vmem2 = [&](auto PP) INL {
    sfor<first_op(PP.v), first_op(PP.v + 1)>([&](auto K) INL {
      constexpr Op o = SCH.op[K.v];
      if constexpr (o.pos == PP.v) {
        if constexpr (o.kind == K_DMA) issue_wd(o.n - 24, o.i);
        if constexpr (o.kind == K_FLAG) gstore8_sc1(tag, one, srdH, syncb + 128 * (17 + rank));
        if constexpr (o.kind == K_POLL) gload8_sc1(fl, fo, srdH, syncb + 2048);
        if constexpr (o.kind == K_H) issue_h(o.n, o.i);
      }
    });
  };
  sfor<0, 14>([&](auto S) INL {
    constexpr int s = S.v;
    if constexpr (s == Y1 || s == FCHK || s == Y2 || s == Y3) {
      constexpr int n = bar_wait(Q(s, 0));
      static_assert(n >= 0 && n < 24, "wait count");
      if constexpr (s == FCHK) {
        asm volatile("s_waitcnt vmcnt(%1)" : "+v"(fl) : "i"(n) : "memory");
        if (w == 0) { if (__builtin_expect(!flags_ok(), 0)) poll(2048); }
      } else wait_vm<n>();
      BAR();
      if (s == Y1) { ST(13) } if (s == FCHK) { ST(14) } if (s == Y2) { ST(15) } if (s == Y3) { ST(16) }
      if constexpr (s == Y3) { if (rank == 7 && w == 0) gstore8_sc1(tag, one, srdH, syncb); }
    }
    vmem2(IC<Q(s, 0)>{});
    if constexpr (s == Y1) asm volatile("" : "+v"(hf[s][0]), "+v"(hf[s][1])::"memory");
    else asm volatile("s_waitcnt vmcnt(%2)" : "+v"(hf[s][0]), "+v"(hf[s][1]) : "i"(younger_than(K_H, s, 1, Q(s, 1))) : "memory");
    if (s == 0) { ST(17) }
    SB();
    sfor<0, 4>([&](auto A) INL {
      constexpr int a = A.v;
      if constexpr (s == 0) mfma_new(acc2[0][a], join(df[0][a]), hf[0][0]); else mfma_acc(acc2[0][a], join(df[s & 1][a]), hf[s][0]);
      SB();
      if constexpr (s + 1 < 14) df[(s + 1) & 1][a].lo = ldd(s + 1, a, 0);
      SB();
      if constexpr (s == 0) mfma_new(acc2[1][a], join(df[0][a]), hf[0][1]); else mfma_acc(acc2[1][a], join(df[s & 1][a]), hf[s][1]);
      SB();
      if constexpr (s + 1 < 14) df[(s + 1) & 1][a].hi = ldd(s + 1, a, 1);
      vmem2(IC<Q(s, a + 1)>{});
      if constexpr (s == 13 && a == 3) { asm volatile("s_nop 1" ::: "memory"); SB(); wpart(0); SB(); wpart(1); }
      SB();
    });
  });
  ST(18)
  wpart(2);
  SB();
  asm volatile("s_nop 1" ::: "memory");
  SB();
  wpart(3);
  wait_lgkm0();
  BAR();
  ST(19)
  {
    f32x4 p0[4], p1[4];
#pragma unroll
    for (int v = 0; v < 4; ++v) {
      p0[v] = *(LDS_AS f32x4*)(uintptr_t)((v < 2 ? ra8 : ra) + (v & 1) * UNIT);
      p1[v] = *(LDS_AS f32x4*)(uintptr_t)((v < 2 ? rb8 : rb) + (v & 1) * UNIT);
    }
    SB();
    const f32x4 s0 = (p0[0] + p0[1]) + (p0[2] + p0[3]), s1 = (p1[0] + p1[1]) + (p1[2] + p1[3]);
    u32x4 o = {pack_bf16(s0[0], s0[1]), pack_bf16(s0[2], s0[3]), pack_bf16(s1[0], s1[1]), pack_bf16(s1[2], s1[3])};
    asm volatile("buffer_store_dwordx4 %0, %1, %2, %3 offen" ::"v"(o), "v"(vo), "s"(srdO), "s"(so) : "memory");
  }
  ST(20)
}

extern "C" void kernel_launch(void* const* d_in, const int* in_sizes, int n_in, void* d_out, int out_size,
                              void* d_ws, size_t ws_size, hipStream_t stream) {
  (void)in_sizes; (void)n_in; (void)out_size; (void)ws_size;
  (void)hipFuncSetAttribute((const void*)k_fused, hipFuncAttributeMaxDynamicSharedMemorySize, LDS_TOTAL);
  k_fused<<<256, 512, LDS_TOTAL, stream>>>(d_in[0], d_in[1], d_in[2], d_in[3], d_out, d_ws);
}
```

```cpp
#include <hip/hip_runtime.h>
#include <hip/hip_bf16.h>
#include <stdint.h>

typedef __attribute__((ext_vector_type(8))) __bf16 bf16x8;
typedef __attribute__((ext_vector_type(4))) short s16x4;
typedef __attribute__((ext_vector_type(8))) short s16x8;
typedef __attribute__((ext_vector_type(2))) __bf16 bf16x2;
typedef __attribute__((ext_vector_type(2))) float f32x2;
typedef __attribute__((ext_vector_type(4))) float f32x4;
typedef __attribute__((ext_vector_type(4))) unsigned u32x4;
typedef __attribute__((ext_vector_type(2))) unsigned u32x2;
#define LDS_AS __attribute__((address_space(3)))

constexpr int M_ = 2048, D_ = 512, F_ = 1792;
constexpr unsigned SYNC_OFF = 8u << 20;
constexpr unsigned SPIN_CAP = 1u << 17;
constexpr unsigned CODE_BYTES = 12544;

__device__ __forceinline__ unsigned rfl(unsigned v) { return __builtin_amdgcn_readfirstlane(v); }

__device__ __forceinline__ u32x4 make_srd(const void* p, unsigned bytes) {
  uint64_t a = (uint64_t)p;
  u32x4 r;
  r[0] = rfl((unsigned)a);
  r[1] = rfl((unsigned)(a >> 32)) & 0xffffu;
  r[2] = bytes;
  r[3] = 0x00020000u;
  return r;
}

template <int IMM>
__device__ __forceinline__ void dma16(unsigned m0v, unsigned voff, u32x4 srd, unsigned soff) {
  asm volatile("s_mov_b32 m0, %0\n\ts_nop 0\n\tbuffer_load_dwordx4 %1, %2, %3 offen offset:%4 lds"
               ::"s"(m0v), "v"(voff), "s"(srd), "s"(soff), "i"(IMM) : "memory");
}
template <int IMM>
__device__ __forceinline__ void dma16n(unsigned voff, u32x4 srd, unsigned soff) {
  asm volatile("buffer_load_dwordx4 %0, %1, %2 offen offset:%3 lds" ::"v"(voff), "s"(srd), "s"(soff), "i"(IMM) : "memory");
}
__device__ __forceinline__ void set_m0(unsigned v) { asm volatile("s_mov_b32 m0, %0" ::"s"(v) : "memory"); }
__device__ __forceinline__ unsigned opq(unsigned v) { asm volatile("" : "+v"(v)); return v; }
__device__ __forceinline__ unsigned opqs(unsigned v) { asm volatile("" : "+s"(v)); return v; }
__device__ __forceinline__ void gload16_sc1(u32x4& dst, unsigned voff, u32x4 srd, unsigned soff) {
  asm volatile("buffer_load_dwordx4 %0, %1, %2, %3 offen sc1" : "=v"(dst) : "v"(voff), "s"(srd), "s"(soff) : "memory");
}
__device__ __forceinline__ void gstore16_sc1(u32x4 v, unsigned voff, u32x4 srd, unsigned soff) {
  asm volatile("buffer_store_dwordx4 %0, %1, %2, %3 offen sc1\n\ts_nop 1" ::"v"(v), "v"(voff), "s"(srd), "s"(soff) : "memory");
}
__device__ __forceinline__ void gload8_sc1(u32x2& dst, unsigned voff, u32x4 srd, unsigned soff) {
  asm volatile("buffer_load_dwordx2 %0, %1, %2, %3 offen sc1" : "=v"(dst) : "v"(voff), "s"(srd), "s"(soff) : "memory");
}
__device__ __forceinline__ void gstore8_sc1(u32x2 v, unsigned voff, u32x4 srd, unsigned soff) {
  asm volatile("buffer_store_dwordx2 %0, %1, %2, %3 offen sc1" ::"v"(v), "v"(voff), "s"(srd), "s"(soff) : "memory");
}

__device__ __forceinline__ s16x4 tr_read(unsigned addr) {
  return __builtin_amdgcn_ds_read_tr16_b64_v4i16((LDS_AS s16x4*)(uintptr_t)addr);
}
struct Frag { s16x4 lo, hi; };
__device__ __forceinline__ bf16x8 join(const Frag& f) {
  s16x8 v = __builtin_shufflevector(f.lo, f.hi, 0, 1, 2, 3, 4, 5, 6, 7);
  return __builtin_bit_cast(bf16x8, v);
}
__device__ __forceinline__ bf16x8 lds_read16(unsigned addr) { return *(LDS_AS bf16x8*)(uintptr_t)addr; }
__device__ __forceinline__ unsigned pack_bf16(float a, float b) {
  f32x2 v = {a, b};
  bf16x2 r = __builtin_convertvector(v, bf16x2);
  return __builtin_bit_cast(unsigned, r);
}
template <class TB>
__device__ __forceinline__ void mfma_acc(f32x4& c, bf16x8 a, TB b) {
  asm volatile("v_mfma_f32_16x16x32_bf16 %0, %1, %2, %0" : "+v"(c) : "v"(a), "v"(b));
}
template <class TB>
__device__ __forceinline__ void mfma_new(f32x4& c, bf16x8 a, TB b) {
  asm volatile("v_mfma_f32_16x16x32_bf16 %0, %1, %2, 0" : "=&v"(c) : "v"(a), "v"(b));
}

template <int I> struct IC { static constexpr int v = I; };
template <int I, int N, class F> __device__ __forceinline__ void sfor(F&& f) {
  if constexpr (I < N) { f(IC<I>{}); sfor<I + 1, N>(f); }
}
template <int N> __device__ __forceinline__ void wait_vm() { asm volatile("s_waitcnt vmcnt(%0)" ::"i"(N) : "memory"); }
__device__ __forceinline__ void wait_lgkm0() { asm volatile("s_waitcnt lgkmcnt(0)" ::: "memory"); }
#define BAR() asm volatile("s_barrier" ::: "memory")
#define SB() __builtin_amdgcn_sched_barrier(0)
#define INL __attribute__((always_inline))

constexpr int UNIT = 16384, LDS_TOTAL = 10 * UNIT;
constexpr int uaddr(int n) { return (n % 10) * UNIT; }


enum { K_DMA = 1, K_STORE, K_FLAG, K_POLL, K_H };
struct Op { int pos, kind, n, i; };
struct Sched { int cnt; Op op[200]; };
constexpr void put(Sched& s, int pos, int kind, int n, int i) { s.op[s.cnt++] = Op{pos, kind, n, i}; }
constexpr void spread(Sched& s, int p0, int p1, int u0, int nu) {
  for (int k = 0; k < 2 * nu; ++k) put(s, p0 + (k * (p1 - p0)) / (2 * nu), K_DMA, u0 + (k >> 1), k & 1);
}
constexpr int Q(int step, int slot) { return 200 + 8 * step + slot; }
constexpr void pair(Sched& s, int step, int slot, int kind, int n) { put(s, Q(step, slot), kind, n, 0); put(s, Q(step, slot == 0 ? 0 : slot + 1), kind, n, 1); }
constexpr int HB = 3;
constexpr int Y1 = 2, Y2 = 5, FCHK = 7, Y3 = 10;
constexpr Sched make_sched() {
  Sched s{};
  for (int n = 0; n < 4; ++n) { put(s, -1, K_DMA, n, 0); put(s, -1, K_DMA, n, 1); }
  put(s, -1, K_POLL, 0, 0);
  spread(s, 0, 2, 4, 2);
  for (int t = 0; t < 7; ++t) spread(s, 8 * t + 2, 8 * t + 9, 2 * t + 6, 2);
  spread(s, 58, 64, 20, 2);
  put(s, 65, K_STORE, 0, 0);
  put(s, 66, K_STORE, 0, 1);
  spread(s, 72, 80, 22, 1);
  spread(s, 82, 89, 23, 2);
  put(s, 90, K_FLAG, 0, 0);
  spread(s, 91, 103, 25, 2);
  spread(s, 103, 111, 27, 2);
  put(s, 111, K_POLL, 1, 0);
  spread(s, 112, 121, 29, 2);
  for (int k = 0; k < 4; ++k) put(s, 122 + k, K_H, k >> 1, k & 1);
  put(s, 128, K_STORE, 1, 0);
  put(s, 128, K_STORE, 1, 1);
  put(s, 128, K_H, 2, 0);
  put(s, 128, K_H, 2, 1);
  put(s, Q(Y1, 0), K_FLAG, 1, 0);
  pair(s, 2, 0, K_H, 3); pair(s, 2, 0, K_H, 4);
  pair(s, 2, 3, K_DMA, 31);
  pair(s, 3, 1, K_H, 5); pair(s, 3, 3, K_H, 6);
  pair(s, 4, 1, K_DMA, 32); pair(s, 4, 3, K_DMA, 33);
  pair(s, 5, 1, K_DMA, 34); pair(s, 5, 3, K_DMA, 35);
  put(s, Q(6, 0), K_POLL, 2, 0);
  pair(s, 6, 1, K_DMA, 36); pair(s, 6, 3, K_DMA, 37);
  for (int k = 0; k < HB; ++k) pair(s, 7, 0, K_H, 7 + k);
  for (int k = 7 + HB; k < 14; ++k) pair(s, k - HB, 1, K_H, k);
  return s;
}
constexpr Sched SCH = make_sched();
constexpr int younger_than(int kind, int n, int i, int P) {
  int c = -1;
  for (int k = 0; k < SCH.cnt; ++k) {
    if (SCH.op[k].pos >= P) break;
    if (SCH.op[k].kind == kind && SCH.op[k].n == n && SCH.op[k].i == i) c = 0;
    else if (c >= 0 && SCH.op[k].kind != K_FLAG && !(SCH.op[k].kind == K_POLL && SCH.op[k].n > 0)) ++c;
  }
  return c;
}
constexpr int first_op(int P) { int k = 0; while (k < SCH.cnt && SCH.op[k].pos < P) ++k; return k; }
constexpr bool is_bar(int P) { return P < 64 ? (P & 7) == 2 : (P & 15) == 10; }
constexpr int bar_wait(int P) {
  if (P < 64) return younger_than(K_DMA, 2 * (P >> 3) + 3, 1, P);
  if (P == 74) return younger_than(K_DMA, 19, 1, P);
  if (P == 90) return younger_than(K_STORE, 0, 1, P);
  if (P == 106) return younger_than(K_DMA, 23, 1, P);
  if (P == 122) return younger_than(K_POLL, 1, 0, P);
  if (P == Q(Y1, 0)) return 0;
  if (P == Q(Y2, 0)) return younger_than(K_DMA, 31, 1, P);
  if (P == Q(FCHK, 0)) return younger_than(K_POLL, 2, 0, P);
  return younger_than(K_DMA, 37, 1, P);
}
constexpr bool sched_sorted() { for (int k = 1; k < SCH.cnt; ++k) if (SCH.op[k].pos < SCH.op[k - 1].pos) return false; return true; }
static_assert(sched_sorted(), "schedule order");

__global__ __launch_bounds__(512, 2) void k_fused(const void* __restrict__ x, const void* __restrict__ Wg,
                                                  const void* __restrict__ Wu, const void* __restrict__ Wd,
                                                  void* __restrict__ out, void* __restrict__ ws) {
  extern __shared__ __attribute__((aligned(16))) char smem[];
  const unsigned lds0 = (unsigned)(uintptr_t)(LDS_AS char*)smem;
  const unsigned bid = blockIdx.x;
  const unsigned w = rfl(threadIdx.x >> 6);
  const bool busy = bid < 224;
  uint64_t pc;
  asm volatile("s_getpc_b64 %0" : "=s"(pc));
  u32x2 px, pw;
  asm volatile("s_load_dwordx2 %0, %2, 0x0\n\ts_load_dwordx2 %1, %2, %3"
               : "=&s"(px), "=&s"(pw) : "s"(__builtin_amdgcn_kernarg_segment_ptr()), "s"(8 + 8 * (w >> 2)));
  SB();
  const unsigned mi = (bid & 6) * 2 + ((bid >> 3) & 3), half = bid & 1, idx = bid >> 5;
  const unsigned rot = ((bid & 6) + 4 * half + ((bid >> 3) & 1) + (idx & 3)) & 7;
  unsigned vw0, vw1, vx0, vx1;
  {
    const unsigned lane = threadIdx.x & 63, g = lane >> 4;
    vw0 = (lane >> 3) * 3584 + (((lane & 7) ^ ((g & 1) << 1)) << 4);
    vw1 = (vw0 ^ 64) + 8 * 3584 - 1024;
    vx0 = (lane >> 3) * 1024 + (((lane & 7) ^ g) << 4);
    vx1 = (vx0 ^ 64) + 8192 - 1024;
  }
  unsigned sx0 = (mi * 128 + 16 * w) * 1024;
  unsigned sw0 = (16 * (w & 3)) * 3584 + (half * 14 + idx) * 128;
  unsigned mu0 = lds0 + w * 2048;
  unsigned sw00 = sw0 + rot * (64 * 3584), sx00 = sx0 + rot * 128;
  asm volatile("" : "+v"(vw0), "+v"(vw1), "+v"(vx0), "+v"(vx1));
  asm volatile("" : "+s"(sx0), "+s"(sw0), "+s"(mu0), "+s"(sw00), "+s"(sx00));
  SB();
  asm volatile("s_waitcnt lgkmcnt(0)" : "+s"(px), "+s"(pw));
  const u32x4 srdX = {px[0], px[1] & 0xffffu, M_ * D_ * 2, 0x00020000u};
  const u32x4 srdW = {pw[0], pw[1] & 0xffffu, D_ * F_ * 2, 0x00020000u};
  auto issue_xw = [&](int n, int i, bool pre = false) {
    const int s = n < 16 ? n >> 1 : n - 16;
    const unsigned sg = (s + rot) & 7;
    if (n < 16 && (n & 1) == 0) {
      const unsigned so = n == 0 ? sx00 : sx0 + sg * 128;
      if (i == 0) { if (pre) dma16n<0>(vx0, srdX, so); else dma16<0>(mu0 + uaddr(n), vx0, srdX, so); } else dma16n<1024>(vx1, srdX, so);
    } else {
      const unsigned so = n == 1 ? sw00 : sw0 + sg * (64 * 3584) + (n >= 16 ? 896 : 0);
      if (i == 0) { if (pre) dma16n<0>(vw0, srdW, so); else dma16<0>(mu0 + uaddr(n), vw0, srdW, so); } else dma16n<1024>(vw1, srdW, so);
    }
  };
  if (busy) {
    issue_xw(0, 0); issue_xw(0, 1); issue_xw(1, 0); issue_xw(1, 1);
    BAR();
    issue_xw(2, 0); issue_xw(2, 1); issue_xw(3, 0); issue_xw(3, 1);
  } else {
    const unsigned o = min(threadIdx.x * 64u, CODE_BYTES);
    asm volatile("global_load_dword v249, %0, %1" ::"v"(o), "s"(pc) : "v249", "memory");
  }
  SB();
  const unsigned tid = opq(threadIdx.x), lane = tid & 63;
  const unsigned bid2 = opqs(bid), xcd = bid2 & 7, ml = (bid2 >> 3) & 3;
  unsigned rank = half * 8 + idx;
  const unsigned m64 = 2 * mi + half, n64 = idx;
  const unsigned g = lane >> 4, q = (lane & 15) >> 2, p = lane & 3;
  const u32x4 srdH = make_srd(ws, SYNC_OFF + 16 * 8192);
  const u32x4 srdD = make_srd(Wd, F_ * D_ * 2);
  unsigned syncb = SYNC_OFF + mi * 8192;
  const unsigned lane16 = lane * 16;

  const unsigned kq = w >> 1, mb = w & 1;
  unsigned rot2 = 2 * ml + idx; rot2 = rot2 >= 7 ? rot2 - 7 : rot2;
  unsigned kb[7];
  auto set_kb = [&]() {
    unsigned e = opqs(rot2);
#pragma unroll
    for (int j = 0; j < 7; ++j) {
      kb[j] = opqs((e + 7 * kq + (kq >> 1) * 14) * 1024);
      e = e == 6 ? 0 : e + 1;
    }
  };
  auto kap = [&](int s) { return kb[s % 7] + (s >= 7 ? 14 * 1024 : 0); };
  const unsigned vd0 = (lane >> 3) * 1024 + (((lane & 7) ^ (g << 1)) << 4);
  const unsigned vd1 = vd0 + 8192 - 1024;
  unsigned sd0 = (16 * mb) * 1024 + n64 * 128;
  auto issue_wd = [&](int s, int i, bool pre = false) {
    if (i == 0) { if (pre) dma16n<0>(vd0, srdD, sd0 + kap(s) * 32); else dma16<0>(mu0 + uaddr(24 + s), vd0, srdD, sd0 + kap(s) * 32); }
    else dma16n<1024>(vd1, srdD, sd0 + kap(s) * 32);
  };
  const unsigned dl = lds0 + kq * 4096 + (4 * g + q) * 128 + (((2 * g + (q >> 1)) & 3) << 5) + p * 8;
  unsigned db[3][4];
  auto set_db = [&](int k) {
#pragma unroll
    for (int c = 0; c < 4; ++c) db[k][c] = opq((dl ^ (c << 5)) + k * 4 * UNIT);
  };
  auto ldd = [&](int s, int a, int u) {
    const int ua = uaddr(24 + s);
    return tr_read(db[ua >> 16][a] + (ua & 65535) + u * 2048);
  };
  u32x2 ep, tag, fl;
  auto mktag = [&]() {
    const uint64_t t = (((uint64_t)ep[1] << 32) | ep[0]) * 0x9E3779B97F4A7C15ull + 1ull;
    tag = u32x2{(unsigned)t, (unsigned)(t >> 32)};
  };
  const unsigned one = tid == 0 ? 0u : 0x40000000u;
  const unsigned fo = tid < 16 ? 128 * (1 + tid) : 0x40000000u;
  unsigned fmask = 0x7F7Fu;
  auto flags_ok = [&]() { return (rfl((unsigned)__builtin_amdgcn_ballot_w64(fl[0] == tag[0] && fl[1] == tag[1])) & fmask) == fmask; };
  auto poll = [&](unsigned off) {
    unsigned cnt = SPIN_CAP, t;
#define PLD(r) "buffer_load_dwordx2 " r ", %2, %3, %4 offen sc1\n\t"
#define PCK(r) "s_waitcnt vmcnt(2)\n\tv_cmp_eq_u64 vcc, " r ", %5\n\ts_nop 1\n\ts_and_b32 %1, vcc_lo, %6\n\ts_cmp_eq_u32 %1, %6\n\ts_cbranch_scc1 2f\n\t"
    asm volatile(PLD("v[250:251]") "s_sleep 3\n\t" PLD("v[252:253]") "s_sleep 3\n\t" PLD("v[254:255]")
                 "1:\n\t"
                 PCK("v[250:251]") PLD("v[250:251]") PCK("v[252:253]") PLD("v[252:253]") PCK("v[254:255]") PLD("v[254:255]")
                 "s_sub_u32 %0, %0, 1\n\ts_cbranch_scc0 1b\n\t"
                 "2:"
                 : "+s"(cnt), "=&s"(t) : "v"(fo), "s"(srdH), "s"(syncb + off), "v"(tag), "s"(fmask)
                 : "v250", "v251", "v252", "v253", "v254", "v255", "vcc", "scc", "memory");
#undef PLD
#undef PCK
  };
  auto poll_slow = [&](unsigned off) {
#pragma nounroll
    for (unsigned spin = 0; spin < SPIN_CAP; ++spin) {
      gload8_sc1(fl, fo, srdH, syncb + off);
      asm volatile("s_waitcnt vmcnt(0)" : "+v"(fl)::"memory");
      if (flags_ok()) break;
      __builtin_amdgcn_s_sleep(8);
    }
  };
  Frag df[2][4];
  u32x4 hf[14][2];
  unsigned sh2 = ((m64 * 4 + mb * 2) * 56) * 1024;
  const unsigned lane16b = lane16 + 56 * 1024;
  auto issue_h = [&](int s, int b) { gload16_sc1(hf[s][b], b ? lane16b : lane16, srdH, sh2 + kap(s)); };

  set_kb();
  if (__builtin_expect(busy, 1)) {
    gload8_sc1(ep, 0u, srdH, syncb);
    const unsigned wm = w & 3, wn = w >> 2;
    const unsigned xrow = 32 * wm + (lane & 15);
    const unsigned xa0 = lds0 + xrow * 128 + ((g ^ ((xrow >> 1) & 7)) << 4);
    const unsigned wa0 = lds0 + (8 * g + q) * 128 + (((2 * wn) ^ ((q >> 1) | ((g & 1) << 1))) << 5) + p * 8;
    unsigned xa[3][2], wa[3][2];
#pragma unroll
    for (int k = 0; k < 3; ++k)
#pragma unroll
      for (int c = 0; c < 2; ++c) { xa[k][c] = opq((xa0 ^ (c << 6)) + k * 4 * UNIT); wa[k][c] = opq((wa0 ^ (c << 5)) + k * 4 * UNIT); }

    f32x4 acc[2][2][4];
    bf16x8 xf[8][2][2];
    Frag wf[4][2];
    auto ldx = [&](int s, int b, int kk) { const int ua = uaddr(2 * s); xf[s][b][kk] = lds_read16(xa[ua >> 16][kk] + (ua & 65535) + b * 2048); };
    auto ldw = [&](int G, int kk, int u = 2) {
      const int st = G >> 2, a = ((G & 1) << 1) | ((G >> 1) & 1), ua = uaddr(st < 8 ? 2 * st + 1 : 8 + st);
      const unsigned ad = wa[ua >> 16][a & 1] + (ua & 65535) + (a >> 1) * 8192 + kk * 4096;
      if (u != 1) wf[G & 3][kk].lo = tr_read(ad);
      if (u != 0) wf[G & 3][kk].hi = tr_read(ad + 512);
    };
    const unsigned sh = ((mi * 8 + wm * 2) * 56 + half * 28 + idx * 2 + wn) * 1024;
    u32x4 ho[2];
    auto epi = [&](int ps, int b, int c) {
      const int t = c >> 1, i = c & 1;
      float g0 = __builtin_elementwise_maximum(acc[ps][b][t][2 * i], 0.f), g1 = __builtin_elementwise_maximum(acc[ps][b][t][2 * i + 1], 0.f);
      float p0 = g0 * acc[ps][b][2 + t][2 * i];
      asm volatile("" : "+v"(p0));
      ho[b][c] = pack_bf16(p0, g1 * acc[ps][b][2 + t][2 * i + 1]);
    };
    auto sth = [&](int ps, int b) { gstore16_sc1(ho[b], lane16, srdH, sh + b * (56 * 1024) + ps * (14 * 1024)); };

    auto vmem = [&](auto PP) INL {
      sfor<first_op(PP.v), first_op(PP.v + 1)>([&](auto K) INL {
        constexpr Op o = SCH.op[K.v];
        if constexpr (o.pos == PP.v) {
          constexpr bool pre = PP.v < 128 && K.v == first_op(PP.v);
          if constexpr (o.kind == K_DMA) { if constexpr (o.n < 24) issue_xw(o.n, o.i, pre); else issue_wd(o.n - 24, o.i, pre); }
          if constexpr (o.kind == K_STORE) sth(o.n, o.i);
          if constexpr (o.kind == K_FLAG) gstore8_sc1(tag, one, srdH, syncb + 128 * (1 + rank));
          if constexpr (o.kind == K_POLL) gload8_sc1(fl, fo, srdH, syncb);
          if constexpr (o.kind == K_H) issue_h(o.n, o.i);
        }
      });
    };
    wait_vm<5>();
    BAR();
    ldw(0, 0); ldx(0, 0, 0); ldw(0, 1); ldx(0, 0, 1); ldx(0, 1, 0); ldx(0, 1, 1); ldw(1, 0); ldw(1, 1); ldw(2, 0); ldw(2, 1);
    SB();
    sfor<0, 128>([&](auto Z) INL {
      constexpr int P = Z.v, ps = P >> 6, s = (P >> 3) & 7, G = P >> 1, a = ((G & 1) << 1) | ((G >> 1) & 1), b = P & 1;
      if constexpr (is_bar(P)) {
        constexpr int n = bar_wait(P);
        static_assert(n >= 0 && n < 24, "wait count");
        if constexpr (P == 10) asm volatile("s_waitcnt vmcnt(%1)" : "+v"(ep) : "i"(n) : "memory");
        else if constexpr (P == 122) {
          asm volatile("s_waitcnt vmcnt(%1)" : "+v"(fl) : "i"(n) : "memory");
          if (w == 0) { if (__builtin_expect(!flags_ok(), 0)) poll(0); }
        } else wait_vm<n>();
        BAR();
      }
      if constexpr (P < 58 ? (P & 1) == 0 : (P & 3) == 2) __builtin_amdgcn_s_setprio(P < 58 ? 3 - (((P + 6) & 7) >> 1) : 3 - (((P + 6) & 15) >> 2));
      if constexpr (s == 0) mfma_new(acc[ps][b][a], join(wf[G & 3][0]), xf[s][b][0]); else mfma_acc(acc[ps][b][a], join(wf[G & 3][0]), xf[s][b][0]);
      SB();
      if constexpr (G + 3 < 64) ldw(G + 3, b, 0);
      if constexpr (P >= 124) df[0][P - 124].lo = ldd(0, P - 124, 0);
      {
        constexpr int k0 = first_op(P);
        if constexpr (k0 < first_op(P + 1) && SCH.op[k0].kind == K_DMA && SCH.op[k0].i == 0) set_m0(mu0 + uaddr(SCH.op[k0].n));
        else if constexpr (P < 56 && (P & 7) >= 2 && (P & 7) < 6) ldx(s + 1, ((P - 2) >> 1) & 1, P & 1);
      }
      SB();
      mfma_acc(acc[ps][b][a], join(wf[G & 3][1]), xf[s][b][1]);
      SB();
      if constexpr (G + 3 < 64) ldw(G + 3, b, 1);
      if constexpr (P >= 124) df[0][P - 124].hi = ldd(0, P - 124, 1);
      {
        constexpr int k0 = first_op(P);
        if constexpr (k0 < first_op(P + 1) && SCH.op[k0].kind == K_DMA && SCH.op[k0].i == 0 && P < 56 && (P & 7) >= 2 && (P & 7) < 6) ldx(s + 1, ((P - 2) >> 1) & 1, P & 1);
      }
      if constexpr (P == 120) set_db(1);
      if constexpr ((P & 63) == 62 || (P & 63) == 63) { epi(ps, P & 1, 0); epi(ps, P & 1, 1); }
      if constexpr (P == 65 || P == 66) { epi(0, P - 65, 2); epi(0, P - 65, 3); }
      if constexpr (P == 116) set_db(0);
      if constexpr (P == 118) set_db(2);
      if constexpr (P == 84) mktag();
      vmem(Z);
      SB();
    });
    epi(1, 0, 2); epi(1, 0, 3);
    sth(1, 0);
    SB();
    epi(1, 1, 2); epi(1, 1, 3);
    sth(1, 1);
    issue_h(2, 0); issue_h(2, 1);
    __builtin_amdgcn_s_setprio(0);
  } else {
    gload8_sc1(ep, 0u, srdH, syncb);
    set_db(0); set_db(1); set_db(2);
    {
      unsigned t = 0;
      const unsigned rotx = ((xcd & 6) + 4 * half) & 7;
      {
        const unsigned li = (ml * 4 + (w & 3)) * 56 + lane;
        const unsigned row = li / 7, c = li - row * 7;
        const unsigned vo = lane < 56 ? (rotx * 64 + row) * 3584 + (half * 14 + 7 + c) * 128 : 0x40000000u;
        asm volatile("buffer_load_dword %0, %1, %2, 0 offen" : "+v"(t) : "v"(vo), "s"(srdW) : "memory");
      }
#pragma unroll
      for (int i = 0; i < 3; ++i) {
        unsigned c = i + xcd; c = c >= 7 ? c - 7 : c; c = c >= 7 ? c - 7 : c;
        asm volatile("buffer_load_dword %0, %1, %2, %3 offen" : "+v"(t) : "v"(lane * 128), "s"(srdD), "s"(((c * 4 + ml) * 8 + w) * 8192) : "memory");
      }
      asm volatile("s_waitcnt vmcnt(0)" : "+v"(t), "+v"(ep)::"memory");
      mktag();
    }
#pragma unroll
    for (int s = 0; s < 7; ++s) { issue_wd(s, 0); issue_wd(s, 1); }
    if (w == 0) poll_slow(0); else wait_vm<0>();
    BAR();
#pragma unroll
    for (int s = 0; s < 3; ++s) { issue_h(s, 0); issue_h(s, 1); }
#pragma unroll
    for (int a = 0; a < 4; ++a) { df[0][a].lo = ldd(0, a, 0); df[0][a].hi = ldd(0, a, 1); }
    wait_vm<0>();
  }
  asm volatile("" : "+s"(mu0), "+s"(sd0), "+s"(sh2), "+s"(syncb), "+s"(rank));
  asm volatile("" : "+s"(kb[0]), "+s"(kb[1]), "+s"(kb[2]), "+s"(kb[3]), "+s"(kb[4]), "+s"(kb[5]), "+s"(kb[6]));
  fmask = rank == 7 ? 0xFFFFu : 0x7F7Fu;
  f32x4 acc2[2][4];
  const unsigned pa = opq(lds0 + ((kq + 8) % 10) * UNIT + (mb * 32 + (lane & 15)) * 256 + ((g ^ (lane & 7)) << 4));
  const unsigned pa1 = opq(pa ^ 64);
  const unsigned ra = opq(lds0 + (w * 8 + (lane >> 3)) * 256 + (((2 * (lane & 7)) ^ (lane >> 3)) << 4)), rb = opq(ra ^ 16);
  const unsigned ra8 = opq(ra + 8 * UNIT), rb8 = opq(rb + 8 * UNIT);
  const unsigned vo = opq((w * 8 + (lane >> 3)) * 1024 + (lane & 7) * 16), so = opqs(m64 * 65536 + n64 * 128);
  const u32x4 srdO = make_srd(out, M_ * D_ * 2);
  auto wpart = [&](int a) {
    const unsigned ad = ((a & 1) ? pa1 : pa) + (a >> 1) * 128;
    *(LDS_AS f32x4*)(uintptr_t)(ad) = acc2[0][a];
    *(LDS_AS f32x4*)(uintptr_t)(ad + 16 * 256) = acc2[1][a];
  };
  auto vmem2 = [&](auto PP) INL {
    sfor<first_op(PP.v), first_op(PP.v + 1)>([&](auto K) INL {
      constexpr Op o = SCH.op[K.v];
      if constexpr (o.pos == PP.v) {
        if constexpr (o.kind == K_DMA) issue_wd(o.n - 24, o.i);
        if constexpr (o.kind == K_FLAG) gstore8_sc1(tag, one, srdH, syncb + 128 * (17 + rank));
        if constexpr (o.kind == K_POLL) gload8_sc1(fl, fo, srdH, syncb + 2048);
        if constexpr (o.kind == K_H) issue_h(o.n, o.i);
      }
    });
  };
  sfor<0, 14>([&](auto S) INL {
    constexpr int s = S.v;
    if constexpr (s == Y1 || s == FCHK || s == Y2 || s == Y3) {
      constexpr int n = bar_wait(Q(s, 0));
      static_assert(n >= 0 && n < 24, "wait count");
      if constexpr (s == FCHK) {
        asm volatile("s_waitcnt vmcnt(%1)" : "+v"(fl) : "i"(n) : "memory");
        if (w == 0) { if (__builtin_expect(!flags_ok(), 0)) poll(2048); }
      } else wait_vm<n>();
      BAR();
      if constexpr (s == Y3) { if (rank == 7 && w == 0) gstore8_sc1(tag, one, srdH, syncb); }
    }
    vmem2(IC<Q(s, 0)>{});
    if constexpr (s == Y1) asm volatile("" : "+v"(hf[s][0]), "+v"(hf[s][1])::"memory");
    else asm volatile("s_waitcnt vmcnt(%2)" : "+v"(hf[s][0]), "+v"(hf[s][1]) : "i"(younger_than(K_H, s, 1, Q(s, 1))) : "memory");
    SB();
    sfor<0, 4>([&](auto A) INL {
      constexpr int a = A.v;
      if constexpr (s == 0) mfma_new(acc2[0][a], join(df[0][a]), hf[0][0]); else mfma_acc(acc2[0][a], join(df[s & 1][a]), hf[s][0]);
      SB();
      if constexpr (s + 1 < 14) df[(s + 1) & 1][a].lo = ldd(s + 1, a, 0);
      SB();
      if constexpr (s == 0) mfma_new(acc2[1][a], join(df[0][a]), hf[0][1]); else mfma_acc(acc2[1][a], join(df[s & 1][a]), hf[s][1]);
      SB();
      if constexpr (s + 1 < 14) df[(s + 1) & 1][a].hi = ldd(s + 1, a, 1);
      vmem2(IC<Q(s, a + 1)>{});
      if constexpr (s == 13 && a == 3) { asm volatile("s_nop 1" ::: "memory"); SB(); wpart(0); SB(); wpart(1); }
      SB();
    });
  });
  wpart(2);
  SB();
  asm volatile("s_nop 1" ::: "memory");
  SB();
  wpart(3);
  wait_lgkm0();
  BAR();
  {
    f32x4 p0[4], p1[4];
#pragma unroll
    for (int v = 0; v < 4; ++v) {
      p0[v] = *(LDS_AS f32x4*)(uintptr_t)((v < 2 ? ra8 : ra) + (v & 1) * UNIT);
      p1[v] = *(LDS_AS f32x4*)(uintptr_t)((v < 2 ? rb8 : rb) + (v & 1) * UNIT);
    }
    SB();
    const f32x4 s0 = (p0[0] + p0[1]) + (p0[2] + p0[3]), s1 = (p1[0] + p1[1]) + (p1[2] + p1[3]);
    u32x4 o = {pack_bf16(s0[0], s0[1]), pack_bf16(s0[2], s0[3]), pack_bf16(s1[0], s1[1]), pack_bf16(s1[2], s1[3])};
    asm volatile("buffer_store_dwordx4 %0, %1, %2, %3 offen" ::"v"(o), "v"(vo), "s"(srdO), "s"(so) : "memory");
  }
}

extern "C" void kernel_launch(void* const* d_in, const int* in_sizes, int n_in, void* d_out, int out_size,
                              void* d_ws, size_t ws_size, hipStream_t stream) {
  (void)in_sizes; (void)n_in; (void)out_size; (void)ws_size;
  (void)hipFuncSetAttribute((const void*)k_fused, hipFuncAttributeMaxDynamicSharedMemorySize, LDS_TOTAL);
  k_fused<<<256, 512, LDS_TOTAL, stream>>>(d_in[0], d_in[1], d_in[2], d_in[3], d_out, d_ws);
}
```

```cpp
#include <hip/hip_runtime.h>
#include <hip/hip_bf16.h>
#include <stdint.h>

typedef __attribute__((ext_vector_type(8))) __bf16 bf16x8;
typedef __attribute__((ext_vector_type(4))) short s16x4;
typedef __attribute__((ext_vector_type(8))) short s16x8;
typedef __attribute__((ext_vector_type(2))) __bf16 bf16x2;
typedef __attribute__((ext_vector_type(2))) float f32x2;
typedef __attribute__((ext_vector_type(4))) float f32x4;
typedef __attribute__((ext_vector_type(4))) unsigned u32x4;
typedef __attribute__((ext_vector_type(2))) unsigned u32x2;
#define LDS_AS __attribute__((address_space(3)))

constexpr int M_ = 2048, D_ = 512, F_ = 1792;
constexpr unsigned SYNC_OFF = 8u << 20;
constexpr unsigned SPIN_CAP = 1u << 17;
constexpr unsigned CODE_BYTES = 12544;

__device__ __forceinline__ unsigned rfl(unsigned v) { return __builtin_amdgcn_readfirstlane(v); }

__device__ __forceinline__ u32x4 make_srd(const void* p, unsigned bytes) {
  uint64_t a = (uint64_t)p;
  u32x4 r;
  r[0] = rfl((unsigned)a);
  r[1] = rfl((unsigned)(a >> 32)) & 0xffffu;
  r[2] = bytes;
  r[3] = 0x00020000u;
  return r;
}

template <int IMM>
__device__ __forceinline__ void dma16(unsigned m0v, unsigned voff, u32x4 srd, unsigned soff) {
  asm volatile("s_mov_b32 m0, %0\n\ts_nop 0\n\tbuffer_load_dwordx4 %1, %2, %3 offen offset:%4 lds"
               ::"s"(m0v), "v"(voff), "s"(srd), "s"(soff), "i"(IMM) : "memory");
}
template <int IMM>
__device__ __forceinline__ void dma16n(unsigned voff, u32x4 srd, unsigned soff) {
  asm volatile("buffer_load_dwordx4 %0, %1, %2 offen offset:%3 lds" ::"v"(voff), "s"(srd), "s"(soff), "i"(IMM) : "memory");
}
__device__ __forceinline__ void set_m0(unsigned v) { asm volatile("s_mov_b32 m0, %0" ::"s"(v) : "memory"); }
__device__ __forceinline__ unsigned opq(unsigned v) { asm volatile("" : "+v"(v)); return v; }
__device__ __forceinline__ unsigned opqs(unsigned v) { asm volatile("" : "+s"(v)); return v; }
__device__ __forceinline__ void gload16_sc1(u32x4& dst, unsigned voff, u32x4 srd, unsigned soff) {
  asm volatile("buffer_load_dwordx4 %0, %1, %2, %3 offen sc1" : "=v"(dst) : "v"(voff), "s"(srd), "s"(soff) : "memory");
}
__device__ __forceinline__ void gstore16_sc1(u32x4 v, unsigned voff, u32x4 srd, unsigned soff) {
  asm volatile("buffer_store_dwordx4 %0, %1, %2, %3 offen sc1\n\ts_nop 1" ::"v"(v), "v"(voff), "s"(srd), "s"(soff) : "memory");
}
__device__ __forceinline__ void gload8_sc1(u32x2& dst, unsigned voff, u32x4 srd, unsigned soff) {
  asm volatile("buffer_load_dwordx2 %0, %1, %2, %3 offen sc1" : "=v"(dst) : "v"(voff), "s"(srd), "s"(soff) : "memory");
}
__device__ __forceinline__ void gstore8_sc1(u32x2 v, unsigned voff, u32x4 srd, unsigned soff) {
  asm volatile("buffer_store_dwordx2 %0, %1, %2, %3 offen sc1" ::"v"(v), "v"(voff), "s"(srd), "s"(soff) : "memory");
}

__device__ __forceinline__ s16x4 tr_read(unsigned addr) {
  return __builtin_amdgcn_ds_read_tr16_b64_v4i16((LDS_AS s16x4*)(uintptr_t)addr);
}
struct Frag { s16x4 lo, hi; };
__device__ __forceinline__ bf16x8 join(const Frag& f) {
  s16x8 v = __builtin_shufflevector(f.lo, f.hi, 0, 1, 2, 3, 4, 5, 6, 7);
  return __builtin_bit_cast(bf16x8, v);
}
__device__ __forceinline__ bf16x8 lds_read16(unsigned addr) { return *(LDS_AS bf16x8*)(uintptr_t)addr; }
__device__ __forceinline__ unsigned pack_bf16(float a, float b) {
  f32x2 v = {a, b};
  bf16x2 r = __builtin_convertvector(v, bf16x2);
  return __builtin_bit_cast(unsigned, r);
}
template <class TB>
__device__ __forceinline__ void mfma_acc(f32x4& c, bf16x8 a, TB b) {
  asm volatile("v_mfma_f32_16x16x32_bf16 %0, %1, %2, %0" : "+v"(c) : "v"(a), "v"(b));
}
template <class TB>
__device__ __forceinline__ void mfma_new(f32x4& c, bf16x8 a, TB b) {
  asm volatile("v_mfma_f32_16x16x32_bf16 %0, %1, %2, 0" : "=&v"(c) : "v"(a), "v"(b));
}

template <int I> struct IC { static constexpr int v = I; };
template <int I, int N, class F> __device__ __forceinline__ void sfor(F&& f) {
  if constexpr (I < N) { f(IC<I>{}); sfor<I + 1, N>(f); }
}
template <int N> __device__ __forceinline__ void wait_vm() { asm volatile("s_waitcnt vmcnt(%0)" ::"i"(N) : "memory"); }
__device__ __forceinline__ void wait_lgkm0() { asm volatile("s_waitcnt lgkmcnt(0)" ::: "memory"); }
#define BAR() asm volatile("s_barrier" ::: "memory")
#define SB() __builtin_amdgcn_sched_barrier(0)
#define INL __attribute__((always_inline))

constexpr int UNIT = 16384, LDS_TOTAL = 10 * UNIT;
constexpr int uaddr(int n) { return (n % 10) * UNIT; }


enum { K_DMA = 1, K_STORE, K_FLAG, K_POLL, K_H };
struct Op { int pos, kind, n, i; };
struct Sched { int cnt; Op op[200]; };
constexpr void put(Sched& s, int pos, int kind, int n, int i) { s.op[s.cnt++] = Op{pos, kind, n, i}; }
constexpr void spread(Sched& s, int p0, int p1, int u0, int nu) {
  for (int k = 0; k < 2 * nu; ++k) put(s, p0 + (k * (p1 - p0)) / (2 * nu), K_DMA, u0 + (k >> 1), k & 1);
}
constexpr int Q(int step, int slot) { return 200 + 8 * step + slot; }
constexpr void pair(Sched& s, int step, int slot, int kind, int n) { put(s, Q(step, slot), kind, n, 0); put(s, Q(step, slot == 0 ? 0 : slot + 1), kind, n, 1); }
constexpr int HB = 3;
constexpr int Y1 = 2, Y2 = 5, FCHK = 7, Y3 = 10;
constexpr Sched make_sched() {
  Sched s{};
  for (int n = 0; n < 4; ++n) { put(s, -1, K_DMA, n, 0); put(s, -1, K_DMA, n, 1); }
  put(s, -1, K_POLL, 0, 0);
  spread(s, 0, 2, 4, 2);
  for (int t = 0; t < 7; ++t) spread(s, 8 * t + 2, 8 * t + 9, 2 * t + 6, 2);
  spread(s, 58, 64, 20, 2);
  put(s, 65, K_STORE, 0, 0);
  put(s, 66, K_STORE, 0, 1);
  spread(s, 72, 80, 22, 1);
  spread(s, 82, 89, 23, 2);
  put(s, 90, K_FLAG, 0, 0);
  spread(s, 91, 103, 25, 2);
  spread(s, 103, 111, 27, 2);
  put(s, 111, K_POLL, 1, 0);
  spread(s, 112, 121, 29, 2);
  for (int k = 0; k < 4; ++k) put(s, 122 + k, K_H, k >> 1, k & 1);
  put(s, 128, K_STORE, 1, 0);
  put(s, 128, K_STORE, 1, 1);
  put(s, 128, K_H, 2, 0);
  put(s, 128, K_H, 2, 1);
  put(s, Q(Y1, 0), K_FLAG, 1, 0);
  pair(s, 2, 0, K_H, 3); pair(s, 2, 0, K_H, 4);
  pair(s, 2, 3, K_DMA, 31);
  pair(s, 3, 1, K_H, 5); pair(s, 3, 3, K_H, 6);
  pair(s, 4, 1, K_DMA, 32); pair(s, 4, 3, K_DMA, 33);
  pair(s, 5, 1, K_DMA, 34); pair(s, 5, 3, K_DMA, 35);
  put(s, Q(6, 0), K_POLL, 2, 0);
  pair(s, 6, 1, K_DMA, 36); pair(s, 6, 3, K_DMA, 37);
  for (int k = 0; k < HB; ++k) pair(s, 7, 0, K_H, 7 + k);
  for (int k = 7 + HB; k < 14; ++k) pair(s, k - HB, 1, K_H, k);
  return s;
}
constexpr Sched SCH = make_sched();
constexpr int younger_than(int kind, int n, int i, int P) {
  int c = -1;
  for (int k = 0; k < SCH.cnt; ++k) {
    if (SCH.op[k].pos >= P) break;
    if (SCH.op[k].kind == kind && SCH.op[k].n == n && SCH.op[k].i == i) c = 0;
    else if (c >= 0 && SCH.op[k].kind != K_FLAG && !(SCH.op[k].kind == K_POLL && SCH.op[k].n > 0)) ++c;
  }
  return c;
}
constexpr int first_op(int P) { int k = 0; while (k < SCH.cnt && SCH.op[k].pos < P) ++k; return k; }
constexpr bool is_bar(int P) { return P < 64 ? (P & 7) == 2 : (P & 15) == 10; }
constexpr int bar_wait(int P) {
  if (P < 64) return younger_than(K_DMA, 2 * (P >> 3) + 3, 1, P);
  if (P == 74) return younger_than(K_DMA, 19, 1, P);
  if (P == 90) return younger_than(K_STORE, 0, 1, P);
  if (P == 106) return younger_than(K_DMA, 23, 1, P);
  if (P == 122) return younger_than(K_POLL, 1, 0, P);
  if (P == Q(Y1, 0)) return 0;
  if (P == Q(Y2, 0)) return younger_than(K_DMA, 31, 1, P);
  if (P == Q(FCHK, 0)) return younger_than(K_POLL, 2, 0, P);
  return younger_than(K_DMA, 37, 1, P);
}
constexpr int last_use(int n) { return n < 16 ? 8 * (n >> 1) + 7 : (n < 24 ? 8 * (n - 8) + 7 : Q(n - 24, 4)); }
constexpr int first_read(int n) { return n < 16 ? 8 * (n >> 1) - 6 : (n < 24 ? 8 * (n - 8) - 6 : (n == 24 ? 124 : Q(n - 25, 0))); }
constexpr bool any_bar(int P) { return P < 128 ? is_bar(P) : (P == Q(Y1, 0) || P == Q(Y2, 0) || P == Q(FCHK, 0) || P == Q(Y3, 0)); }
constexpr bool ring_ok() {
  for (int k = 0; k < SCH.cnt; ++k) {
    if (SCH.op[k].kind != K_DMA || SCH.op[k].n < 10) continue;
    bool ok = false;
    for (int P = last_use(SCH.op[k].n - 10) + 1; P <= SCH.op[k].pos; ++P) ok = ok || any_bar(P);
    if (!ok) return false;
  }
  for (int n = 2; n < 38; ++n) {
    bool ok = false;
    for (int P = 0; P <= first_read(n); ++P) ok = ok || (any_bar(P) && younger_than(K_DMA, n, 1, P) >= bar_wait(P));
    if (!ok) return false;
  }
  return true;
}
static_assert(ring_ok(), "LDS ring schedule");
constexpr bool sched_sorted() { for (int k = 1; k < SCH.cnt; ++k) if (SCH.op[k].pos < SCH.op[k - 1].pos) return false; return true; }
static_assert(sched_sorted(), "schedule order");

__global__ __launch_bounds__(512, 2) void k_fused(const void* __restrict__ x, const void* __restrict__ Wg,
                                                  const void* __restrict__ Wu, const void* __restrict__ Wd,
                                                  void* __restrict__ out, void* __restrict__ ws) {
  extern __shared__ __attribute__((aligned(16))) char smem[];
  const unsigned lds0 = (unsigned)(uintptr_t)(LDS_AS char*)smem;
  const unsigned bid = blockIdx.x;
  const unsigned w = rfl(threadIdx.x >> 6);
  const bool busy = bid < 224;
  uint64_t pc;
  asm volatile("s_getpc_b64 %0" : "=s"(pc));
  u32x2 px, pw;
  asm volatile("s_load_dwordx2 %0, %2, 0x0\n\ts_load_dwordx2 %1, %2, %3"
               : "=&s"(px), "=&s"(pw) : "s"(__builtin_amdgcn_kernarg_segment_ptr()), "s"(8 + 8 * (w >> 2)));
  SB();
  const unsigned mi = (bid & 6) * 2 + ((bid >> 3) & 3), half = bid & 1, idx = bid >> 5;
  const unsigned rot = ((bid & 6) + 4 * half + ((bid >> 3) & 1) + (idx & 3)) & 7;
  unsigned vw0, vw1, vx0, vx1;
  {
    const unsigned lane = threadIdx.x & 63, g = lane >> 4;
    vw0 = (lane >> 3) * 3584 + (((lane & 7) ^ ((g & 1) << 1)) << 4);
    vw1 = (vw0 ^ 64) + 8 * 3584 - 1024;
    vx0 = (lane >> 3) * 1024 + (((lane & 7) ^ g) << 4);
    vx1 = (vx0 ^ 64) + 8192 - 1024;
  }
  unsigned sx0 = (mi * 128 + 16 * w) * 1024;
  unsigned sw0 = (16 * (w & 3)) * 3584 + (half * 14 + idx) * 128;
  unsigned mu0 = lds0 + w * 2048;
  unsigned sw00 = sw0 + rot * (64 * 3584), sx00 = sx0 + rot * 128;
  asm volatile("" : "+v"(vw0), "+v"(vw1), "+v"(vx0), "+v"(vx1));
  asm volatile("" : "+s"(sx0), "+s"(sw0), "+s"(mu0), "+s"(sw00), "+s"(sx00));
  SB();
  asm volatile("s_waitcnt lgkmcnt(0)" : "+s"(px), "+s"(pw));
  const u32x4 srdX = {px[0], px[1] & 0xffffu, M_ * D_ * 2, 0x00020000u};
  const u32x4 srdW = {pw[0], pw[1] & 0xffffu, D_ * F_ * 2, 0x00020000u};
  auto issue_xw = [&](int n, int i, bool pre = false) {
    const int s = n < 16 ? n >> 1 : n - 16;
    const unsigned sg = (s + rot) & 7;
    if (n < 16 && (n & 1) == 0) {
      const unsigned so = n == 0 ? sx00 : sx0 + sg * 128;
      if (i == 0) { if (pre) dma16n<0>(vx0, srdX, so); else dma16<0>(mu0 + uaddr(n), vx0, srdX, so); } else dma16n<1024>(vx1, srdX, so);
    } else {
      const unsigned so = n == 1 ? sw00 : sw0 + sg * (64 * 3584) + (n >= 16 ? 896 : 0);
      if (i == 0) { if (pre) dma16n<0>(vw0, srdW, so); else dma16<0>(mu0 + uaddr(n), vw0, srdW, so); } else dma16n<1024>(vw1, srdW, so);
    }
  };
  if (busy) {
    issue_xw(0, 0); issue_xw(0, 1); issue_xw(1, 0); issue_xw(1, 1);
    BAR();
    issue_xw(2, 0); issue_xw(2, 1); issue_xw(3, 0); issue_xw(3, 1);
  } else {
    const unsigned o = min(threadIdx.x * 64u, CODE_BYTES);
    asm volatile("global_load_dword v249, %0, %1" ::"v"(o), "s"(pc) : "v249", "memory");
  }
  SB();
  const unsigned tid = opq(threadIdx.x), lane = tid & 63;
  const unsigned bid2 = opqs(bid), xcd = bid2 & 7, ml = (bid2 >> 3) & 3;
  unsigned rank = half * 8 + idx;
  const unsigned m64 = 2 * mi + half, n64 = idx;
  const unsigned g = lane >> 4, q = (lane & 15) >> 2, p = lane & 3;
  const u32x4 srdH = make_srd(ws, SYNC_OFF + 16 * 8192);
  const u32x4 srdD = make_srd(Wd, F_ * D_ * 2);
  unsigned syncb = SYNC_OFF + mi * 8192;
  const unsigned lane16 = lane * 16;

  const unsigned kq = w >> 1, mb = w & 1;
  unsigned rot2 = 2 * ml + idx; rot2 = rot2 >= 7 ? rot2 - 7 : rot2;
  unsigned kb[7];
  auto set_kb = [&]() {
    unsigned e = opqs(rot2);
#pragma unroll
    for (int j = 0; j < 7; ++j) {
      kb[j] = opqs((e + 7 * kq + (kq >> 1) * 14) * 1024);
      e = e == 6 ? 0 : e + 1;
    }
  };
  auto kap = [&](int s) { return kb[s % 7] + (s >= 7 ? 14 * 1024 : 0); };
  const unsigned vd0 = (lane >> 3) * 1024 + (((lane & 7) ^ (g << 1)) << 4);
  const unsigned vd1 = vd0 + 8192 - 1024;
  unsigned sd0 = (16 * mb) * 1024 + n64 * 128;
  auto issue_wd = [&](int s, int i, bool pre = false) {
    if (i == 0) { if (pre) dma16n<0>(vd0, srdD, sd0 + kap(s) * 32); else dma16<0>(mu0 + uaddr(24 + s), vd0, srdD, sd0 + kap(s) * 32); }
    else dma16n<1024>(vd1, srdD, sd0 + kap(s) * 32);
  };
  const unsigned dl = lds0 + kq * 4096 + (4 * g + q) * 128 + (((2 * g + (q >> 1)) & 3) << 5) + p * 8;
  unsigned db[3][4];
  auto set_db = [&](int k) {
#pragma unroll
    for (int c = 0; c < 4; ++c) db[k][c] = opq((dl ^ (c << 5)) + k * 4 * UNIT);
  };
  auto ldd = [&](int s, int a, int u) {
    const int ua = uaddr(24 + s);
    return tr_read(db[ua >> 16][a] + (ua & 65535) + u * 2048);
  };
  u32x2 ep, tag, fl;
  auto mktag = [&]() {
    const uint64_t t = (((uint64_t)ep[1] << 32) | ep[0]) * 0x9E3779B97F4A7C15ull + 1ull;
    tag = u32x2{(unsigned)t, (unsigned)(t >> 32)};
  };
  const unsigned one = tid == 0 ? 0u : 0x40000000u;
  const unsigned fo = tid < 16 ? 128 * (1 + tid) : 0x40000000u;
  unsigned fmask = 0x7F7Fu;
  auto flags_ok = [&]() { return (rfl((unsigned)__builtin_amdgcn_ballot_w64(fl[0] == tag[0] && fl[1] == tag[1])) & fmask) == fmask; };
  auto poll = [&](unsigned off) {
    unsigned cnt = SPIN_CAP, t;
#define PLD(r) "buffer_load_dwordx2 " r ", %2, %3, %4 offen sc1\n\t"
#define PCK(r) "s_waitcnt vmcnt(2)\n\tv_cmp_eq_u64 vcc, " r ", %5\n\ts_nop 1\n\ts_and_b32 %1, vcc_lo, %6\n\ts_cmp_eq_u32 %1, %6\n\ts_cbranch_scc1 2f\n\t"
    asm volatile(PLD("v[250:251]") "s_sleep 3\n\t" PLD("v[252:253]") "s_sleep 3\n\t" PLD("v[254:255]")
                 "1:\n\t"
                 PCK("v[250:251]") PLD("v[250:251]") PCK("v[252:253]") PLD("v[252:253]") PCK("v[254:255]") PLD("v[254:255]")
                 "s_sub_u32 %0, %0, 1\n\ts_cbranch_scc0 1b\n\t"
                 "2:"
                 : "+s"(cnt), "=&s"(t) : "v"(fo), "s"(srdH), "s"(syncb + off), "v"(tag), "s"(fmask)
                 : "v250", "v251", "v252", "v253", "v254", "v255", "vcc", "scc", "memory");
#undef PLD
#undef PCK
  };
  auto poll_slow = [&](unsigned off) {
#pragma nounroll
    for (unsigned spin = 0; spin < SPIN_CAP; ++spin) {
      gload8_sc1(fl, fo, srdH, syncb + off);
      asm volatile("s_waitcnt vmcnt(0)" : "+v"(fl)::"memory");
      if (flags_ok()) break;
      __builtin_amdgcn_s_sleep(8);
    }
  };
  Frag df[2][4];
  u32x4 hf[14][2];
  unsigned sh2 = ((m64 * 4 + mb * 2) * 56) * 1024;
  const unsigned lane16b = lane16 + 56 * 1024;
  auto issue_h = [&](int s, int b) { gload16_sc1(hf[s][b], b ? lane16b : lane16, srdH, sh2 + kap(s)); };

  set_kb();
  if (__builtin_expect(busy, 1)) {
    gload8_sc1(ep, 0u, srdH, syncb);
    const unsigned wm = w & 3, wn = w >> 2;
    const unsigned xrow = 32 * wm + (lane & 15);
    const unsigned xa0 = lds0 + xrow * 128 + ((g ^ ((xrow >> 1) & 7)) << 4);
    const unsigned wa0 = lds0 + (8 * g + q) * 128 + (((2 * wn) ^ ((q >> 1) | ((g & 1) << 1))) << 5) + p * 8;
    unsigned xa[3][2], wa[3][2];
#pragma unroll
    for (int k = 0; k < 3; ++k)
#pragma unroll
      for (int c = 0; c < 2; ++c) { xa[k][c] = opq((xa0 ^ (c << 6)) + k * 4 * UNIT); wa[k][c] = opq((wa0 ^ (c << 5)) + k * 4 * UNIT); }

    f32x4 acc[2][2][4];
    bf16x8 xf[8][2][2];
    Frag wf[4][2];
    auto ldx = [&](int s, int b, int kk) { const int ua = uaddr(2 * s); xf[s][b][kk] = lds_read16(xa[ua >> 16][kk] + (ua & 65535) + b * 2048); };
    auto ldw = [&](int G, int kk, int u = 2) {
      const int st = G >> 2, a = ((G & 1) << 1) | ((G >> 1) & 1), ua = uaddr(st < 8 ? 2 * st + 1 : 8 + st);
      const unsigned ad = wa[ua >> 16][a & 1] + (ua & 65535) + (a >> 1) * 8192 + kk * 4096;
      if (u != 1) wf[G & 3][kk].lo = tr_read(ad);
      if (u != 0) wf[G & 3][kk].hi = tr_read(ad + 512);
    };
    const unsigned sh = ((mi * 8 + wm * 2) * 56 + half * 28 + idx * 2 + wn) * 1024;
    u32x4 ho[2];
    auto epi = [&](int ps, int b, int c) {
      const int t = c >> 1, i = c & 1;
      float g0 = __builtin_elementwise_maximum(acc[ps][b][t][2 * i], 0.f), g1 = __builtin_elementwise_maximum(acc[ps][b][t][2 * i + 1], 0.f);
      float p0 = g0 * acc[ps][b][2 + t][2 * i];
      asm volatile("" : "+v"(p0));
      ho[b][c] = pack_bf16(p0, g1 * acc[ps][b][2 + t][2 * i + 1]);
    };
    auto sth = [&](int ps, int b) { gstore16_sc1(ho[b], lane16, srdH, sh + b * (56 * 1024) + ps * (14 * 1024)); };

    auto vmem = [&](auto PP) INL {
      sfor<first_op(PP.v), first_op(PP.v + 1)>([&](auto K) INL {
        constexpr Op o = SCH.op[K.v];
        if constexpr (o.pos == PP.v) {
          constexpr bool pre = PP.v < 128 && K.v == first_op(PP.v);
          if constexpr (o.kind == K_DMA) { if constexpr (o.n < 24) issue_xw(o.n, o.i, pre); else issue_wd(o.n - 24, o.i, pre); }
          if constexpr (o.kind == K_STORE) sth(o.n, o.i);
          if constexpr (o.kind == K_FLAG) gstore8_sc1(tag, one, srdH, syncb + 128 * (1 + rank));
          if constexpr (o.kind == K_POLL) gload8_sc1(fl, fo, srdH, syncb);
          if constexpr (o.kind == K_H) issue_h(o.n, o.i);
        }
      });
    };
    wait_vm<5>();
    BAR();
    ldw(0, 0); ldx(0, 0, 0); ldw(0, 1); ldx(0, 0, 1); ldx(0, 1, 0); ldx(0, 1, 1); ldw(1, 0); ldw(1, 1); ldw(2, 0); ldw(2, 1);
    SB();
    sfor<0, 128>([&](auto Z) INL {
      constexpr int P = Z.v, ps = P >> 6, s = (P >> 3) & 7, G = P >> 1, a = ((G & 1) << 1) | ((G >> 1) & 1), b = P & 1;
      if constexpr (is_bar(P)) {
        constexpr int n = bar_wait(P);
        static_assert(n >= 0 && n < 24, "wait count");
        if constexpr (P == 10) asm volatile("s_waitcnt vmcnt(%1)" : "+v"(ep) : "i"(n) : "memory");
        else if constexpr (P == 122) {
          asm volatile("s_waitcnt vmcnt(%1)" : "+v"(fl) : "i"(n) : "memory");
          if (w == 0) { if (__builtin_expect(!flags_ok(), 0)) poll(0); }
        } else wait_vm<n>();
        BAR();
      }
      if constexpr (P < 58 ? (P & 1) == 0 : (P & 3) == 2) __builtin_amdgcn_s_setprio(P < 58 ? 3 - (((P + 6) & 7) >> 1) : 3 - (((P + 6) & 15) >> 2));
      if constexpr (s == 0) mfma_new(acc[ps][b][a], join(wf[G & 3][0]), xf[s][b][0]); else mfma_acc(acc[ps][b][a], join(wf[G & 3][0]), xf[s][b][0]);
      SB();
      if constexpr (G + 3 < 64) ldw(G + 3, b, 0);
      if constexpr (P >= 124) df[0][P - 124].lo = ldd(0, P - 124, 0);
      {
        constexpr int k0 = first_op(P);
        if constexpr (k0 < first_op(P + 1) && SCH.op[k0].kind == K_DMA && SCH.op[k0].i == 0) set_m0(mu0 + uaddr(SCH.op[k0].n));
        else if constexpr (P < 56 && (P & 7) >= 2 && (P & 7) < 6) ldx(s + 1, ((P - 2) >> 1) & 1, P & 1);
      }
      SB();
      mfma_acc(acc[ps][b][a], join(wf[G & 3][1]), xf[s][b][1]);
      SB();
      if constexpr (G + 3 < 64) ldw(G + 3, b, 1);
      if constexpr (P >= 124) df[0][P - 124].hi = ldd(0, P - 124, 1);
      {
        constexpr int k0 = first_op(P);
        if constexpr (k0 < first_op(P + 1) && SCH.op[k0].kind == K_DMA && SCH.op[k0].i == 0 && P < 56 && (P & 7) >= 2 && (P & 7) < 6) ldx(s + 1, ((P - 2) >> 1) & 1, P & 1);
      }
      if constexpr (P == 120) set_db(1);
      if constexpr ((P & 63) == 62 || (P & 63) == 63) { epi(ps, P & 1, 0); epi(ps, P & 1, 1); }
      if constexpr (P == 65 || P == 66) { epi(0, P - 65, 2); epi(0, P - 65, 3); }
      if constexpr (P == 116) set_db(0);
      if constexpr (P == 118) set_db(2);
      if constexpr (P == 84) mktag();
      vmem(Z);
      SB();
    });
    epi(1, 0, 2); epi(1, 0, 3);
    sth(1, 0);
    SB();
    epi(1, 1, 2); epi(1, 1, 3);
    sth(1, 1);
    issue_h(2, 0); issue_h(2, 1);
    __builtin_amdgcn_s_setprio(0);
  } else {
    gload8_sc1(ep, 0u, srdH, syncb);
    set_db(0); set_db(1); set_db(2);
    {
      unsigned t = 0;
      const unsigned rotx = ((xcd & 6) + 4 * half) & 7;
      {
        const unsigned li = (ml * 4 + (w & 3)) * 56 + lane;
        const unsigned row = li / 7, c = li - row * 7;
        const unsigned vo = lane < 56 ? (rotx * 64 + row) * 3584 + (half * 14 + 7 + c) * 128 : 0x40000000u;
        asm volatile("buffer_load_dword %0, %1, %2, 0 offen" : "+v"(t) : "v"(vo), "s"(srdW) : "memory");
      }
#pragma unroll
      for (int i = 0; i < 3; ++i) {
        unsigned c = i + xcd; c = c >= 7 ? c - 7 : c; c = c >= 7 ? c - 7 : c;
        asm volatile("buffer_load_dword %0, %1, %2, %3 offen" : "+v"(t) : "v"(lane * 128), "s"(srdD), "s"(((c * 4 + ml) * 8 + w) * 8192) : "memory");
      }
      asm volatile("s_waitcnt vmcnt(0)" : "+v"(t), "+v"(ep)::"memory");
      mktag();
    }
#pragma unroll
    for (int s = 0; s < 7; ++s) { issue_wd(s, 0); issue_wd(s, 1); }
    if (w == 0) poll_slow(0); else wait_vm<0>();
    BAR();
#pragma unroll
    for (int s = 0; s < 3; ++s) { issue_h(s, 0); issue_h(s, 1); }
#pragma unroll
    for (int a = 0; a < 4; ++a) { df[0][a].lo = ldd(0, a, 0); df[0][a].hi = ldd(0, a, 1); }
    wait_vm<0>();
  }
  asm volatile("" : "+s"(mu0), "+s"(sd0), "+s"(sh2), "+s"(syncb), "+s"(rank));
  asm volatile("" : "+s"(kb[0]), "+s"(kb[1]), "+s"(kb[2]), "+s"(kb[3]), "+s"(kb[4]), "+s"(kb[5]), "+s"(kb[6]));
  fmask = rank == 7 ? 0xFFFFu : 0x7F7Fu;
  f32x4 acc2[2][4];
  const unsigned pa = opq(lds0 + ((kq + 8) % 10) * UNIT + (mb * 32 + (lane & 15)) * 256 + ((g ^ (lane & 7)) << 4));
  const unsigned pa1 = opq(pa ^ 64);
  const unsigned ra = opq(lds0 + (w * 8 + (lane >> 3)) * 256 + (((2 * (lane & 7)) ^ (lane >> 3)) << 4)), rb = opq(ra ^ 16);
  const unsigned ra8 = opq(ra + 8 * UNIT), rb8 = opq(rb + 8 * UNIT);
  const unsigned vo = opq((w * 8 + (lane >> 3)) * 1024 + (lane & 7) * 16), so = opqs(m64 * 65536 + n64 * 128);
  const u32x4 srdO = make_srd(out, M_ * D_ * 2);
  auto wpart = [&](int a) {
    const unsigned ad = ((a & 1) ? pa1 : pa) + (a >> 1) * 128;
    *(LDS_AS f32x4*)(uintptr_t)(ad) = acc2[0][a];
    *(LDS_AS f32x4*)(uintptr_t)(ad + 16 * 256) = acc2[1][a];
  };
  auto vmem2 = [&](auto PP) INL {
    sfor<first_op(PP.v), first_op(PP.v + 1)>([&](auto K) INL {
      constexpr Op o = SCH.op[K.v];
      if constexpr (o.pos == PP.v) {
        if constexpr (o.kind == K_DMA) issue_wd(o.n - 24, o.i);
        if constexpr (o.kind == K_FLAG) gstore8_sc1(tag, one, srdH, syncb + 128 * (17 + rank));
        if constexpr (o.kind == K_POLL) gload8_sc1(fl, fo, srdH, syncb + 2048);
        if constexpr (o.kind == K_H) issue_h(o.n, o.i);
      }
    });
  };
  sfor<0, 14>([&](auto S) INL {
    constexpr int s = S.v;
    if constexpr (s == Y1 || s == FCHK || s == Y2 || s == Y3) {
      constexpr int n = bar_wait(Q(s, 0));
      static_assert(n >= 0 && n < 24, "wait count");
      if constexpr (s == FCHK) {
        asm volatile("s_waitcnt vmcnt(%1)" : "+v"(fl) : "i"(n) : "memory");
        if (w == 0) { if (__builtin_expect(!flags_ok(), 0)) poll(2048); }
      } else wait_vm<n>();
      BAR();
      if constexpr (s == Y3) { if (rank == 7 && w == 0) gstore8_sc1(tag, one, srdH, syncb); }
    }
    vmem2(IC<Q(s, 0)>{});
    if constexpr (s == Y1) asm volatile("" : "+v"(hf[s][0]), "+v"(hf[s][1])::"memory");
    else asm volatile("s_waitcnt vmcnt(%2)" : "+v"(hf[s][0]), "+v"(hf[s][1]) : "i"(younger_than(K_H, s, 1, Q(s, 1))) : "memory");
    SB();
    sfor<0, 4>([&](auto A) INL {
      constexpr int a = A.v;
      if constexpr (s == 0) mfma_new(acc2[0][a], join(df[0][a]), hf[0][0]); else mfma_acc(acc2[0][a], join(df[s & 1][a]), hf[s][0]);
      SB();
      if constexpr (s + 1 < 14) df[(s + 1) & 1][a].lo = ldd(s + 1, a, 0);
      SB();
      if constexpr (s == 0) mfma_new(acc2[1][a], join(df[0][a]), hf[0][1]); else mfma_acc(acc2[1][a], join(df[s & 1][a]), hf[s][1]);
      SB();
      if constexpr (s + 1 < 14) df[(s + 1) & 1][a].hi = ldd(s + 1, a, 1);
      vmem2(IC<Q(s, a + 1)>{});
      if constexpr (s == 13 && a == 3) { asm volatile("s_nop 1" ::: "memory"); SB(); wpart(0); SB(); wpart(1); }
      SB();
    });
  });
  wpart(2);
  SB();
  asm volatile("s_nop 1" ::: "memory");
  SB();
  wpart(3);
  wait_lgkm0();
  BAR();
  {
    f32x4 p0[4], p1[4];
#pragma unroll
    for (int v = 0; v < 4; ++v) {
      p0[v] = *(LDS_AS f32x4*)(uintptr_t)((v < 2 ? ra8 : ra) + (v & 1) * UNIT);
      p1[v] = *(LDS_AS f32x4*)(uintptr_t)((v < 2 ? rb8 : rb) + (v & 1) * UNIT);
    }
    SB();
    const f32x4 s0 = (p0[0] + p0[1]) + (p0[2] + p0[3]), s1 = (p1[0] + p1[1]) + (p1[2] + p1[3]);
    u32x4 o = {pack_bf16(s0[0], s0[1]), pack_bf16(s0[2], s0[3]), pack_bf16(s1[0], s1[1]), pack_bf16(s1[2], s1[3])};
    asm volatile("buffer_store_dwordx4 %0, %1, %2, %3 offen" ::"v"(o), "v"(vo), "s"(srdO), "s"(so) : "memory");
  }
}

extern "C" void kernel_launch(void* const* d_in, const int* in_sizes, int n_in, void* d_out, int out_size,
                              void* d_ws, size_t ws_size, hipStream_t stream) {
  (void)in_sizes; (void)n_in; (void)out_size; (void)ws_size;
  (void)hipFuncSetAttribute((const void*)k_fused, hipFuncAttributeMaxDynamicSharedMemorySize, LDS_TOTAL);
  k_fused<<<256, 512, LDS_TOTAL, stream>>>(d_in[0], d_in[1], d_in[2], d_in[3], d_out, d_ws);
}
```

```cpp
#include <hip/hip_runtime.h>
#include <hip/hip_bf16.h>
#include <stdint.h>

typedef __attribute__((ext_vector_type(8))) __bf16 bf16x8;
typedef __attribute__((ext_vector_type(4))) short s16x4;
typedef __attribute__((ext_vector_type(8))) short s16x8;
typedef __attribute__((ext_vector_type(2))) __bf16 bf16x2;
typedef __attribute__((ext_vector_type(2))) float f32x2;
typedef __attribute__((ext_vector_type(4))) float f32x4;
typedef __attribute__((ext_vector_type(4))) unsigned u32x4;
typedef __attribute__((ext_vector_type(2))) unsigned u32x2;
#define LDS_AS __attribute__((address_space(3)))
#define ST_DECL
#define ST(i)
#define ST_FLUSH(a,b)

#define RA 1
#define RB 1
#define KEEPX 1
#define SWAP 1
#define T3A 0
#define T3B 0
#define NPF 3
constexpr int M_ = 2048, D_ = 512, F_ = 1792;
constexpr unsigned SYNC_OFF = 8u << 20;
constexpr unsigned SPIN_CAP = 1u << 17;
constexpr unsigned CODE_BYTES = 12544;

__device__ __forceinline__ unsigned rfl(unsigned v) { return __builtin_amdgcn_readfirstlane(v); }

__device__ __forceinline__ u32x4 make_srd(const void* p, unsigned bytes) {
  uint64_t a = (uint64_t)p;
  u32x4 r;
  r[0] = rfl((unsigned)a);
  r[1] = rfl((unsigned)(a >> 32)) & 0xffffu;
  r[2] = bytes;
  r[3] = 0x00020000u;
  return r;
}

template <int IMM>
__device__ __forceinline__ void dma16(unsigned m0v, unsigned voff, u32x4 srd, unsigned soff) {
  asm volatile("s_mov_b32 m0, %0\n\ts_nop 0\n\tbuffer_load_dwordx4 %1, %2, %3 offen offset:%4 lds"
               ::"s"(m0v), "v"(voff), "s"(srd), "s"(soff), "i"(IMM) : "memory");
}
template <int IMM>
__device__ __forceinline__ void dma16n(unsigned voff, u32x4 srd, unsigned soff) {
  asm volatile("buffer_load_dwordx4 %0, %1, %2 offen offset:%3 lds" ::"v"(voff), "s"(srd), "s"(soff), "i"(IMM) : "memory");
}
__device__ __forceinline__ void set_m0(unsigned v) { asm volatile("s_mov_b32 m0, %0" ::"s"(v) : "memory"); }
__device__ __forceinline__ unsigned opq(unsigned v) { asm volatile("" : "+v"(v)); return v; }
__device__ __forceinline__ unsigned opqs(unsigned v) { asm volatile("" : "+s"(v)); return v; }
__device__ __forceinline__ void gload16_sc1(u32x4& dst, unsigned voff, u32x4 srd, unsigned soff) {
  asm volatile("buffer_load_dwordx4 %0, %1, %2, %3 offen sc1" : "=v"(dst) : "v"(voff), "s"(srd), "s"(soff) : "memory");
}
__device__ __forceinline__ void gstore16_sc1(u32x4 v, unsigned voff, u32x4 srd, unsigned soff) {
  asm volatile("buffer_store_dwordx4 %0, %1, %2, %3 offen sc1\n\ts_nop 1" ::"v"(v), "v"(voff), "s"(srd), "s"(soff) : "memory");
}
__device__ __forceinline__ void gload8_sc1(u32x2& dst, unsigned voff, u32x4 srd, unsigned soff) {
  asm volatile("buffer_load_dwordx2 %0, %1, %2, %3 offen sc1" : "=v"(dst) : "v"(voff), "s"(srd), "s"(soff) : "memory");
}
__device__ __forceinline__ void gstore8_sc1(u32x2 v, unsigned voff, u32x4 srd, unsigned soff) {
  asm volatile("buffer_store_dwordx2 %0, %1, %2, %3 offen sc1" ::"v"(v), "v"(voff), "s"(srd), "s"(soff) : "memory");
}

__device__ __forceinline__ s16x4 tr_read(unsigned addr) {
  return __builtin_amdgcn_ds_read_tr16_b64_v4i16((LDS_AS s16x4*)(uintptr_t)addr);
}
struct Frag { s16x4 lo, hi; };
__device__ __forceinline__ bf16x8 join(const Frag& f) {
  s16x8 v = __builtin_shufflevector(f.lo, f.hi, 0, 1, 2, 3, 4, 5, 6, 7);
  return __builtin_bit_cast(bf16x8, v);
}
__device__ __forceinline__ bf16x8 lds_read16(unsigned addr) { return *(LDS_AS bf16x8*)(uintptr_t)addr; }
__device__ __forceinline__ unsigned pack_bf16(float a, float b) {
  f32x2 v = {a, b};
  bf16x2 r = __builtin_convertvector(v, bf16x2);
  return __builtin_bit_cast(unsigned, r);
}
template <class TB>
__device__ __forceinline__ void mfma_acc(f32x4& c, bf16x8 a, TB b) {
  asm volatile("v_mfma_f32_16x16x32_bf16 %0, %1, %2, %0" : "+v"(c) : "v"(a), "v"(b));
}
template <class TB>
__device__ __forceinline__ void mfma_new(f32x4& c, bf16x8 a, TB b) {
  asm volatile("v_mfma_f32_16x16x32_bf16 %0, %1, %2, 0" : "=&v"(c) : "v"(a), "v"(b));
}

template <int I> struct IC { static constexpr int v = I; };
template <int I, int N, class F> __device__ __forceinline__ void sfor(F&& f) {
  if constexpr (I < N) { f(IC<I>{}); sfor<I + 1, N>(f); }
}
template <int N> __device__ __forceinline__ void wait_vm() { asm volatile("s_waitcnt vmcnt(%0)" ::"i"(N) : "memory"); }
__device__ __forceinline__ void wait_lgkm0() { asm volatile("s_waitcnt lgkmcnt(0)" ::: "memory"); }
#define BAR() asm volatile("s_barrier" ::: "memory")
#define SB() __builtin_amdgcn_sched_barrier(0)
#define INL __attribute__((always_inline))

constexpr int UNIT = 16384, LDS_TOTAL = 10 * UNIT;
constexpr int uaddr(int n) { return (n % 10) * UNIT; }

enum { K_DMA = 1, K_STORE, K_FLAG, K_POLL, K_H };
struct Op { int pos, kind, n, i; };
struct Sched { int cnt; Op op[200]; };
constexpr void put(Sched& s, int pos, int kind, int n, int i) { s.op[s.cnt++] = Op{pos, kind, n, i}; }
constexpr void spread(Sched& s, int p0, int p1, int u0, int nu) {
  for (int k = 0; k < 2 * nu; ++k) put(s, p0 + (k * (p1 - p0)) / (2 * nu), K_DMA, u0 + (k >> 1), k & 1);
}
constexpr int Q(int step, int slot) { return 200 + 8 * step + slot; }
constexpr void pair(Sched& s, int step, int slot, int kind, int n) { put(s, Q(step, slot), kind, n, 0); put(s, Q(step, slot == 0 ? 0 : slot + 1), kind, n, 1); }
constexpr int HB = 3;
constexpr int Y1 = 2, Y2 = 5, FCHK = 7, Y3 = 10;
constexpr Sched make_sched() {
  Sched s{};
  for (int n = 0; n < 4; ++n) { put(s, -1, K_DMA, n, 0); put(s, -1, K_DMA, n, 1); }
  put(s, -1, K_POLL, 0, 0);
  spread(s, 0, 2, 4, 2);
  for (int t = 0; t < 7; ++t) spread(s, 8 * t + 2, 8 * t + 9, 2 * t + 6, 2);
  spread(s, 58, 64, 20, 2);
  put(s, 65, K_STORE, 0, 0);
  put(s, 66, K_STORE, 0, 1);
  spread(s, 72, 80, 22, 1);
  spread(s, 82, 89, 23, 2);
  put(s, 90, K_FLAG, 0, 0);
  spread(s, 91, 103, 25, 2);
  spread(s, 103, 111, 27, 2);
  put(s, 111, K_POLL, 1, 0);
  spread(s, 112, 121, 29, 2);
  for (int k = 0; k < 4; ++k) put(s, 122 + k, K_H, k >> 1, k & 1);
  put(s, 128, K_STORE, 1, 0);
  put(s, 128, K_STORE, 1, 1);
  put(s, 128, K_H, 2, 0);
  put(s, 128, K_H, 2, 1);
  put(s, Q(Y1, 0), K_FLAG, 1, 0);
  pair(s, 2, 0, K_H, 3); pair(s, 2, 0, K_H, 4);
  pair(s, 2, 3, K_DMA, 31);
  pair(s, 3, 1, K_H, 5); pair(s, 3, 3, K_H, 6);
  pair(s, 4, 1, K_DMA, 32); pair(s, 4, 3, K_DMA, 33);
  pair(s, 5, 1, K_DMA, 34); pair(s, 5, 3, K_DMA, 35);
  put(s, Q(6, 0), K_POLL, 2, 0);
  pair(s, 6, 1, K_DMA, 36); pair(s, 6, 3, K_DMA, 37);
  for (int k = 0; k < HB; ++k) pair(s, 7, 0, K_H, 7 + k);
  for (int k = 7 + HB; k < 14; ++k) pair(s, k - HB, 1, K_H, k);
  return s;
}
constexpr Sched SCH = make_sched();
constexpr int younger_than(int kind, int n, int i, int P) {
  int c = -1;
  for (int k = 0; k < SCH.cnt; ++k) {
    if (SCH.op[k].pos >= P) break;
    if (SCH.op[k].kind == kind && SCH.op[k].n == n && SCH.op[k].i == i) c = 0;
    else if (c >= 0 && SCH.op[k].kind != K_FLAG && !(SCH.op[k].kind == K_POLL && SCH.op[k].n > 0)) ++c;
  }
  return c;
}
constexpr int first_op(int P) { int k = 0; while (k < SCH.cnt && SCH.op[k].pos < P) ++k; return k; }
constexpr bool is_bar(int P) { return P < 64 ? (P & 7) == 2 : (P & 15) == 10; }
constexpr int bar_wait(int P) {
  if (P < 64) return younger_than(K_DMA, 2 * (P >> 3) + 3, 1, P);
  if (P == 74) return younger_than(K_DMA, 19, 1, P);
  if (P == 90) return younger_than(K_STORE, 0, 1, P);
  if (P == 106) return younger_than(K_DMA, 23, 1, P);
  if (P == 122) return younger_than(K_POLL, 1, 0, P);
  if (P == Q(Y1, 0)) return 0;
  if (P == Q(Y2, 0)) return younger_than(K_DMA, 31, 1, P);
  if (P == Q(FCHK, 0)) return younger_than(K_POLL, 2, 0, P);
  return younger_than(K_DMA, 37, 1, P);
}
constexpr int last_use(int n) { return n < 16 ? 8 * (n >> 1) + 7 : (n < 24 ? 8 * (n - 8) + 7 : Q(n - 24, 4)); }
constexpr int first_read(int n) { return n < 16 ? 8 * (n >> 1) - 6 : (n < 24 ? 8 * (n - 8) - 6 : (n == 24 ? 124 : Q(n - 25, 0))); }
constexpr bool any_bar(int P) { return P < 128 ? is_bar(P) : (P == Q(Y1, 0) || P == Q(Y2, 0) || P == Q(FCHK, 0) || P == Q(Y3, 0)); }
constexpr bool ring_ok() {
  for (int k = 0; k < SCH.cnt; ++k) {
    if (SCH.op[k].kind != K_DMA || SCH.op[k].n < 10) continue;
    bool ok = false;
    for (int P = last_use(SCH.op[k].n - 10) + 1; P <= SCH.op[k].pos; ++P) ok = ok || any_bar(P);
    if (!ok) return false;
  }
  for (int n = 2; n < 38; ++n) {
    bool ok = false;
    for (int P = 0; P <= first_read(n); ++P) ok = ok || (any_bar(P) && younger_than(K_DMA, n, 1, P) >= bar_wait(P));
    if (!ok) return false;
  }
  return true;
}
static_assert(ring_ok(), "LDS ring schedule");
constexpr bool sched_sorted() { for (int k = 1; k < SCH.cnt; ++k) if (SCH.op[k].pos < SCH.op[k - 1].pos) return false; return true; }
static_assert(sched_sorted(), "schedule order");

__global__ __launch_bounds__(512, 2) void k_fused(const void* __restrict__ x, const void* __restrict__ Wg,
                                                  const void* __restrict__ Wu, const void* __restrict__ Wd,
                                                  void* __restrict__ out, void* __restrict__ ws) {
  extern __shared__ __attribute__((aligned(16))) char smem[];
  const unsigned lds0 = (unsigned)(uintptr_t)(LDS_AS char*)smem;
  ST_DECL ST(0)
  const unsigned bid = blockIdx.x;
  const unsigned w = rfl(threadIdx.x >> 6);
  const bool busy = bid < 224;
  uint64_t pc;
  asm volatile("s_getpc_b64 %0" : "=s"(pc));
  u32x2 px, pw;
  asm volatile("s_load_dwordx2 %0, %2, 0x0\n\ts_load_dwordx2 %1, %2, %3"
               : "=&s"(px), "=&s"(pw) : "s"(__builtin_amdgcn_kernarg_segment_ptr()), "s"(8 + 8 * (w >> 2)));
  SB();
  const unsigned mi = (bid & 6) * 2 + ((bid >> 3) & 3), half = bid & 1, idx = bid >> 5;
  const unsigned rot = ((bid & 6) + 4 * half + ((bid >> 3) & RA) + (idx & RB)) & 7;
  unsigned vw0, vw1, vx0, vx1;
  {
    const unsigned lane = threadIdx.x & 63, g = lane >> 4;
    vw0 = (lane >> 3) * 3584 + (((lane & 7) ^ ((g & 1) << 1)) << 4);
    vw1 = (vw0 ^ 64) + 8 * 3584 - 1024;
    vx0 = (lane >> 3) * 1024 + (((lane & 7) ^ g) << 4);
    vx1 = (vx0 ^ 64) + 8192 - 1024;
  }
  unsigned sx0 = (mi * 128 + 16 * w) * 1024;
  unsigned sw0 = (16 * (w & 3)) * 3584 + (half * 14 + idx) * 128;
  unsigned mu0 = lds0 + w * 2048;
  unsigned sw00 = sw0 + rot * (64 * 3584), sx00 = sx0 + rot * 128;
  asm volatile("" : "+v"(vw0), "+v"(vw1), "+v"(vx0), "+v"(vx1));
  asm volatile("" : "+s"(sx0), "+s"(sw0), "+s"(mu0), "+s"(sw00), "+s"(sx00));
  SB();
  asm volatile("s_waitcnt lgkmcnt(0)" : "+s"(px), "+s"(pw));
  const u32x4 srdX = {px[0], px[1] & 0xffffu, M_ * D_ * 2, 0x00020000u};
  const u32x4 srdW = {pw[0], pw[1] & 0xffffu, D_ * F_ * 2, 0x00020000u};
  auto issue_xw = [&](int n, int i, bool pre = false) {
    const int s = n < 16 ? n >> 1 : n - 16;
    const unsigned sg = (s + rot) & 7;
    if (n < 16 && (n & 1) == 0) {
      const unsigned so = n == 0 ? sx00 : sx0 + sg * 128;
      if (i == 0) { if (pre) dma16n<0>(vx0, srdX, so); else dma16<0>(mu0 + uaddr(n), vx0, srdX, so); } else dma16n<1024>(vx1, srdX, so);
    } else {
      const unsigned so = n == 1 ? sw00 : sw0 + sg * (64 * 3584) + (n >= 16 ? 896 : 0);
      if (i == 0) { if (pre) dma16n<0>(vw0, srdW, so); else dma16<0>(mu0 + uaddr(n), vw0, srdW, so); } else dma16n<1024>(vw1, srdW, so);
    }
  };
  if (busy) {
    issue_xw(0, 0); issue_xw(0, 1); issue_xw(1, 0); issue_xw(1, 1);
    BAR();
    issue_xw(2, 0); issue_xw(2, 1); issue_xw(3, 0); issue_xw(3, 1);
  } else {
    const unsigned o = min(threadIdx.x * 64u, CODE_BYTES);
    asm volatile("global_load_dword v249, %0, %1" ::"v"(o), "s"(pc) : "v249", "memory");
  }
  SB();
  const unsigned tid = opq(threadIdx.x), lane = tid & 63;
  const unsigned bid2 = opqs(bid), xcd = bid2 & 7, ml = (bid2 >> 3) & 3;
  unsigned rank = half * 8 + idx;
  const unsigned m64 = 2 * mi + half, n64 = SWAP ? (((idx & 3) == 3 ? idx ^ 4 : idx) - (rfl((unsigned)(uintptr_t)Wd) >> 7)) & 7 : idx;
  const unsigned g = lane >> 4, q = (lane & 15) >> 2, p = lane & 3;
  const u32x4 srdH = make_srd(ws, SYNC_OFF + 16 * 8192);
  const u32x4 srdD = make_srd(Wd, F_ * D_ * 2);
  unsigned syncb = SYNC_OFF + mi * 8192;
  const unsigned lane16 = lane * 16;

  const unsigned kq = w >> 1, mb = w & 1;
  unsigned rot2 = 2 * ml + idx; rot2 = rot2 >= 7 ? rot2 - 7 : rot2;
  unsigned kb[7];
  auto set_kb = [&]() {
    unsigned e = opqs(rot2);
#pragma unroll
    for (int j = 0; j < 7; ++j) {
      kb[j] = opqs((e + 7 * kq + (kq >> 1) * 14) * 1024);
      e = e == 6 ? 0 : e + 1;
    }
  };
  auto kap = [&](int s) { return kb[s % 7] + (s >= 7 ? 14 * 1024 : 0); };
  const unsigned vd0 = (lane >> 3) * 1024 + (((lane & 7) ^ (g << 1)) << 4);
  const unsigned vd1 = vd0 + 8192 - 1024;
  unsigned sd0 = (16 * mb) * 1024 + n64 * 128;
  auto issue_wd = [&](int s, int i, bool pre = false) {
    if (i == 0) { if (pre) dma16n<0>(vd0, srdD, sd0 + kap(s) * 32); else dma16<0>(mu0 + uaddr(24 + s), vd0, srdD, sd0 + kap(s) * 32); }
    else dma16n<1024>(vd1, srdD, sd0 + kap(s) * 32);
  };
  const unsigned dl = lds0 + kq * 4096 + (4 * g + q) * 128 + (((2 * g + (q >> 1)) & 3) << 5) + p * 8;
  unsigned db[3][4];
  auto set_db = [&](int k) {
#pragma unroll
    for (int c = 0; c < 4; ++c) db[k][c] = opq((dl ^ (c << 5)) + k * 4 * UNIT);
  };
  auto ldd = [&](int s, int a, int u) {
    const int ua = uaddr(24 + s);
    return tr_read(db[ua >> 16][a] + (ua & 65535) + u * 2048);
  };
  u32x2 ep, tag, fl;
  auto mktag = [&]() {
    const uint64_t t = (((uint64_t)ep[1] << 32) | ep[0]) * 0x9E3779B97F4A7C15ull + 1ull;
    tag = u32x2{(unsigned)t, (unsigned)(t >> 32)};
  };
  const unsigned one = tid == 0 ? 0u : 0x40000000u;
  const unsigned fo = tid < 16 ? 128 * (1 + tid) : 0x40000000u;
  unsigned fmask = 0x7F7Fu;
  auto flags_ok = [&]() { return (rfl((unsigned)__builtin_amdgcn_ballot_w64(fl[0] == tag[0] && fl[1] == tag[1])) & fmask) == fmask; };
  auto poll = [&](unsigned off) {
    unsigned cnt = SPIN_CAP, t;
#define PLD(r) "buffer_load_dwordx2 " r ", %2, %3, %4 offen sc1\n\t"
#define PCK(r) "s_waitcnt vmcnt(2)\n\tv_cmp_eq_u64 vcc, " r ", %5\n\ts_nop 1\n\ts_and_b32 %1, vcc_lo, %6\n\ts_cmp_eq_u32 %1, %6\n\ts_cbranch_scc1 2f\n\t"
    asm volatile(PLD("v[250:251]") "s_sleep 3\n\t" PLD("v[252:253]") "s_sleep 3\n\t" PLD("v[254:255]")
                 "1:\n\t"
                 PCK("v[250:251]") PLD("v[250:251]") PCK("v[252:253]") PLD("v[252:253]") PCK("v[254:255]") PLD("v[254:255]")
                 "s_sub_u32 %0, %0, 1\n\ts_cbranch_scc0 1b\n\t"
                 "2:"
                 : "+s"(cnt), "=&s"(t) : "v"(fo), "s"(srdH), "s"(syncb + off), "v"(tag), "s"(fmask)
                 : "v250", "v251", "v252", "v253", "v254", "v255", "vcc", "scc", "memory");
#undef PLD
#undef PCK
  };
  auto poll_slow = [&](unsigned off) {
#pragma nounroll
    for (unsigned spin = 0; spin < SPIN_CAP; ++spin) {
      gload8_sc1(fl, fo, srdH, syncb + off);
      asm volatile("s_waitcnt vmcnt(0)" : "+v"(fl)::"memory");
      if (flags_ok()) break;
      __builtin_amdgcn_s_sleep(8);
    }
  };
  Frag df[2][4];
  u32x4 hf[14][2];
  unsigned sh2 = ((m64 * 4 + mb * 2) * 56) * 1024;
  const unsigned lane16b = lane16 + 56 * 1024;
  auto issue_h = [&](int s, int b) { gload16_sc1(hf[s][b], b ? lane16b : lane16, srdH, sh2 + kap(s)); };

  set_kb();
  if (__builtin_expect(busy, 1)) {
    gload8_sc1(ep, 0u, srdH, syncb);
    const unsigned wm = w & 3, wn = w >> 2;
    const unsigned xrow = 32 * wm + (lane & 15);
    const unsigned xa0 = lds0 + xrow * 128 + ((g ^ ((xrow >> 1) & 7)) << 4);
    const unsigned wa0 = lds0 + (8 * g + q) * 128 + (((2 * wn) ^ ((q >> 1) | ((g & 1) << 1))) << 5) + p * 8;
    unsigned xa[3][2], wa[3][2];
#pragma unroll
    for (int k = 0; k < 3; ++k)
#pragma unroll
      for (int c = 0; c < 2; ++c) { xa[k][c] = opq((xa0 ^ (c << 6)) + k * 4 * UNIT); wa[k][c] = opq((wa0 ^ (c << 5)) + k * 4 * UNIT); }
    ST(1)

    f32x4 acc[2][2][4];
    bf16x8 xf[8][2][2];
    Frag wf[4][2];
    auto ldx = [&](int s, int b, int kk) { const int ua = uaddr(2 * s); xf[s][b][kk] = lds_read16(xa[ua >> 16][kk] + (ua & 65535) + b * 2048); };
    auto ldw = [&](int G, int kk, int u = 2) {
      const int st = G >> 2, a = ((G & 1) << 1) | ((G >> 1) & 1), ua = uaddr(st < 8 ? 2 * st + 1 : 8 + st);
      const unsigned ad = wa[ua >> 16][a & 1] + (ua & 65535) + (a >> 1) * 8192 + kk * 4096;
      if (u != 1) wf[G & 3][kk].lo = tr_read(ad);
      if (u != 0) wf[G & 3][kk].hi = tr_read(ad + 512);
    };
    const unsigned sh = ((mi * 8 + wm * 2) * 56 + half * 28 + idx * 2 + wn) * 1024;
    u32x4 ho[2];
    auto epi = [&](int ps, int b, int c) {
      const int t = c >> 1, i = c & 1;
      float g0 = __builtin_elementwise_maximum(acc[ps][b][t][2 * i], 0.f), g1 = __builtin_elementwise_maximum(acc[ps][b][t][2 * i + 1], 0.f);
      float p0 = g0 * acc[ps][b][2 + t][2 * i];
      asm volatile("" : "+v"(p0));
      ho[b][c] = pack_bf16(p0, g1 * acc[ps][b][2 + t][2 * i + 1]);
    };
    auto sth = [&](int ps, int b) { gstore16_sc1(ho[b], lane16, srdH, sh + b * (56 * 1024) + ps * (14 * 1024)); };

    auto vmem = [&](auto PP) INL {
      sfor<first_op(PP.v), first_op(PP.v + 1)>([&](auto K) INL {
        constexpr Op o = SCH.op[K.v];
        if constexpr (o.pos == PP.v) {
          constexpr bool pre = PP.v < 128 && K.v == first_op(PP.v);
          if constexpr (o.kind == K_DMA) { if constexpr (o.n < 24) issue_xw(o.n, o.i, pre); else issue_wd(o.n - 24, o.i, pre); }
          if constexpr (o.kind == K_STORE) sth(o.n, o.i);
          if constexpr (o.kind == K_FLAG) gstore8_sc1(tag, one, srdH, syncb + 128 * (1 + rank));
          if constexpr (o.kind == K_POLL) gload8_sc1(fl, fo, srdH, syncb);
          if constexpr (o.kind == K_H) issue_h(o.n, o.i);
        }
      });
    };
    wait_vm<5>();
    ST(2)
    BAR();
    ldw(0, 0); ldx(0, 0, 0); ldw(0, 1); ldx(0, 0, 1); ldx(0, 1, 0); ldx(0, 1, 1); ldw(1, 0); ldw(1, 1); ldw(2, 0); ldw(2, 1);
    SB();
    sfor<0, 128>([&](auto Z) INL {
      constexpr int P = Z.v, ps = P >> 6, s = (P >> 3) & 7, G = P >> 1, a = ((G & 1) << 1) | ((G >> 1) & 1), b = P & 1;
      if constexpr (is_bar(P)) {
        constexpr int n = bar_wait(P);
        static_assert(n >= 0 && n < 24, "wait count");
        if constexpr (P == 10) asm volatile("s_waitcnt vmcnt(%1)" : "+v"(ep) : "i"(n) : "memory");
        else if constexpr (P == 122) {
          asm volatile("s_waitcnt vmcnt(%1)" : "+v"(fl) : "i"(n) : "memory");
          if (w == 0) { if (__builtin_expect(!flags_ok(), 0)) poll(0); }
        } else wait_vm<n>();
        BAR();
        if (P == 2) { ST(3) } if (P == 26) { ST(4) } if (P == 58) { ST(5) } if (P == 74) { ST(6) } if (P == 90) { ST(7) } if (P == 106) { ST(8) } if (P == 122) { ST(9) }
      }
      if constexpr (P < 58 ? (P & 1) == 0 : (P & 3) == 2) __builtin_amdgcn_s_setprio(P < 58 ? 3 - (((P + 6) & 7) >> 1) : 3 - (((P + 6) & 15) >> 2));
      if constexpr (s == 0) mfma_new(acc[ps][b][a], join(wf[G & 3][0]), xf[s][b][0]); else mfma_acc(acc[ps][b][a], join(wf[G & 3][0]), xf[s][b][0]);
      SB();
      if constexpr (G + 3 < 64) ldw(G + 3, b, 0);
      if constexpr (P >= 124) df[0][P - 124].lo = ldd(0, P - 124, 0);
      {
        constexpr int k0 = first_op(P);
        if constexpr (k0 < first_op(P + 1) && SCH.op[k0].kind == K_DMA && SCH.op[k0].i == 0) set_m0(mu0 + uaddr(SCH.op[k0].n));
        else if constexpr (P < 56 && (P & 7) >= 2 && (P & 7) < 6) ldx(s + 1, ((P - 2) >> 1) & 1, P & 1);
      }
      SB();
      mfma_acc(acc[ps][b][a], join(wf[G & 3][1]), xf[s][b][1]);
      SB();
      if constexpr (G + 3 < 64) ldw(G + 3, b, 1);
      if constexpr (P >= 124) df[0][P - 124].hi = ldd(0, P - 124, 1);
      {
        constexpr int k0 = first_op(P);
        if constexpr (k0 < first_op(P + 1) && SCH.op[k0].kind == K_DMA && SCH.op[k0].i == 0 && P < 56 && (P & 7) >= 2 && (P & 7) < 6) ldx(s + 1, ((P - 2) >> 1) & 1, P & 1);
      }
      if constexpr (P == 120) set_db(1);
      if constexpr ((P & 63) == 62 || (P & 63) == 63) { epi(ps, P & 1, 0); epi(ps, P & 1, 1); }
      if constexpr (P == 65 || P == 66) { epi(0, P - 65, 2); epi(0, P - 65, 3); }
      if constexpr (P == 116) set_db(0);
      if constexpr (P == 118) set_db(2);
      if constexpr (P == 84) mktag();
      vmem(Z);
      SB();
    });
    ST(11)
    epi(1, 0, 2); epi(1, 0, 3);
    sth(1, 0);
    SB();
    epi(1, 1, 2); epi(1, 1, 3);
    sth(1, 1);
    issue_h(2, 0); issue_h(2, 1);
    __builtin_amdgcn_s_setprio(0);
    ST(12)
  } else {
    gload8_sc1(ep, 0u, srdH, syncb);
    set_db(0); set_db(1); set_db(2);
    {
      unsigned t = 0;
      const unsigned rotx = ((xcd & 6) + 4 * half) & 7;
      auto touch = [&](unsigned vo, u32x4 srd, unsigned so) { asm volatile("buffer_load_dword %0, %1, %2, %3 offen" : "+v"(t) : "v"(vo), "s"(srd), "s"(so) : "memory"); };
      const unsigned c3 = (3 - (pw[0] >> 7) - half * 14) & 3;
      const unsigned r3 = (((rotx + 2) * 64 + (ml * 4 + (w & 3)) * 32 + (lane >> 1)) & 511) * 3584;
      const unsigned ba = c3 + 4 * (lane & 1), bb = ((c3 + 1) & 3) + 4 * (lane & 1);
      if (KEEPX) {
        const unsigned st = (rotx + (ml & RA) + 2 + (lane & 3)) & 7;
        touch((lane & 3) < 2 ? (mi * 128 + w * 16 + (lane >> 2)) * 1024 + st * 128 : 0x40000000u, srdX, 0);
      }
      if (T3A) touch(ba < 7 ? r3 + (half * 14 + ba) * 128 : 0x40000000u, srdW, 0);
      {
        touch((lane & 7) < 7 ? (lane >> 3) * 3584 + (lane & 7) * 128 : 0x40000000u, srdW, (rotx * 64 + (ml * 4 + (w & 3)) * 8) * 3584 + (half * 14 + 7) * 128);
      }
      if (T3B == 1) touch(bb < 7 ? r3 + (half * 14 + 7 + bb) * 128 : 0x40000000u, srdW, 0);
#pragma unroll
      for (int i = 0; i < NPF; ++i) {
        unsigned c = i + xcd; c = c >= 7 ? c - 7 : c; c = c >= 7 ? c - 7 : c;
        touch(lane * 128, srdD, ((c * 4 + ml) * 8 + w) * 8192);
        if (T3B == 2 && i == 0) touch(bb < 7 ? r3 + (half * 14 + 7 + bb) * 128 : 0x40000000u, srdW, 0);
      }
      asm volatile("s_waitcnt vmcnt(0)" : "+v"(t), "+v"(ep)::"memory");
      mktag();
    }
#pragma unroll
    for (int s = 0; s < 7; ++s) { issue_wd(s, 0); issue_wd(s, 1); }
    if (w == 0) poll_slow(0); else wait_vm<0>();
    BAR();
#pragma unroll
    for (int s = 0; s < 3; ++s) { issue_h(s, 0); issue_h(s, 1); }
#pragma unroll
    for (int a = 0; a < 4; ++a) { df[0][a].lo = ldd(0, a, 0); df[0][a].hi = ldd(0, a, 1); }
    wait_vm<0>();
  }
  asm volatile("" : "+s"(mu0), "+s"(sd0), "+s"(sh2), "+s"(syncb), "+s"(rank));
  asm volatile("" : "+s"(kb[0]), "+s"(kb[1]), "+s"(kb[2]), "+s"(kb[3]), "+s"(kb[4]), "+s"(kb[5]), "+s"(kb[6]));
  fmask = rank == 7 ? 0xFFFFu : 0x7F7Fu;
  f32x4 acc2[2][4];
  const unsigned pa = opq(lds0 + ((kq + 8) % 10) * UNIT + (mb * 32 + (lane & 15)) * 256 + ((g ^ (lane & 7)) << 4));
  const unsigned pa1 = opq(pa ^ 64);
  const unsigned ra = opq(lds0 + (w * 8 + (lane >> 3)) * 256 + (((2 * (lane & 7)) ^ (lane >> 3)) << 4)), rb = opq(ra ^ 16);
  const unsigned ra8 = opq(ra + 8 * UNIT), rb8 = opq(rb + 8 * UNIT);
  const unsigned vo = opq((w * 8 + (lane >> 3)) * 1024 + (lane & 7) * 16), so = opqs(m64 * 65536 + n64 * 128);
  const u32x4 srdO = make_srd(out, M_ * D_ * 2);
  auto wpart = [&](int a) {
    const unsigned ad = ((a & 1) ? pa1 : pa) + (a >> 1) * 128;
    *(LDS_AS f32x4*)(uintptr_t)(ad) = acc2[0][a];
    *(LDS_AS f32x4*)(uintptr_t)(ad + 16 * 256) = acc2[1][a];
  };
  auto vmem2 = [&](auto PP) INL {
    sfor<first_op(PP.v), first_op(PP.v + 1)>([&](auto K) INL {
      constexpr Op o = SCH.op[K.v];
      if constexpr (o.pos == PP.v) {
        if constexpr (o.kind == K_DMA) issue_wd(o.n - 24, o.i);
        if constexpr (o.kind == K_FLAG) gstore8_sc1(tag, one, srdH, syncb + 128 * (17 + rank));
        if constexpr (o.kind == K_POLL) gload8_sc1(fl, fo, srdH, syncb + 2048);
        if constexpr (o.kind == K_H) issue_h(o.n, o.i);
      }
    });
  };
  sfor<0, 14>([&](auto S) INL {
    constexpr int s = S.v;
    if constexpr (s == Y1 || s == FCHK || s == Y2 || s == Y3) {
      constexpr int n = bar_wait(Q(s, 0));
      static_assert(n >= 0 && n < 24, "wait count");
      if constexpr (s == FCHK) {
        asm volatile("s_waitcnt vmcnt(%1)" : "+v"(fl) : "i"(n) : "memory");
        if (w == 0) { if (__builtin_expect(!flags_ok(), 0)) poll(2048); }
      } else wait_vm<n>();
      BAR();
      if (s == Y1) { ST(13) } if (s == FCHK) { ST(14) } if (s == Y2) { ST(15) } if (s == Y3) { ST(16) }
      if constexpr (s == Y3) { if (rank == 7 && w == 0) gstore8_sc1(tag, one, srdH, syncb); }
    }
    vmem2(IC<Q(s, 0)>{});
    if constexpr (s == Y1) asm volatile("" : "+v"(hf[s][0]), "+v"(hf[s][1])::"memory");
    else asm volatile("s_waitcnt vmcnt(%2)" : "+v"(hf[s][0]), "+v"(hf[s][1]) : "i"(younger_than(K_H, s, 1, Q(s, 1))) : "memory");
    if (s == 0) { ST(17) }
    SB();
    sfor<0, 4>([&](auto A) INL {
      constexpr int a = A.v;
      if constexpr (s == 0) mfma_new(acc2[0][a], join(df[0][a]), hf[0][0]); else mfma_acc(acc2[0][a], join(df[s & 1][a]), hf[s][0]);
      SB();
      if constexpr (s + 1 < 14) df[(s + 1) & 1][a].lo = ldd(s + 1, a, 0);
      SB();
      if constexpr (s == 0) mfma_new(acc2[1][a], join(df[0][a]), hf[0][1]); else mfma_acc(acc2[1][a], join(df[s & 1][a]), hf[s][1]);
      SB();
      if constexpr (s + 1 < 14) df[(s + 1) & 1][a].hi = ldd(s + 1, a, 1);
      vmem2(IC<Q(s, a + 1)>{});
      if constexpr (s == 13 && a == 3) { asm volatile("s_nop 1" ::: "memory"); SB(); wpart(0); SB(); wpart(1); }
      SB();
    });
  });
  ST(18)
  wpart(2);
  SB();
  asm volatile("s_nop 1" ::: "memory");
  SB();
  wpart(3);
  wait_lgkm0();
  BAR();
  ST(19)
  {
    f32x4 p0[4], p1[4];
#pragma unroll
    for (int v = 0; v < 4; ++v) {
      p0[v] = *(LDS_AS f32x4*)(uintptr_t)((v < 2 ? ra8 : ra) + (v & 1) * UNIT);
      p1[v] = *(LDS_AS f32x4*)(uintptr_t)((v < 2 ? rb8 : rb) + (v & 1) * UNIT);
    }
    SB();
    const f32x4 s0 = (p0[0] + p0[1]) + (p0[2] + p0[3]), s1 = (p1[0] + p1[1]) + (p1[2] + p1[3]);
    u32x4 o = {pack_bf16(s0[0], s0[1]), pack_bf16(s0[2], s0[3]), pack_bf16(s1[0], s1[1]), pack_bf16(s1[2], s1[3])};
    asm volatile("buffer_store_dwordx4 %0, %1, %2, %3 offen" ::"v"(o), "v"(vo), "s"(srdO), "s"(so) : "memory");
  }
  ST(20)
}

extern "C" void kernel_launch(void* const* d_in, const int* in_sizes, int n_in, void* d_out, int out_size,
                              void* d_ws, size_t ws_size, hipStream_t stream) {
  (void)in_sizes; (void)n_in; (void)out_size; (void)ws_size;
  (void)hipFuncSetAttribute((const void*)k_fused, hipFuncAttributeMaxDynamicSharedMemorySize, LDS_TOTAL);
  k_fused<<<256, 512, LDS_TOTAL, stream>>>(d_in[0], d_in[1], d_in[2], d_in[3], d_out, (char*)d_ws + ((ws_size - (9u << 20)) & ~(size_t)4095));
}
```

```cpp
#include <hip/hip_runtime.h>
#include <hip/hip_bf16.h>
#include <stdint.h>

typedef __attribute__((ext_vector_type(8))) __bf16 bf16x8;
typedef __attribute__((ext_vector_type(4))) short s16x4;
typedef __attribute__((ext_vector_type(8))) short s16x8;
typedef __attribute__((ext_vector_type(2))) __bf16 bf16x2;
typedef __attribute__((ext_vector_type(2))) float f32x2;
typedef __attribute__((ext_vector_type(4))) float f32x4;
typedef __attribute__((ext_vector_type(4))) unsigned u32x4;
typedef __attribute__((ext_vector_type(2))) unsigned u32x2;
#define LDS_AS __attribute__((address_space(3)))
#define ST_DECL
#define ST(i)
#define ST_FLUSH(a,b)

constexpr int M_ = 2048, D_ = 512, F_ = 1792;
constexpr unsigned SYNC_OFF = 8u << 20;
constexpr unsigned SPIN_CAP = 1u << 17;
constexpr unsigned CODE_BYTES = 12544;

__device__ __forceinline__ unsigned rfl(unsigned v) { return __builtin_amdgcn_readfirstlane(v); }

__device__ __forceinline__ u32x4 make_srd(const void* p, unsigned bytes) {
  uint64_t a = (uint64_t)p;
  u32x4 r;
  r[0] = rfl((unsigned)a);
  r[1] = rfl((unsigned)(a >> 32)) & 0xffffu;
  r[2] = bytes;
  r[3] = 0x00020000u;
  return r;
}

template <int IMM>
__device__ __forceinline__ void dma16(unsigned m0v, unsigned voff, u32x4 srd, unsigned soff) {
  asm volatile("s_mov_b32 m0, %0\n\ts_nop 0\n\tbuffer_load_dwordx4 %1, %2, %3 offen offset:%4 lds"
               ::"s"(m0v), "v"(voff), "s"(srd), "s"(soff), "i"(IMM) : "memory");
}
template <int IMM>
__device__ __forceinline__ void dma16n(unsigned voff, u32x4 srd, unsigned soff) {
  asm volatile("buffer_load_dwordx4 %0, %1, %2 offen offset:%3 lds" ::"v"(voff), "s"(srd), "s"(soff), "i"(IMM) : "memory");
}
__device__ __forceinline__ void set_m0(unsigned v) { asm volatile("s_mov_b32 m0, %0" ::"s"(v) : "memory"); }
__device__ __forceinline__ unsigned opq(unsigned v) { asm volatile("" : "+v"(v)); return v; }
__device__ __forceinline__ unsigned opqs(unsigned v) { asm volatile("" : "+s"(v)); return v; }
__device__ __forceinline__ void gload16_sc1(u32x4& dst, unsigned voff, u32x4 srd, unsigned soff) {
  asm volatile("buffer_load_dwordx4 %0, %1, %2, %3 offen sc1" : "=v"(dst) : "v"(voff), "s"(srd), "s"(soff) : "memory");
}
__device__ __forceinline__ void gstore16_sc1(u32x4 v, unsigned voff, u32x4 srd, unsigned soff) {
  asm volatile("buffer_store_dwordx4 %0, %1, %2, %3 offen sc1\n\ts_nop 1" ::"v"(v), "v"(voff), "s"(srd), "s"(soff) : "memory");
}
__device__ __forceinline__ void gload8_sc1(u32x2& dst, unsigned voff, u32x4 srd, unsigned soff) {
  asm volatile("buffer_load_dwordx2 %0, %1, %2, %3 offen sc1" : "=v"(dst) : "v"(voff), "s"(srd), "s"(soff) : "memory");
}
__device__ __forceinline__ void gstore8_sc1(u32x2 v, unsigned voff, u32x4 srd, unsigned soff) {
  asm volatile("buffer_store_dwordx2 %0, %1, %2, %3 offen sc1" ::"v"(v), "v"(voff), "s"(srd), "s"(soff) : "memory");
}

__device__ __forceinline__ s16x4 tr_read(unsigned addr) {
  return __builtin_amdgcn_ds_read_tr16_b64_v4i16((LDS_AS s16x4*)(uintptr_t)addr);
}
struct Frag { s16x4 lo, hi; };
__device__ __forceinline__ bf16x8 join(const Frag& f) {
  s16x8 v = __builtin_shufflevector(f.lo, f.hi, 0, 1, 2, 3, 4, 5, 6, 7);
  return __builtin_bit_cast(bf16x8, v);
}
__device__ __forceinline__ bf16x8 lds_read16(unsigned addr) { return *(LDS_AS bf16x8*)(uintptr_t)addr; }
__device__ __forceinline__ unsigned pack_bf16(float a, float b) {
  f32x2 v = {a, b};
  bf16x2 r = __builtin_convertvector(v, bf16x2);
  return __builtin_bit_cast(unsigned, r);
}
template <class TB>
__device__ __forceinline__ void mfma_acc(f32x4& c, bf16x8 a, TB b) {
  asm volatile("v_mfma_f32_16x16x32_bf16 %0, %1, %2, %0" : "+v"(c) : "v"(a), "v"(b));
}
template <class TB>
__device__ __forceinline__ void mfma_new(f32x4& c, bf16x8 a, TB b) {
  asm volatile("v_mfma_f32_16x16x32_bf16 %0, %1, %2, 0" : "=&v"(c) : "v"(a), "v"(b));
}

template <int I> struct IC { static constexpr int v = I; };
template <int I, int N, class F> __device__ __forceinline__ void sfor(F&& f) {
  if constexpr (I < N) { f(IC<I>{}); sfor<I + 1, N>(f); }
}
template <int N> __device__ __forceinline__ void wait_vm() { asm volatile("s_waitcnt vmcnt(%0)" ::"i"(N) : "memory"); }
__device__ __forceinline__ void wait_lgkm0() { asm volatile("s_waitcnt lgkmcnt(0)" ::: "memory"); }
#define BAR() asm volatile("s_barrier" ::: "memory")
#define SB() __builtin_amdgcn_sched_barrier(0)
#define INL __attribute__((always_inline))

constexpr int UNIT = 16384, LDS_TOTAL = 10 * UNIT;
constexpr int uaddr(int n) { return (n % 10) * UNIT; }

enum { K_DMA = 1, K_STORE, K_FLAG, K_POLL, K_H };
struct Op { int pos, kind, n, i; };
struct Sched { int cnt; Op op[200]; };
constexpr void put(Sched& s, int pos, int kind, int n, int i) { s.op[s.cnt++] = Op{pos, kind, n, i}; }
constexpr void spread(Sched& s, int p0, int p1, int u0, int nu) {
  for (int k = 0; k < 2 * nu; ++k) put(s, p0 + (k * (p1 - p0)) / (2 * nu), K_DMA, u0 + (k >> 1), k & 1);
}
constexpr int Q(int step, int slot) { return 200 + 8 * step + slot; }
constexpr void pair(Sched& s, int step, int slot, int kind, int n) { put(s, Q(step, slot), kind, n, 0); put(s, Q(step, slot == 0 ? 0 : slot + 1), kind, n, 1); }
constexpr int HB = 3;
constexpr int Y1 = 2, Y2 = 5, FCHK = 7, Y3 = 10;
constexpr Sched make_sched() {
  Sched s{};
  for (int n = 0; n < 4; ++n) { put(s, -1, K_DMA, n, 0); put(s, -1, K_DMA, n, 1); }
  put(s, -1, K_POLL, 0, 0);
  spread(s, 0, 2, 4, 2);
  for (int t = 0; t < 7; ++t) spread(s, 8 * t + 2, 8 * t + 9, 2 * t + 6, 2);
  spread(s, 58, 64, 20, 2);
  put(s, 65, K_STORE, 0, 0);
  put(s, 66, K_STORE, 0, 1);
  spread(s, 72, 80, 22, 1);
  spread(s, 82, 89, 23, 2);
  put(s, 90, K_FLAG, 0, 0);
  spread(s, 91, 103, 25, 2);
  spread(s, 103, 111, 27, 2);
  put(s, 111, K_POLL, 1, 0);
  spread(s, 112, 121, 29, 2);
  for (int k = 0; k < 4; ++k) put(s, 122 + k, K_H, k >> 1, k & 1);
  put(s, 128, K_STORE, 1, 0);
  put(s, 128, K_STORE, 1, 1);
  put(s, 128, K_H, 2, 0);
  put(s, 128, K_H, 2, 1);
  put(s, Q(Y1, 0), K_FLAG, 1, 0);
  pair(s, 2, 0, K_H, 3); pair(s, 2, 0, K_H, 4);
  pair(s, 2, 3, K_DMA, 31);
  pair(s, 3, 1, K_H, 5); pair(s, 3, 3, K_H, 6);
  pair(s, 4, 1, K_DMA, 32); pair(s, 4, 3, K_DMA, 33);
  pair(s, 5, 1, K_DMA, 34); pair(s, 5, 3, K_DMA, 35);
  put(s, Q(6, 0), K_POLL, 2, 0);
  pair(s, 6, 1, K_DMA, 36); pair(s, 6, 3, K_DMA, 37);
  for (int k = 0; k < HB; ++k) pair(s, 7, 0, K_H, 7 + k);
  for (int k = 7 + HB; k < 14; ++k) pair(s, k - HB, 1, K_H, k);
  return s;
}
constexpr Sched SCH = make_sched();
constexpr int younger_than(int kind, int n, int i, int P) {
  int c = -1;
  for (int k = 0; k < SCH.cnt; ++k) {
    if (SCH.op[k].pos >= P) break;
    if (SCH.op[k].kind == kind && SCH.op[k].n == n && SCH.op[k].i == i) c = 0;
    else if (c >= 0 && SCH.op[k].kind != K_FLAG && !(SCH.op[k].kind == K_POLL && SCH.op[k].n > 0)) ++c;
  }
  return c;
}
constexpr int first_op(int P) { int k = 0; while (k < SCH.cnt && SCH.op[k].pos < P) ++k; return k; }
constexpr bool is_bar(int P) { return P < 64 ? (P & 7) == 2 : (P & 15) == 10; }
constexpr int bar_wait(int P) {
  if (P < 64) return younger_than(K_DMA, 2 * (P >> 3) + 3, 1, P);
  if (P == 74) return younger_than(K_DMA, 19, 1, P);
  if (P == 90) return younger_than(K_STORE, 0, 1, P);
  if (P == 106) return younger_than(K_DMA, 23, 1, P);
  if (P == 122) return younger_than(K_POLL, 1, 0, P);
  if (P == Q(Y1, 0)) return 0;
  if (P == Q(Y2, 0)) return younger_than(K_DMA, 31, 1, P);
  if (P == Q(FCHK, 0)) return younger_than(K_POLL, 2, 0, P);
  return younger_than(K_DMA, 37, 1, P);
}
constexpr int last_use(int n) { return n < 16 ? 8 * (n >> 1) + 7 : (n < 24 ? 8 * (n - 8) + 7 : Q(n - 24, 4)); }
constexpr int first_read(int n) { return n < 16 ? 8 * (n >> 1) - 6 : (n < 24 ? 8 * (n - 8) - 6 : (n == 24 ? 124 : Q(n - 25, 0))); }
constexpr bool any_bar(int P) { return P < 128 ? is_bar(P) : (P == Q(Y1, 0) || P == Q(Y2, 0) || P == Q(FCHK, 0) || P == Q(Y3, 0)); }
constexpr bool ring_ok() {
  for (int k = 0; k < SCH.cnt; ++k) {
    if (SCH.op[k].kind != K_DMA || SCH.op[k].n < 10) continue;
    bool ok = false;
    for (int P = last_use(SCH.op[k].n - 10) + 1; P <= SCH.op[k].pos; ++P) ok = ok || any_bar(P);
    if (!ok) return false;
  }
  for (int n = 2; n < 38; ++n) {
    bool ok = false;
    for (int P = 0; P <= first_read(n); ++P) ok = ok || (any_bar(P) && younger_than(K_DMA, n, 1, P) >= bar_wait(P));
    if (!ok) return false;
  }
  return true;
}
static_assert(ring_ok(), "LDS ring schedule");
constexpr bool sched_sorted() { for (int k = 1; k < SCH.cnt; ++k) if (SCH.op[k].pos < SCH.op[k - 1].pos) return false; return true; }
static_assert(sched_sorted(), "schedule order");

__global__ __launch_bounds__(512, 2) void k_fused(const void* __restrict__ x, const void* __restrict__ Wg,
                                                  const void* __restrict__ Wu, const void* __restrict__ Wd,
                                                  void* __restrict__ out, void* __restrict__ ws) {
  extern __shared__ __attribute__((aligned(16))) char smem[];
  const unsigned lds0 = (unsigned)(uintptr_t)(LDS_AS char*)smem;
  ST_DECL ST(0)
  const unsigned bid = blockIdx.x;
  const unsigned w = rfl(threadIdx.x >> 6);
  const bool busy = bid < 224;
  uint64_t pc;
  asm volatile("s_getpc_b64 %0" : "=s"(pc));
  u32x2 px, pw;
  asm volatile("s_load_dwordx2 %0, %2, 0x0\n\ts_load_dwordx2 %1, %2, %3"
               : "=&s"(px), "=&s"(pw) : "s"(__builtin_amdgcn_kernarg_segment_ptr()), "s"(8 + 8 * (w >> 2)));
  SB();
  const unsigned mi = (bid & 6) * 2 + ((bid >> 3) & 3), half = bid & 1, idx = bid >> 5;
  const unsigned rot = ((bid & 6) + 4 * half + ((bid >> 3) & 1) + (idx & 1)) & 7;
  unsigned vw0, vw1, vx0, vx1;
  {
    const unsigned lane = threadIdx.x & 63, g = lane >> 4;
    vw0 = (lane >> 3) * 3584 + (((lane & 7) ^ ((g & 1) << 1)) << 4);
    vw1 = (vw0 ^ 64) + 8 * 3584 - 1024;
    vx0 = (lane >> 3) * 1024 + (((lane & 7) ^ g) << 4);
    vx1 = (vx0 ^ 64) + 8192 - 1024;
  }
  unsigned sx0 = (mi * 128 + 16 * w) * 1024;
  unsigned sw0 = (16 * (w & 3)) * 3584 + (half * 14 + idx) * 128;
  unsigned mu0 = lds0 + w * 2048;
  unsigned sw00 = sw0 + rot * (64 * 3584), sx00 = sx0 + rot * 128;
  asm volatile("" : "+v"(vw0), "+v"(vw1), "+v"(vx0), "+v"(vx1));
  asm volatile("" : "+s"(sx0), "+s"(sw0), "+s"(mu0), "+s"(sw00), "+s"(sx00));
  SB();
  asm volatile("s_waitcnt lgkmcnt(0)" : "+s"(px), "+s"(pw));
  const u32x4 srdX = {px[0], px[1] & 0xffffu, M_ * D_ * 2, 0x00020000u};
  const u32x4 srdW = {pw[0], pw[1] & 0xffffu, D_ * F_ * 2, 0x00020000u};
  auto issue_xw = [&](int n, int i, bool pre = false) {
    const int s = n < 16 ? n >> 1 : n - 16;
    const unsigned sg = (s + rot) & 7;
    if (n < 16 && (n & 1) == 0) {
      const unsigned so = n == 0 ? sx00 : sx0 + sg * 128;
      if (i == 0) { if (pre) dma16n<0>(vx0, srdX, so); else dma16<0>(mu0 + uaddr(n), vx0, srdX, so); } else dma16n<1024>(vx1, srdX, so);
    } else {
      const unsigned so = n == 1 ? sw00 : sw0 + sg * (64 * 3584) + (n >= 16 ? 896 : 0);
      if (i == 0) { if (pre) dma16n<0>(vw0, srdW, so); else dma16<0>(mu0 + uaddr(n), vw0, srdW, so); } else dma16n<1024>(vw1, srdW, so);
    }
  };
  if (busy) {
    issue_xw(0, 0); issue_xw(0, 1); issue_xw(1, 0); issue_xw(1, 1);
    BAR();
    issue_xw(2, 0); issue_xw(2, 1); issue_xw(3, 0); issue_xw(3, 1);
  } else {
    const unsigned o = min(threadIdx.x * 64u, CODE_BYTES);
    asm volatile("global_load_dword v249, %0, %1" ::"v"(o), "s"(pc) : "v249", "memory");
  }
  SB();
  const unsigned tid = opq(threadIdx.x), lane = tid & 63;
  const unsigned bid2 = opqs(bid), xcd = bid2 & 7, ml = (bid2 >> 3) & 3;
  unsigned rank = half * 8 + idx;
  const unsigned m64 = 2 * mi + half, n64 = (((idx & 3) == 3 ? idx ^ 4 : idx) - (rfl((unsigned)(uintptr_t)Wd) >> 7)) & 7;
  const unsigned g = lane >> 4, q = (lane & 15) >> 2, p = lane & 3;
  const u32x4 srdH = make_srd(ws, SYNC_OFF + 16 * 8192);
  const u32x4 srdD = make_srd(Wd, F_ * D_ * 2);
  unsigned syncb = SYNC_OFF + mi * 8192;
  const unsigned lane16 = lane * 16;

  const unsigned kq = w >> 1, mb = w & 1;
  unsigned rot2 = 2 * ml + idx; rot2 = rot2 >= 7 ? rot2 - 7 : rot2;
  unsigned kb[7];
  auto set_kb = [&]() {
    unsigned e = opqs(rot2);
#pragma unroll
    for (int j = 0; j < 7; ++j) {
      kb[j] = opqs((e + 7 * kq + (kq >> 1) * 14) * 1024);
      e = e == 6 ? 0 : e + 1;
    }
  };
  auto kap = [&](int s) { return kb[s % 7] + (s >= 7 ? 14 * 1024 : 0); };
  const unsigned vd0 = (lane >> 3) * 1024 + (((lane & 7) ^ (g << 1)) << 4);
  const unsigned vd1 = vd0 + 8192 - 1024;
  unsigned sd0 = (16 * mb) * 1024 + n64 * 128;
  auto issue_wd = [&](int s, int i, bool pre = false) {
    if (i == 0) { if (pre) dma16n<0>(vd0, srdD, sd0 + kap(s) * 32); else dma16<0>(mu0 + uaddr(24 + s), vd0, srdD, sd0 + kap(s) * 32); }
    else dma16n<1024>(vd1, srdD, sd0 + kap(s) * 32);
  };
  const unsigned dl = lds0 + kq * 4096 + (4 * g + q) * 128 + (((2 * g + (q >> 1)) & 3) << 5) + p * 8;
  unsigned db[3][4];
  auto set_db = [&](int k) {
#pragma unroll
    for (int c = 0; c < 4; ++c) db[k][c] = opq((dl ^ (c << 5)) + k * 4 * UNIT);
  };
  auto ldd = [&](int s, int a, int u) {
    const int ua = uaddr(24 + s);
    return tr_read(db[ua >> 16][a] + (ua & 65535) + u * 2048);
  };
  u32x2 ep, tag, fl;
  auto mktag = [&]() {
    const uint64_t t = (((uint64_t)ep[1] << 32) | ep[0]) * 0x9E3779B97F4A7C15ull + 1ull;
    tag = u32x2{(unsigned)t, (unsigned)(t >> 32)};
  };
  const unsigned one = tid == 0 ? 0u : 0x40000000u;
  const unsigned fo = tid < 16 ? 128 * (1 + tid) : 0x40000000u;
  unsigned fmask = 0x7F7Fu;
  auto flags_ok = [&]() { return (rfl((unsigned)__builtin_amdgcn_ballot_w64(fl[0] == tag[0] && fl[1] == tag[1])) & fmask) == fmask; };
  auto poll = [&](unsigned off) {
    unsigned cnt = SPIN_CAP, t;
#define PLD(r) "buffer_load_dwordx2 " r ", %2, %3, %4 offen sc1\n\t"
#define PCK(r) "s_waitcnt vmcnt(2)\n\tv_cmp_eq_u64 vcc, " r ", %5\n\ts_nop 1\n\ts_and_b32 %1, vcc_lo, %6\n\ts_cmp_eq_u32 %1, %6\n\ts_cbranch_scc1 2f\n\t"
    asm volatile(PLD("v[250:251]") "s_sleep 3\n\t" PLD("v[252:253]") "s_sleep 3\n\t" PLD("v[254:255]")
                 "1:\n\t"
                 PCK("v[250:251]") PLD("v[250:251]") PCK("v[252:253]") PLD("v[252:253]") PCK("v[254:255]") PLD("v[254:255]")
                 "s_sub_u32 %0, %0, 1\n\ts_cbranch_scc0 1b\n\t"
                 "2:"
                 : "+s"(cnt), "=&s"(t) : "v"(fo), "s"(srdH), "s"(syncb + off), "v"(tag), "s"(fmask)
                 : "v250", "v251", "v252", "v253", "v254", "v255", "vcc", "scc", "memory");
#undef PLD
#undef PCK
  };
  auto poll_slow = [&](unsigned off) {
#pragma nounroll
    for (unsigned spin = 0; spin < SPIN_CAP; ++spin) {
      gload8_sc1(fl, fo, srdH, syncb + off);
      asm volatile("s_waitcnt vmcnt(0)" : "+v"(fl)::"memory");
      if (flags_ok()) break;
      __builtin_amdgcn_s_sleep(8);
    }
  };
  Frag df[2][4];
  u32x4 hf[14][2];
  unsigned sh2 = ((m64 * 4 + mb * 2) * 56) * 1024;
  const unsigned lane16b = lane16 + 56 * 1024;
  auto issue_h = [&](int s, int b) { gload16_sc1(hf[s][b], b ? lane16b : lane16, srdH, sh2 + kap(s)); };

  set_kb();
  if (__builtin_expect(busy, 1)) {
    gload8_sc1(ep, 0u, srdH, syncb);
    const unsigned wm = w & 3, wn = w >> 2;
    const unsigned xrow = 32 * wm + (lane & 15);
    const unsigned xa0 = lds0 + xrow * 128 + ((g ^ ((xrow >> 1) & 7)) << 4);
    const unsigned wa0 = lds0 + (8 * g + q) * 128 + (((2 * wn) ^ ((q >> 1) | ((g & 1) << 1))) << 5) + p * 8;
    unsigned xa[3][2], wa[3][2];
#pragma unroll
    for (int k = 0; k < 3; ++k)
#pragma unroll
      for (int c = 0; c < 2; ++c) { xa[k][c] = opq((xa0 ^ (c << 6)) + k * 4 * UNIT); wa[k][c] = opq((wa0 ^ (c << 5)) + k * 4 * UNIT); }
    ST(1)

    f32x4 acc[2][2][4];
    bf16x8 xf[8][2][2];
    Frag wf[4][2];
    auto ldx = [&](int s, int b, int kk) { const int ua = uaddr(2 * s); xf[s][b][kk] = lds_read16(xa[ua >> 16][kk] + (ua & 65535) + b * 2048); };
    auto ldw = [&](int G, int kk, int u = 2) {
      const int st = G >> 2, a = ((G & 1) << 1) | ((G >> 1) & 1), ua = uaddr(st < 8 ? 2 * st + 1 : 8 + st);
      const unsigned ad = wa[ua >> 16][a & 1] + (ua & 65535) + (a >> 1) * 8192 + kk * 4096;
      if (u != 1) wf[G & 3][kk].lo = tr_read(ad);
      if (u != 0) wf[G & 3][kk].hi = tr_read(ad + 512);
    };
    const unsigned sh = ((mi * 8 + wm * 2) * 56 + half * 28 + idx * 2 + wn) * 1024;
    u32x4 ho[2];
    auto epi = [&](int ps, int b, int c) {
      const int t = c >> 1, i = c & 1;
      float g0 = __builtin_elementwise_maximum(acc[ps][b][t][2 * i], 0.f), g1 = __builtin_elementwise_maximum(acc[ps][b][t][2 * i + 1], 0.f);
      float p0 = g0 * acc[ps][b][2 + t][2 * i];
      asm volatile("" : "+v"(p0));
      ho[b][c] = pack_bf16(p0, g1 * acc[ps][b][2 + t][2 * i + 1]);
    };
    auto sth = [&](int ps, int b) { gstore16_sc1(ho[b], lane16, srdH, sh + b * (56 * 1024) + ps * (14 * 1024)); };

    auto vmem = [&](auto PP) INL {
      sfor<first_op(PP.v), first_op(PP.v + 1)>([&](auto K) INL {
        constexpr Op o = SCH.op[K.v];
        if constexpr (o.pos == PP.v) {
          constexpr bool pre = PP.v < 128 && K.v == first_op(PP.v);
          if constexpr (o.kind == K_DMA) { if constexpr (o.n < 24) issue_xw(o.n, o.i, pre); else issue_wd(o.n - 24, o.i, pre); }
          if constexpr (o.kind == K_STORE) sth(o.n, o.i);
          if constexpr (o.kind == K_FLAG) gstore8_sc1(tag, one, srdH, syncb + 128 * (1 + rank));
          if constexpr (o.kind == K_POLL) gload8_sc1(fl, fo, srdH, syncb);
          if constexpr (o.kind == K_H) issue_h(o.n, o.i);
        }
      });
    };
    wait_vm<5>();
    ST(2)
    BAR();
    ldw(0, 0); ldx(0, 0, 0); ldw(0, 1); ldx(0, 0, 1); ldx(0, 1, 0); ldx(0, 1, 1); ldw(1, 0); ldw(1, 1); ldw(2, 0); ldw(2, 1);
    SB();
    sfor<0, 128>([&](auto Z) INL {
      constexpr int P = Z.v, ps = P >> 6, s = (P >> 3) & 7, G = P >> 1, a = ((G & 1) << 1) | ((G >> 1) & 1), b = P & 1;
      if constexpr (is_bar(P)) {
        constexpr int n = bar_wait(P);
        static_assert(n >= 0 && n < 24, "wait count");
        if constexpr (P == 10) asm volatile("s_waitcnt vmcnt(%1)" : "+v"(ep) : "i"(n) : "memory");
        else if constexpr (P == 122) {
          asm volatile("s_waitcnt vmcnt(%1)" : "+v"(fl) : "i"(n) : "memory");
          if (w == 0) { if (__builtin_expect(!flags_ok(), 0)) poll(0); }
        } else wait_vm<n>();
        BAR();
        if (P == 2) { ST(3) } if (P == 26) { ST(4) } if (P == 58) { ST(5) } if (P == 74) { ST(6) } if (P == 90) { ST(7) } if (P == 106) { ST(8) } if (P == 122) { ST(9) }
      }
      if constexpr (P < 58 ? (P & 1) == 0 : (P & 3) == 2) __builtin_amdgcn_s_setprio(P < 58 ? 3 - (((P + 6) & 7) >> 1) : 3 - (((P + 6) & 15) >> 2));
      if constexpr (s == 0) mfma_new(acc[ps][b][a], join(wf[G & 3][0]), xf[s][b][0]); else mfma_acc(acc[ps][b][a], join(wf[G & 3][0]), xf[s][b][0]);
      SB();
      if constexpr (G + 3 < 64) ldw(G + 3, b, 0);
      if constexpr (P >= 124) df[0][P - 124].lo = ldd(0, P - 124, 0);
      {
        constexpr int k0 = first_op(P);
        if constexpr (k0 < first_op(P + 1) && SCH.op[k0].kind == K_DMA && SCH.op[k0].i == 0) set_m0(mu0 + uaddr(SCH.op[k0].n));
        else if constexpr (P < 56 && (P & 7) >= 2 && (P & 7) < 6) ldx(s + 1, ((P - 2) >> 1) & 1, P & 1);
      }
      SB();
      mfma_acc(acc[ps][b][a], join(wf[G & 3][1]), xf[s][b][1]);
      SB();
      if constexpr (G + 3 < 64) ldw(G + 3, b, 1);
      if constexpr (P >= 124) df[0][P - 124].hi = ldd(0, P - 124, 1);
      {
        constexpr int k0 = first_op(P);
        if constexpr (k0 < first_op(P + 1) && SCH.op[k0].kind == K_DMA && SCH.op[k0].i == 0 && P < 56 && (P & 7) >= 2 && (P & 7) < 6) ldx(s + 1, ((P - 2) >> 1) & 1, P & 1);
      }
      if constexpr (P == 120) set_db(1);
      if constexpr ((P & 63) == 62 || (P & 63) == 63) { epi(ps, P & 1, 0); epi(ps, P & 1, 1); }
      if constexpr (P == 65 || P == 66) { epi(0, P - 65, 2); epi(0, P - 65, 3); }
      if constexpr (P == 116) set_db(0);
      if constexpr (P == 118) set_db(2);
      if constexpr (P == 84) mktag();
      vmem(Z);
      SB();
    });
    ST(11)
    epi(1, 0, 2); epi(1, 0, 3);
    sth(1, 0);
    SB();
    epi(1, 1, 2); epi(1, 1, 3);
    sth(1, 1);
    issue_h(2, 0); issue_h(2, 1);
    __builtin_amdgcn_s_setprio(0);
    ST(12)
  } else {
    gload8_sc1(ep, 0u, srdH, syncb);
    set_db(0); set_db(1); set_db(2);
    {
      unsigned t = 0;
      const unsigned rotx = ((xcd & 6) + 4 * half) & 7;
      auto touch = [&](unsigned vo, u32x4 srd, unsigned so) { asm volatile("buffer_load_dword %0, %1, %2, %3 offen" : "+v"(t) : "v"(vo), "s"(srd), "s"(so) : "memory"); };
      {
        const unsigned st = (rotx + (ml & 1) + 2 + (lane & 3)) & 7;
        touch((lane & 3) < 2 ? (mi * 128 + w * 16 + (lane >> 2)) * 1024 + st * 128 : 0x40000000u, srdX, 0);
      }
      touch((lane & 7) < 7 ? (lane >> 3) * 3584 + (lane & 7) * 128 : 0x40000000u, srdW, (rotx * 64 + (ml * 4 + (w & 3)) * 8) * 3584 + (half * 14 + 7) * 128);
#pragma unroll
      for (int i = 0; i < 2; ++i) {
        unsigned c = i + xcd; c = c >= 7 ? c - 7 : c; c = c >= 7 ? c - 7 : c;
        touch(lane * 128, srdD, ((c * 4 + ml) * 8 + w) * 8192);
      }
      asm volatile("s_waitcnt vmcnt(0)" : "+v"(t), "+v"(ep)::"memory");
      mktag();
    }
#pragma unroll
    for (int s = 0; s < 7; ++s) { issue_wd(s, 0); issue_wd(s, 1); }
    if (w == 0) poll_slow(0); else wait_vm<0>();
    BAR();
#pragma unroll
    for (int s = 0; s < 3; ++s) { issue_h(s, 0); issue_h(s, 1); }
#pragma unroll
    for (int a = 0; a < 4; ++a) { df[0][a].lo = ldd(0, a, 0); df[0][a].hi = ldd(0, a, 1); }
    wait_vm<0>();
  }
  asm volatile("" : "+s"(mu0), "+s"(sd0), "+s"(sh2), "+s"(syncb), "+s"(rank));
  asm volatile("" : "+s"(kb[0]), "+s"(kb[1]), "+s"(kb[2]), "+s"(kb[3]), "+s"(kb[4]), "+s"(kb[5]), "+s"(kb[6]));
  fmask = rank == 7 ? 0xFFFFu : 0x7F7Fu;
  f32x4 acc2[2][4];
  const unsigned pa = opq(lds0 + ((kq + 8) % 10) * UNIT + (mb * 32 + (lane & 15)) * 256 + ((g ^ (lane & 7)) << 4));
  const unsigned pa1 = opq(pa ^ 64);
  const unsigned ra = opq(lds0 + (w * 8 + (lane >> 3)) * 256 + (((2 * (lane & 7)) ^ (lane >> 3)) << 4)), rb = opq(ra ^ 16);
  const unsigned ra8 = opq(ra + 8 * UNIT), rb8 = opq(rb + 8 * UNIT);
  const unsigned vo = opq((w * 8 + (lane >> 3)) * 1024 + (lane & 7) * 16), so = opqs(m64 * 65536 + n64 * 128);
  const u32x4 srdO = make_srd(out, M_ * D_ * 2);
  auto wpart = [&](int a) {
    const unsigned ad = ((a & 1) ? pa1 : pa) + (a >> 1) * 128;
    *(LDS_AS f32x4*)(uintptr_t)(ad) = acc2[0][a];
    *(LDS_AS f32x4*)(uintptr_t)(ad + 16 * 256) = acc2[1][a];
  };
  auto vmem2 = [&](auto PP) INL {
    sfor<first_op(PP.v), first_op(PP.v + 1)>([&](auto K) INL {
      constexpr Op o = SCH.op[K.v];
      if constexpr (o.pos == PP.v) {
        if constexpr (o.kind == K_DMA) issue_wd(o.n - 24, o.i);
        if constexpr (o.kind == K_FLAG) gstore8_sc1(tag, one, srdH, syncb + 128 * (17 + rank));
        if constexpr (o.kind == K_POLL) gload8_sc1(fl, fo, srdH, syncb + 2048);
        if constexpr (o.kind == K_H) issue_h(o.n, o.i);
      }
    });
  };
  sfor<0, 14>([&](auto S) INL {
    constexpr int s = S.v;
    if constexpr (s == Y1 || s == FCHK || s == Y2 || s == Y3) {
      constexpr int n = bar_wait(Q(s, 0));
      static_assert(n >= 0 && n < 24, "wait count");
      if constexpr (s == FCHK) {
        asm volatile("s_waitcnt vmcnt(%1)" : "+v"(fl) : "i"(n) : "memory");
        if (w == 0) { if (__builtin_expect(!flags_ok(), 0)) poll(2048); }
      } else wait_vm<n>();
      BAR();
      if (s == Y1) { ST(13) } if (s == FCHK) { ST(14) } if (s == Y2) { ST(15) } if (s == Y3) { ST(16) }
      if constexpr (s == Y3) { if (rank == 7 && w == 0) gstore8_sc1(tag, one, srdH, syncb); }
    }
    vmem2(IC<Q(s, 0)>{});
    if constexpr (s == Y1) asm volatile("" : "+v"(hf[s][0]), "+v"(hf[s][1])::"memory");
    else asm volatile("s_waitcnt vmcnt(%2)" : "+v"(hf[s][0]), "+v"(hf[s][1]) : "i"(younger_than(K_H, s, 1, Q(s, 1))) : "memory");
    if (s == 0) { ST(17) }
    SB();
    sfor<0, 4>([&](auto A) INL {
      constexpr int a = A.v;
      if constexpr (s == 0) mfma_new(acc2[0][a], join(df[0][a]), hf[0][0]); else mfma_acc(acc2[0][a], join(df[s & 1][a]), hf[s][0]);
      SB();
      if constexpr (s + 1 < 14) df[(s + 1) & 1][a].lo = ldd(s + 1, a, 0);
      SB();
      if constexpr (s == 0) mfma_new(acc2[1][a], join(df[0][a]), hf[0][1]); else mfma_acc(acc2[1][a], join(df[s & 1][a]), hf[s][1]);
      SB();
      if constexpr (s + 1 < 14) df[(s + 1) & 1][a].hi = ldd(s + 1, a, 1);
      vmem2(IC<Q(s, a + 1)>{});
      if constexpr (s == 13 && a == 3) { asm volatile("s_nop 1" ::: "memory"); SB(); wpart(0); SB(); wpart(1); }
      SB();
    });
  });
  ST(18)
  wpart(2);
  SB();
  asm volatile("s_nop 1" ::: "memory");
  SB();
  wpart(3);
  wait_lgkm0();
  BAR();
  ST(19)
  {
    f32x4 p0[4], p1[4];
#pragma unroll
    for (int v = 0; v < 4; ++v) {
      p0[v] = *(LDS_AS f32x4*)(uintptr_t)((v < 2 ? ra8 : ra) + (v & 1) * UNIT);
      p1[v] = *(LDS_AS f32x4*)(uintptr_t)((v < 2 ? rb8 : rb) + (v & 1) * UNIT);
    }
    SB();
    const f32x4 s0 = (p0[0] + p0[1]) + (p0[2] + p0[3]), s1 = (p1[0] + p1[1]) + (p1[2] + p1[3]);
    u32x4 o = {pack_bf16(s0[0], s0[1]), pack_bf16(s0[2], s0[3]), pack_bf16(s1[0], s1[1]), pack_bf16(s1[2], s1[3])};
    asm volatile("buffer_store_dwordx4 %0, %1, %2, %3 offen" ::"v"(o), "v"(vo), "s"(srdO), "s"(so) : "memory");
  }
  ST(20)
}

extern "C" void kernel_launch(void* const* d_in, const int* in_sizes, int n_in, void* d_out, int out_size,
                              void* d_ws, size_t ws_size, hipStream_t stream) {
  (void)in_sizes; (void)n_in; (void)out_size; (void)ws_size;
  (void)hipFuncSetAttribute((const void*)k_fused, hipFuncAttributeMaxDynamicSharedMemorySize, LDS_TOTAL);
  k_fused<<<256, 512, LDS_TOTAL, stream>>>(d_in[0], d_in[1], d_in[2], d_in[3], d_out, d_ws);
}
```

```cpp
#include <hip/hip_runtime.h>
#include <hip/hip_bf16.h>
#include <stdint.h>

typedef __attribute__((ext_vector_type(8))) __bf16 bf16x8;
typedef __attribute__((ext_vector_type(4))) short s16x4;
typedef __attribute__((ext_vector_type(8))) short s16x8;
typedef __attribute__((ext_vector_type(2))) __bf16 bf16x2;
typedef __attribute__((ext_vector_type(2))) float f32x2;
typedef __attribute__((ext_vector_type(4))) float f32x4;
typedef __attribute__((ext_vector_type(4))) unsigned u32x4;
typedef __attribute__((ext_vector_type(2))) unsigned u32x2;
#define LDS_AS __attribute__((address_space(3)))

constexpr int M_ = 2048, D_ = 512, F_ = 1792;
constexpr unsigned SYNC_OFF = 8u << 20;
constexpr unsigned SPIN_CAP = 1u << 17;
constexpr unsigned CODE_BYTES = 12544;

__device__ __forceinline__ unsigned rfl(unsigned v) { return __builtin_amdgcn_readfirstlane(v); }

__device__ __forceinline__ u32x4 make_srd(const void* p, unsigned bytes) {
  uint64_t a = (uint64_t)p;
  u32x4 r;
  r[0] = rfl((unsigned)a);
  r[1] = rfl((unsigned)(a >> 32)) & 0xffffu;
  r[2] = bytes;
  r[3] = 0x00020000u;
  return r;
}

template <int IMM>
__device__ __forceinline__ void dma16(unsigned m0v, unsigned voff, u32x4 srd, unsigned soff) {
  asm volatile("s_mov_b32 m0, %0\n\ts_nop 0\n\tbuffer_load_dwordx4 %1, %2, %3 offen offset:%4 lds"
               ::"s"(m0v), "v"(voff), "s"(srd), "s"(soff), "i"(IMM) : "memory");
}
template <int IMM>
__device__ __forceinline__ void dma16n(unsigned voff, u32x4 srd, unsigned soff) {
  asm volatile("buffer_load_dwordx4 %0, %1, %2 offen offset:%3 lds" ::"v"(voff), "s"(srd), "s"(soff), "i"(IMM) : "memory");
}
__device__ __forceinline__ void set_m0(unsigned v) { asm volatile("s_mov_b32 m0, %0" ::"s"(v) : "memory"); }
__device__ __forceinline__ unsigned opq(unsigned v) { asm volatile("" : "+v"(v)); return v; }
__device__ __forceinline__ unsigned opqs(unsigned v) { asm volatile("" : "+s"(v)); return v; }
__device__ __forceinline__ void gload16_sc1(u32x4& dst, unsigned voff, u32x4 srd, unsigned soff) {
  asm volatile("buffer_load_dwordx4 %0, %1, %2, %3 offen sc1" : "=v"(dst) : "v"(voff), "s"(srd), "s"(soff) : "memory");
}
__device__ __forceinline__ void gstore16_sc1(u32x4 v, unsigned voff, u32x4 srd, unsigned soff) {
  asm volatile("buffer_store_dwordx4 %0, %1, %2, %3 offen sc1\n\ts_nop 1" ::"v"(v), "v"(voff), "s"(srd), "s"(soff) : "memory");
}
__device__ __forceinline__ void gload8_sc1(u32x2& dst, unsigned voff, u32x4 srd, unsigned soff) {
  asm volatile("buffer_load_dwordx2 %0, %1, %2, %3 offen sc1" : "=v"(dst) : "v"(voff), "s"(srd), "s"(soff) : "memory");
}
__device__ __forceinline__ void gstore8_sc1(u32x2 v, unsigned voff, u32x4 srd, unsigned soff) {
  asm volatile("buffer_store_dwordx2 %0, %1, %2, %3 offen sc1" ::"v"(v), "v"(voff), "s"(srd), "s"(soff) : "memory");
}

__device__ __forceinline__ s16x4 tr_read(unsigned addr) {
  return __builtin_amdgcn_ds_read_tr16_b64_v4i16((LDS_AS s16x4*)(uintptr_t)addr);
}
struct Frag { s16x4 lo, hi; };
__device__ __forceinline__ bf16x8 join(const Frag& f) {
  s16x8 v = __builtin_shufflevector(f.lo, f.hi, 0, 1, 2, 3, 4, 5, 6, 7);
  return __builtin_bit_cast(bf16x8, v);
}
__device__ __forceinline__ bf16x8 lds_read16(unsigned addr) { return *(LDS_AS bf16x8*)(uintptr_t)addr; }
__device__ __forceinline__ unsigned pack_bf16(float a, float b) {
  f32x2 v = {a, b};
  bf16x2 r = __builtin_convertvector(v, bf16x2);
  return __builtin_bit_cast(unsigned, r);
}
template <class TB>
__device__ __forceinline__ void mfma_acc(f32x4& c, bf16x8 a, TB b) {
  asm volatile("v_mfma_f32_16x16x32_bf16 %0, %1, %2, %0" : "+v"(c) : "v"(a), "v"(b));
}
template <class TB>
__device__ __forceinline__ void mfma_new(f32x4& c, bf16x8 a, TB b) {
  asm volatile("v_mfma_f32_16x16x32_bf16 %0, %1, %2, 0" : "=&v"(c) : "v"(a), "v"(b));
}

template <int I> struct IC { static constexpr int v = I; };
template <int I, int N, class F> __device__ __forceinline__ void sfor(F&& f) {
  if constexpr (I < N) { f(IC<I>{}); sfor<I + 1, N>(f); }
}
template <int N> __device__ __forceinline__ void wait_vm() { asm volatile("s_waitcnt vmcnt(%0)" ::"i"(N) : "memory"); }
__device__ __forceinline__ void wait_lgkm0() { asm volatile("s_waitcnt lgkmcnt(0)" ::: "memory"); }
#define BAR() asm volatile("s_barrier" ::: "memory")
#define SB() __builtin_amdgcn_sched_barrier(0)
#define INL __attribute__((always_inline))

constexpr int UNIT = 16384, LDS_TOTAL = 10 * UNIT;
constexpr int uaddr(int n) { return (n % 10) * UNIT; }


enum { K_DMA = 1, K_STORE, K_FLAG, K_POLL, K_H };
struct Op { int pos, kind, n, i; };
struct Sched { int cnt; Op op[200]; };
constexpr void put(Sched& s, int pos, int kind, int n, int i) { s.op[s.cnt++] = Op{pos, kind, n, i}; }
constexpr void spread(Sched& s, int p0, int p1, int u0, int nu) {
  for (int k = 0; k < 2 * nu; ++k) put(s, p0 + (k * (p1 - p0)) / (2 * nu), K_DMA, u0 + (k >> 1), k & 1);
}
constexpr int Q(int step, int slot) { return 200 + 8 * step + slot; }
constexpr void pair(Sched& s, int step, int slot, int kind, int n) { put(s, Q(step, slot), kind, n, 0); put(s, Q(step, slot == 0 ? 0 : slot + 1), kind, n, 1); }
constexpr int HB = 3;
constexpr int Y1 = 2, Y2 = 5, FCHK = 7, Y3 = 10;
constexpr Sched make_sched() {
  Sched s{};
  for (int n = 0; n < 4; ++n) { put(s, -1, K_DMA, n, 0); put(s, -1, K_DMA, n, 1); }
  put(s, -1, K_POLL, 0, 0);
  spread(s, 0, 2, 4, 2);
  for (int t = 0; t < 7; ++t) spread(s, 8 * t + 2, 8 * t + 9, 2 * t + 6, 2);
  spread(s, 58, 64, 20, 2);
  put(s, 65, K_STORE, 0, 0);
  put(s, 66, K_STORE, 0, 1);
  spread(s, 72, 80, 22, 1);
  spread(s, 82, 89, 23, 2);
  put(s, 90, K_FLAG, 0, 0);
  spread(s, 91, 103, 25, 2);
  spread(s, 103, 111, 27, 2);
  put(s, 111, K_POLL, 1, 0);
  spread(s, 112, 121, 29, 2);
  for (int k = 0; k < 4; ++k) put(s, 122 + k, K_H, k >> 1, k & 1);
  put(s, 128, K_STORE, 1, 0);
  put(s, 128, K_STORE, 1, 1);
  put(s, 128, K_H, 2, 0);
  put(s, 128, K_H, 2, 1);
  put(s, Q(Y1, 0), K_FLAG, 1, 0);
  pair(s, 2, 0, K_H, 3); pair(s, 2, 0, K_H, 4);
  pair(s, 2, 3, K_DMA, 31);
  pair(s, 3, 1, K_H, 5); pair(s, 3, 3, K_H, 6);
  pair(s, 4, 1, K_DMA, 32); pair(s, 4, 3, K_DMA, 33);
  pair(s, 5, 1, K_DMA, 34); pair(s, 5, 3, K_DMA, 35);
  put(s, Q(6, 0), K_POLL, 2, 0);
  pair(s, 6, 1, K_DMA, 36); pair(s, 6, 3, K_DMA, 37);
  for (int k = 0; k < HB; ++k) pair(s, 7, 0, K_H, 7 + k);
  for (int k = 7 + HB; k < 14; ++k) pair(s, k - HB, 1, K_H, k);
  return s;
}
constexpr Sched SCH = make_sched();
constexpr int younger_than(int kind, int n, int i, int P) {
  int c = -1;
  for (int k = 0; k < SCH.cnt; ++k) {
    if (SCH.op[k].pos >= P) break;
    if (SCH.op[k].kind == kind && SCH.op[k].n == n && SCH.op[k].i == i) c = 0;
    else if (c >= 0 && SCH.op[k].kind != K_FLAG && !(SCH.op[k].kind == K_POLL && SCH.op[k].n > 0)) ++c;
  }
  return c;
}
constexpr int first_op(int P) { int k = 0; while (k < SCH.cnt && SCH.op[k].pos < P) ++k; return k; }
constexpr bool is_bar(int P) { return P < 64 ? (P & 7) == 2 : (P & 15) == 10; }
constexpr int bar_wait(int P) {
  if (P < 64) return younger_than(K_DMA, 2 * (P >> 3) + 3, 1, P);
  if (P == 74) return younger_than(K_DMA, 19, 1, P);
  if (P == 90) return younger_than(K_STORE, 0, 1, P);
  if (P == 106) return younger_than(K_DMA, 23, 1, P);
  if (P == 122) return younger_than(K_POLL, 1, 0, P);
  if (P == Q(Y1, 0)) return 0;
  if (P == Q(Y2, 0)) return younger_than(K_DMA, 31, 1, P);
  if (P == Q(FCHK, 0)) return younger_than(K_POLL, 2, 0, P);
  return younger_than(K_DMA, 37, 1, P);
}
constexpr int last_use(int n) { return n < 16 ? 8 * (n >> 1) + 7 : (n < 24 ? 8 * (n - 8) + 7 : Q(n - 24, 4)); }
constexpr int first_read(int n) { return n < 16 ? 8 * (n >> 1) - 6 : (n < 24 ? 8 * (n - 8) - 6 : (n == 24 ? 124 : Q(n - 25, 0))); }
constexpr bool any_bar(int P) { return P < 128 ? is_bar(P) : (P == Q(Y1, 0) || P == Q(Y2, 0) || P == Q(FCHK, 0) || P == Q(Y3, 0)); }
constexpr bool ring_ok() {
  for (int k = 0; k < SCH.cnt; ++k) {
    if (SCH.op[k].kind != K_DMA || SCH.op[k].n < 10) continue;
    bool ok = false;
    for (int P = last_use(SCH.op[k].n - 10) + 1; P <= SCH.op[k].pos; ++P) ok = ok || any_bar(P);
    if (!ok) return false;
  }
  for (int n = 2; n < 38; ++n) {
    bool ok = false;
    for (int P = 0; P <= first_read(n); ++P) ok = ok || (any_bar(P) && younger_than(K_DMA, n, 1, P) >= bar_wait(P));
    if (!ok) return false;
  }
  return true;
}
static_assert(ring_ok(), "LDS ring schedule");
constexpr bool sched_sorted() { for (int k = 1; k < SCH.cnt; ++k) if (SCH.op[k].pos < SCH.op[k - 1].pos) return false; return true; }
static_assert(sched_sorted(), "schedule order");

__global__ __launch_bounds__(512, 2) void k_fused(const void* __restrict__ x, const void* __restrict__ Wg,
                                                  const void* __restrict__ Wu, const void* __restrict__ Wd,
                                                  void* __restrict__ out, void* __restrict__ ws) {
  extern __shared__ __attribute__((aligned(16))) char smem[];
  const unsigned lds0 = (unsigned)(uintptr_t)(LDS_AS char*)smem;
  const unsigned bid = blockIdx.x;
  const unsigned w = rfl(threadIdx.x >> 6);
  const bool busy = bid < 224;
  uint64_t pc;
  asm volatile("s_getpc_b64 %0" : "=s"(pc));
  u32x2 px, pw;
  asm volatile("s_load_dwordx2 %0, %2, 0x0\n\ts_load_dwordx2 %1, %2, %3"
               : "=&s"(px), "=&s"(pw) : "s"(__builtin_amdgcn_kernarg_segment_ptr()), "s"(8 + 8 * (w >> 2)));
  SB();
  const unsigned mi = (bid & 6) * 2 + ((bid >> 3) & 3), half = bid & 1, idx = bid >> 5;
  const unsigned rot = ((bid & 6) + 4 * half + ((bid >> 3) & 1) + (idx & 1)) & 7;
  unsigned vw0, vw1, vx0, vx1;
  {
    const unsigned lane = threadIdx.x & 63, g = lane >> 4;
    vw0 = (lane >> 3) * 3584 + (((lane & 7) ^ ((g & 1) << 1)) << 4);
    vw1 = (vw0 ^ 64) + 8 * 3584 - 1024;
    vx0 = (lane >> 3) * 1024 + (((lane & 7) ^ g) << 4);
    vx1 = (vx0 ^ 64) + 8192 - 1024;
  }
  unsigned sx0 = (mi * 128 + 16 * w) * 1024;
  unsigned sw0 = (16 * (w & 3)) * 3584 + (half * 14 + idx) * 128;
  unsigned mu0 = lds0 + w * 2048;
  unsigned sw00 = sw0 + rot * (64 * 3584), sx00 = sx0 + rot * 128;
  asm volatile("" : "+v"(vw0), "+v"(vw1), "+v"(vx0), "+v"(vx1));
  asm volatile("" : "+s"(sx0), "+s"(sw0), "+s"(mu0), "+s"(sw00), "+s"(sx00));
  SB();
  asm volatile("s_waitcnt lgkmcnt(0)" : "+s"(px), "+s"(pw));
  const u32x4 srdX = {px[0], px[1] & 0xffffu, M_ * D_ * 2, 0x00020000u};
  const u32x4 srdW = {pw[0], pw[1] & 0xffffu, D_ * F_ * 2, 0x00020000u};
  auto issue_xw = [&](int n, int i, bool pre = false) {
    const int s = n < 16 ? n >> 1 : n - 16;
    const unsigned sg = (s + rot) & 7;
    if (n < 16 && (n & 1) == 0) {
      const unsigned so = n == 0 ? sx00 : sx0 + sg * 128;
      if (i == 0) { if (pre) dma16n<0>(vx0, srdX, so); else dma16<0>(mu0 + uaddr(n), vx0, srdX, so); } else dma16n<1024>(vx1, srdX, so);
    } else {
      const unsigned so = n == 1 ? sw00 : sw0 + sg * (64 * 3584) + (n >= 16 ? 896 : 0);
      if (i == 0) { if (pre) dma16n<0>(vw0, srdW, so); else dma16<0>(mu0 + uaddr(n), vw0, srdW, so); } else dma16n<1024>(vw1, srdW, so);
    }
  };
  if (busy) {
    issue_xw(0, 0); issue_xw(0, 1); issue_xw(1, 0); issue_xw(1, 1);
    BAR();
    issue_xw(2, 0); issue_xw(2, 1); issue_xw(3, 0); issue_xw(3, 1);
  } else {
    const unsigned o = min(threadIdx.x * 64u, CODE_BYTES);
    asm volatile("global_load_dword v249, %0, %1" ::"v"(o), "s"(pc) : "v249", "memory");
  }
  SB();
  const unsigned tid = opq(threadIdx.x), lane = tid & 63;
  const unsigned bid2 = opqs(bid), xcd = bid2 & 7, ml = (bid2 >> 3) & 3;
  unsigned rank = half * 8 + idx;
  const unsigned m64 = 2 * mi + half, n64 = (((idx & 3) == 3 ? idx ^ 4 : idx) - (rfl((unsigned)(uintptr_t)Wd) >> 7)) & 7;
  const unsigned g = lane >> 4, q = (lane & 15) >> 2, p = lane & 3;
  const u32x4 srdH = make_srd(ws, SYNC_OFF + 16 * 8192);
  const u32x4 srdD = make_srd(Wd, F_ * D_ * 2);
  unsigned syncb = SYNC_OFF + mi * 8192;
  const unsigned lane16 = lane * 16;

  const unsigned kq = w >> 1, mb = w & 1;
  unsigned rot2 = 2 * ml + idx; rot2 = rot2 >= 7 ? rot2 - 7 : rot2;
  unsigned kb[7];
  auto set_kb = [&]() {
    unsigned e = opqs(rot2);
#pragma unroll
    for (int j = 0; j < 7; ++j) {
      kb[j] = opqs((e + 7 * kq + (kq >> 1) * 14) * 1024);
      e = e == 6 ? 0 : e + 1;
    }
  };
  auto kap = [&](int s) { return kb[s % 7] + (s >= 7 ? 14 * 1024 : 0); };
  const unsigned vd0 = (lane >> 3) * 1024 + (((lane & 7) ^ (g << 1)) << 4);
  const unsigned vd1 = vd0 + 8192 - 1024;
  unsigned sd0 = (16 * mb) * 1024 + n64 * 128;
  auto issue_wd = [&](int s, int i, bool pre = false) {
    if (i == 0) { if (pre) dma16n<0>(vd0, srdD, sd0 + kap(s) * 32); else dma16<0>(mu0 + uaddr(24 + s), vd0, srdD, sd0 + kap(s) * 32); }
    else dma16n<1024>(vd1, srdD, sd0 + kap(s) * 32);
  };
  const unsigned dl = lds0 + kq * 4096 + (4 * g + q) * 128 + (((2 * g + (q >> 1)) & 3) << 5) + p * 8;
  unsigned db[3][4];
  auto set_db = [&](int k) {
#pragma unroll
    for (int c = 0; c < 4; ++c) db[k][c] = opq((dl ^ (c << 5)) + k * 4 * UNIT);
  };
  auto ldd = [&](int s, int a, int u) {
    const int ua = uaddr(24 + s);
    return tr_read(db[ua >> 16][a] + (ua & 65535) + u * 2048);
  };
  u32x2 ep, tag, fl;
  auto mktag = [&]() {
    const uint64_t t = (((uint64_t)ep[1] << 32) | ep[0]) * 0x9E3779B97F4A7C15ull + 1ull;
    tag = u32x2{(unsigned)t, (unsigned)(t >> 32)};
  };
  const unsigned one = tid == 0 ? 0u : 0x40000000u;
  const unsigned fo = tid < 16 ? 128 * (1 + tid) : 0x40000000u;
  unsigned fmask = 0x7F7Fu;
  auto flags_ok = [&]() { return (rfl((unsigned)__builtin_amdgcn_ballot_w64(fl[0] == tag[0] && fl[1] == tag[1])) & fmask) == fmask; };
  auto poll = [&](unsigned off) {
    unsigned cnt = SPIN_CAP, t;
#define PLD(r) "buffer_load_dwordx2 " r ", %2, %3, %4 offen sc1\n\t"
#define PCK(r) "s_waitcnt vmcnt(2)\n\tv_cmp_eq_u64 vcc, " r ", %5\n\ts_nop 1\n\ts_and_b32 %1, vcc_lo, %6\n\ts_cmp_eq_u32 %1, %6\n\ts_cbranch_scc1 2f\n\t"
    asm volatile(PLD("v[250:251]") "s_sleep 3\n\t" PLD("v[252:253]") "s_sleep 3\n\t" PLD("v[254:255]")
                 "1:\n\t"
                 PCK("v[250:251]") PLD("v[250:251]") PCK("v[252:253]") PLD("v[252:253]") PCK("v[254:255]") PLD("v[254:255]")
                 "s_sub_u32 %0, %0, 1\n\ts_cbranch_scc0 1b\n\t"
                 "2:"
                 : "+s"(cnt), "=&s"(t) : "v"(fo), "s"(srdH), "s"(syncb + off), "v"(tag), "s"(fmask)
                 : "v250", "v251", "v252", "v253", "v254", "v255", "vcc", "scc", "memory");
#undef PLD
#undef PCK
  };
  auto poll_slow = [&](unsigned off) {
#pragma nounroll
    for (unsigned spin = 0; spin < SPIN_CAP; ++spin) {
      gload8_sc1(fl, fo, srdH, syncb + off);
      asm volatile("s_waitcnt vmcnt(0)" : "+v"(fl)::"memory");
      if (flags_ok()) break;
      __builtin_amdgcn_s_sleep(8);
    }
  };
  Frag df[2][4];
  u32x4 hf[14][2];
  unsigned sh2 = ((m64 * 4 + mb * 2) * 56) * 1024;
  const unsigned lane16b = lane16 + 56 * 1024;
  auto issue_h = [&](int s, int b) { gload16_sc1(hf[s][b], b ? lane16b : lane16, srdH, sh2 + kap(s)); };

  set_kb();
  if (__builtin_expect(busy, 1)) {
    gload8_sc1(ep, 0u, srdH, syncb);
    const unsigned wm = w & 3, wn = w >> 2;
    const unsigned xrow = 32 * wm + (lane & 15);
    const unsigned xa0 = lds0 + xrow * 128 + ((g ^ ((xrow >> 1) & 7)) << 4);
    const unsigned wa0 = lds0 + (8 * g + q) * 128 + (((2 * wn) ^ ((q >> 1) | ((g & 1) << 1))) << 5) + p * 8;
    unsigned xa[3][2], wa[3][2];
#pragma unroll
    for (int k = 0; k < 3; ++k)
#pragma unroll
      for (int c = 0; c < 2; ++c) { xa[k][c] = opq((xa0 ^ (c << 6)) + k * 4 * UNIT); wa[k][c] = opq((wa0 ^ (c << 5)) + k * 4 * UNIT); }

    f32x4 acc[2][2][4];
    bf16x8 xf[8][2][2];
    Frag wf[4][2];
    auto ldx = [&](int s, int b, int kk) { const int ua = uaddr(2 * s); xf[s][b][kk] = lds_read16(xa[ua >> 16][kk] + (ua & 65535) + b * 2048); };
    auto ldw = [&](int G, int kk, int u = 2) {
      const int st = G >> 2, a = ((G & 1) << 1) | ((G >> 1) & 1), ua = uaddr(st < 8 ? 2 * st + 1 : 8 + st);
      const unsigned ad = wa[ua >> 16][a & 1] + (ua & 65535) + (a >> 1) * 8192 + kk * 4096;
      if (u != 1) wf[G & 3][kk].lo = tr_read(ad);
      if (u != 0) wf[G & 3][kk].hi = tr_read(ad + 512);
    };
    const unsigned sh = ((mi * 8 + wm * 2) * 56 + half * 28 + idx * 2 + wn) * 1024;
    u32x4 ho[2];
    auto epi = [&](int ps, int b, int c) {
      const int t = c >> 1, i = c & 1;
      float g0 = __builtin_elementwise_maximum(acc[ps][b][t][2 * i], 0.f), g1 = __builtin_elementwise_maximum(acc[ps][b][t][2 * i + 1], 0.f);
      float p0 = g0 * acc[ps][b][2 + t][2 * i];
      asm volatile("" : "+v"(p0));
      ho[b][c] = pack_bf16(p0, g1 * acc[ps][b][2 + t][2 * i + 1]);
    };
    auto sth = [&](int ps, int b) { gstore16_sc1(ho[b], lane16, srdH, sh + b * (56 * 1024) + ps * (14 * 1024)); };

    auto vmem = [&](auto PP) INL {
      sfor<first_op(PP.v), first_op(PP.v + 1)>([&](auto K) INL {
        constexpr Op o = SCH.op[K.v];
        if constexpr (o.pos == PP.v) {
          constexpr bool pre = PP.v < 128 && K.v == first_op(PP.v);
          if constexpr (o.kind == K_DMA) { if constexpr (o.n < 24) issue_xw(o.n, o.i, pre); else issue_wd(o.n - 24, o.i, pre); }
          if constexpr (o.kind == K_STORE) sth(o.n, o.i);
          if constexpr (o.kind == K_FLAG) gstore8_sc1(tag, one, srdH, syncb + 128 * (1 + rank));
          if constexpr (o.kind == K_POLL) gload8_sc1(fl, fo, srdH, syncb);
          if constexpr (o.kind == K_H) issue_h(o.n, o.i);
        }
      });
    };
    wait_vm<5>();
    BAR();
    ldw(0, 0); ldx(0, 0, 0); ldw(0, 1); ldx(0, 0, 1); ldx(0, 1, 0); ldx(0, 1, 1); ldw(1, 0); ldw(1, 1); ldw(2, 0); ldw(2, 1);
    SB();
    sfor<0, 128>([&](auto Z) INL {
      constexpr int P = Z.v, ps = P >> 6, s = (P >> 3) & 7, G = P >> 1, a = ((G & 1) << 1) | ((G >> 1) & 1), b = P & 1;
      if constexpr (is_bar(P)) {
        constexpr int n = bar_wait(P);
        static_assert(n >= 0 && n < 24, "wait count");
        if constexpr (P == 10) asm volatile("s_waitcnt vmcnt(%1)" : "+v"(ep) : "i"(n) : "memory");
        else if constexpr (P == 122) {
          asm volatile("s_waitcnt vmcnt(%1)" : "+v"(fl) : "i"(n) : "memory");
          if (w == 0) { if (__builtin_expect(!flags_ok(), 0)) poll(0); }
        } else wait_vm<n>();
        BAR();
      }
      if constexpr (P < 58 ? (P & 1) == 0 : (P & 3) == 2) __builtin_amdgcn_s_setprio(P < 58 ? 3 - (((P + 6) & 7) >> 1) : 3 - (((P + 6) & 15) >> 2));
      if constexpr (s == 0) mfma_new(acc[ps][b][a], join(wf[G & 3][0]), xf[s][b][0]); else mfma_acc(acc[ps][b][a], join(wf[G & 3][0]), xf[s][b][0]);
      SB();
      if constexpr (G + 3 < 64) ldw(G + 3, b, 0);
      if constexpr (P >= 124) df[0][P - 124].lo = ldd(0, P - 124, 0);
      {
        constexpr int k0 = first_op(P);
        if constexpr (k0 < first_op(P + 1) && SCH.op[k0].kind == K_DMA && SCH.op[k0].i == 0) set_m0(mu0 + uaddr(SCH.op[k0].n));
        else if constexpr (P < 56 && (P & 7) >= 2 && (P & 7) < 6) ldx(s + 1, ((P - 2) >> 1) & 1, P & 1);
      }
      SB();
      mfma_acc(acc[ps][b][a], join(wf[G & 3][1]), xf[s][b][1]);
      SB();
      if constexpr (G + 3 < 64) ldw(G + 3, b, 1);
      if constexpr (P >= 124) df[0][P - 124].hi = ldd(0, P - 124, 1);
      {
        constexpr int k0 = first_op(P);
        if constexpr (k0 < first_op(P + 1) && SCH.op[k0].kind == K_DMA && SCH.op[k0].i == 0 && P < 56 && (P & 7) >= 2 && (P & 7) < 6) ldx(s + 1, ((P - 2) >> 1) & 1, P & 1);
      }
      if constexpr (P == 120) set_db(1);
      if constexpr ((P & 63) == 62 || (P & 63) == 63) { epi(ps, P & 1, 0); epi(ps, P & 1, 1); }
      if constexpr (P == 65 || P == 66) { epi(0, P - 65, 2); epi(0, P - 65, 3); }
      if constexpr (P == 116) set_db(0);
      if constexpr (P == 118) set_db(2);
      if constexpr (P == 84) mktag();
      vmem(Z);
      SB();
    });
    epi(1, 0, 2); epi(1, 0, 3);
    sth(1, 0);
    SB();
    epi(1, 1, 2); epi(1, 1, 3);
    sth(1, 1);
    issue_h(2, 0); issue_h(2, 1);
    __builtin_amdgcn_s_setprio(0);
  } else {
    gload8_sc1(ep, 0u, srdH, syncb);
    set_db(0); set_db(1); set_db(2);
    {
      unsigned t = 0;
      const unsigned rotx = ((xcd & 6) + 4 * half) & 7;
      auto touch = [&](unsigned vo, u32x4 srd, unsigned so) { asm volatile("buffer_load_dword %0, %1, %2, %3 offen" : "+v"(t) : "v"(vo), "s"(srd), "s"(so) : "memory"); };
      {
        const unsigned st = (rotx + (ml & 1) + 2 + (lane & 3)) & 7;
        touch((lane & 3) < 2 ? (mi * 128 + w * 16 + (lane >> 2)) * 1024 + st * 128 : 0x40000000u, srdX, 0);
      }
      touch((lane & 7) < 7 ? (lane >> 3) * 3584 + (lane & 7) * 128 : 0x40000000u, srdW, (rotx * 64 + (ml * 4 + (w & 3)) * 8) * 3584 + (half * 14 + 7) * 128);
#pragma unroll
      for (int i = 0; i < 2; ++i) {
        unsigned c = i + xcd; c = c >= 7 ? c - 7 : c; c = c >= 7 ? c - 7 : c;
        touch(lane * 128, srdD, ((c * 4 + ml) * 8 + w) * 8192);
      }
      asm volatile("s_waitcnt vmcnt(0)" : "+v"(t), "+v"(ep)::"memory");
      mktag();
    }
#pragma unroll
    for (int s = 0; s < 7; ++s) { issue_wd(s, 0); issue_wd(s, 1); }
    if (w == 0) poll_slow(0); else wait_vm<0>();
    BAR();
#pragma unroll
    for (int s = 0; s < 3; ++s) { issue_h(s, 0); issue_h(s, 1); }
#pragma unroll
    for (int a = 0; a < 4; ++a) { df[0][a].lo = ldd(0, a, 0); df[0][a].hi = ldd(0, a, 1); }
    wait_vm<0>();
  }
  asm volatile("" : "+s"(mu0), "+s"(sd0), "+s"(sh2), "+s"(syncb), "+s"(rank));
  asm volatile("" : "+s"(kb[0]), "+s"(kb[1]), "+s"(kb[2]), "+s"(kb[3]), "+s"(kb[4]), "+s"(kb[5]), "+s"(kb[6]));
  fmask = rank == 7 ? 0xFFFFu : 0x7F7Fu;
  f32x4 acc2[2][4];
  const unsigned pa = opq(lds0 + ((kq + 8) % 10) * UNIT + (mb * 32 + (lane & 15)) * 256 + ((g ^ (lane & 7)) << 4));
  const unsigned pa1 = opq(pa ^ 64);
  const unsigned ra = opq(lds0 + (w * 8 + (lane >> 3)) * 256 + (((2 * (lane & 7)) ^ (lane >> 3)) << 4)), rb = opq(ra ^ 16);
  const unsigned ra8 = opq(ra + 8 * UNIT), rb8 = opq(rb + 8 * UNIT);
  const unsigned vo = opq((w * 8 + (lane >> 3)) * 1024 + (lane & 7) * 16), so = opqs(m64 * 65536 + n64 * 128);
  const u32x4 srdO = make_srd(out, M_ * D_ * 2);
  auto wpart = [&](int a) {
    const unsigned ad = ((a & 1) ? pa1 : pa) + (a >> 1) * 128;
    *(LDS_AS f32x4*)(uintptr_t)(ad) = acc2[0][a];
    *(LDS_AS f32x4*)(uintptr_t)(ad + 16 * 256) = acc2[1][a];
  };
  auto vmem2 = [&](auto PP) INL {
    sfor<first_op(PP.v), first_op(PP.v + 1)>([&](auto K) INL {
      constexpr Op o = SCH.op[K.v];
      if constexpr (o.pos == PP.v) {
        if constexpr (o.kind == K_DMA) issue_wd(o.n - 24, o.i);
        if constexpr (o.kind == K_FLAG) gstore8_sc1(tag, one, srdH, syncb + 128 * (17 + rank));
        if constexpr (o.kind == K_POLL) gload8_sc1(fl, fo, srdH, syncb + 2048);
        if constexpr (o.kind == K_H) issue_h(o.n, o.i);
      }
    });
  };
  sfor<0, 14>([&](auto S) INL {
    constexpr int s = S.v;
    if constexpr (s == Y1 || s == FCHK || s == Y2 || s == Y3) {
      constexpr int n = bar_wait(Q(s, 0));
      static_assert(n >= 0 && n < 24, "wait count");
      if constexpr (s == FCHK) {
        asm volatile("s_waitcnt vmcnt(%1)" : "+v"(fl) : "i"(n) : "memory");
        if (w == 0) { if (__builtin_expect(!flags_ok(), 0)) poll(2048); }
      } else wait_vm<n>();
      BAR();
      if constexpr (s == Y3) { if (rank == 7 && w == 0) gstore8_sc1(tag, one, srdH, syncb); }
    }
    vmem2(IC<Q(s, 0)>{});
    if constexpr (s == Y1) asm volatile("" : "+v"(hf[s][0]), "+v"(hf[s][1])::"memory");
    else asm volatile("s_waitcnt vmcnt(%2)" : "+v"(hf[s][0]), "+v"(hf[s][1]) : "i"(younger_than(K_H, s, 1, Q(s, 1))) : "memory");
    SB();
    sfor<0, 4>([&](auto A) INL {
      constexpr int a = A.v;
      if constexpr (s == 0) mfma_new(acc2[0][a], join(df[0][a]), hf[0][0]); else mfma_acc(acc2[0][a], join(df[s & 1][a]), hf[s][0]);
      SB();
      if constexpr (s + 1 < 14) df[(s + 1) & 1][a].lo = ldd(s + 1, a, 0);
      SB();
      if constexpr (s == 0) mfma_new(acc2[1][a], join(df[0][a]), hf[0][1]); else mfma_acc(acc2[1][a], join(df[s & 1][a]), hf[s][1]);
      SB();
      if constexpr (s + 1 < 14) df[(s + 1) & 1][a].hi = ldd(s + 1, a, 1);
      vmem2(IC<Q(s, a + 1)>{});
      if constexpr (s == 13 && a == 3) { asm volatile("s_nop 1" ::: "memory"); SB(); wpart(0); SB(); wpart(1); }
      SB();
    });
  });
  wpart(2);
  SB();
  asm volatile("s_nop 1" ::: "memory");
  SB();
  wpart(3);
  wait_lgkm0();
  BAR();
  {
    f32x4 p0[4], p1[4];
#pragma unroll
    for (int v = 0; v < 4; ++v) {
      p0[v] = *(LDS_AS f32x4*)(uintptr_t)((v < 2 ? ra8 : ra) + (v & 1) * UNIT);
      p1[v] = *(LDS_AS f32x4*)(uintptr_t)((v < 2 ? rb8 : rb) + (v & 1) * UNIT);
    }
    SB();
    const f32x4 s0 = (p0[0] + p0[1]) + (p0[2] + p0[3]), s1 = (p1[0] + p1[1]) + (p1[2] + p1[3]);
    u32x4 o = {pack_bf16(s0[0], s0[1]), pack_bf16(s0[2], s0[3]), pack_bf16(s1[0], s1[1]), pack_bf16(s1[2], s1[3])};
    asm volatile("buffer_store_dwordx4 %0, %1, %2, %3 offen" ::"v"(o), "v"(vo), "s"(srdO), "s"(so) : "memory");
  }
}

extern "C" void kernel_launch(void* const* d_in, const int* in_sizes, int n_in, void* d_out, int out_size,
                              void* d_ws, size_t ws_size, hipStream_t stream) {
  (void)in_sizes; (void)n_in; (void)out_size; (void)ws_size;
  (void)hipFuncSetAttribute((const void*)k_fused, hipFuncAttributeMaxDynamicSharedMemorySize, LDS_TOTAL);
  k_fused<<<256, 512, LDS_TOTAL, stream>>>(d_in[0], d_in[1], d_in[2], d_in[3], d_out, d_ws);
}
```
